```python
import torch

def _softmax(x, axis=-1):
    return torch.softmax(x, dim=axis)
def _gelu(x):
    return torch.nn.functional.gelu(x, approximate="tanh")
def _silu(x):
    return torch.nn.functional.silu(x)
def _sigmoid(x):
    return torch.sigmoid(x)
def _layernorm(x, axis=-1, eps=1e-5):
    m = x.mean(dim=axis, keepdim=True)
    v = x.var(dim=axis, keepdim=True, unbiased=False)
    return (x - m) / torch.sqrt(v + eps)
def _scatter_add(tgt, idx, src, axis=0):
    return tgt.clone().index_add_(axis, idx, src)
def _segment_sum(src, seg, n):
    out = torch.zeros((n,) + src.shape[1:], dtype=src.dtype)
    return out.index_add_(0, seg, src)
def _pad(x, pw):
    flat = tuple(v for pair in reversed(pw) for v in pair)
    return torch.nn.functional.pad(x, flat)

kernel_name = 'concat_heads__sliding_block_cached__gqa_block_cached__decode'

def setup_inputs():

    _w = (lambda a: (lambda t: t.to(torch.bfloat16) if t.is_floating_point() else t)(torch.as_tensor(a)))
    def _A_setup():
        B, Sq, D = 1, 1, 576
        Hq, Hkv, Sk, Dh = 32, 16, 8192, 128
        assert Hq % Hkv == 0, (Hq, Hkv)
        return {
            "x": _w((lambda *s: torch.randn(*s, dtype=torch.bfloat16) * 0.8)(B, Sq, D)),
            "k_cache": _w((lambda *s: torch.randn(*s, dtype=torch.bfloat16) * 1.6)(B, Hkv, Sk, Dh)),
            "v_cache": _w((lambda *s: torch.randn(*s, dtype=torch.bfloat16) * 0.2)(B, Hkv, Sk, Dh)),
            "wq": _w((lambda *s: torch.randn(*s, dtype=torch.bfloat16) * 1.6)(D, Hq * Dh) * (D ** -0.5)),
            "wo": _w((lambda *s: torch.randn(*s, dtype=torch.bfloat16) * 0.2)(Hq * Dh, D) * ((Hq * Dh) ** -0.5)),
        }
    _A_in = _A_setup()

    def _B_setup():
        B, Sq, D = 1, 1, 576
        Hq, Hkv, Sk, Dh = 32, 16, 8192, 128
        assert Hq % Hkv == 0, (Hq, Hkv)
        return {
            "x": _w((lambda *s: torch.randn(*s, dtype=torch.bfloat16) * 0.8)(B, Sq, D)),
            "k_cache": _w((lambda *s: torch.randn(*s, dtype=torch.bfloat16) * 1.6)(B, Hkv, Sk, Dh)),
            "v_cache": _w((lambda *s: torch.randn(*s, dtype=torch.bfloat16) * 0.2)(B, Hkv, Sk, Dh)),
            "wq": _w((lambda *s: torch.randn(*s, dtype=torch.bfloat16) * 1.6)(D, Hq * Dh) * (D ** -0.5)),
            "wo": _w((lambda *s: torch.randn(*s, dtype=torch.bfloat16) * 0.2)(Hq * Dh, D) * ((Hq * Dh) ** -0.5)),
        }
    _B_in = _B_setup()

    return {
        "A_x": _A_in["x"],
        "A_k_cache": _A_in["k_cache"],
        "A_v_cache": _A_in["v_cache"],
        "A_wq": _A_in["wq"],
        "A_wo": _A_in["wo"],
        "B_x": _B_in["x"],
        "B_k_cache": _B_in["k_cache"],
        "B_v_cache": _B_in["v_cache"],
        "B_wq": _B_in["wq"],
        "B_wo": _B_in["wo"],
    }

def reference(A_x, A_k_cache, A_v_cache, A_wq, A_wo, B_x, B_k_cache, B_v_cache, B_wq, B_wo):
    def _A_ref(x, k_cache, v_cache, wq, wo):
        B, Sq, D = x.shape
        Hq, Dh = 32, 128
        Hkv, Sk = k_cache.shape[1], k_cache.shape[2]
        W = min(4096, max(256, Sk // 4))
        rep = Hq // Hkv
        q = (lambda x, *d: x.permute(*d))((x @ wq).reshape(B, Sq, Hq, Dh), 0, 2, 1, 3)
        k = (lambda x, n, axis: torch.repeat_interleave(x, n, dim=axis))(k_cache[:, :, Sk - W:, :], rep, axis=1)
        v = (lambda x, n, axis: torch.repeat_interleave(x, n, dim=axis))(v_cache[:, :, Sk - W:, :], rep, axis=1)
        scores = torch.einsum("bhqd,bhkd->bhqk", q, k) * (Dh ** -0.5)
        p = _softmax(scores, axis=-1)
        o = torch.einsum("bhqk,bhkd->bhqd", p, v)
        return (lambda x, *d: x.permute(*d))(o, 0, 2, 1, 3).reshape(B, Sq, Hq * Dh) @ wo

    def _B_ref(x, k_cache, v_cache, wq, wo):
        B, Sq, D = x.shape
        Hq, Dh = 32, 128
        Hkv = k_cache.shape[1]
        rep = Hq // Hkv
        q = (lambda x, *d: x.permute(*d))((x @ wq).reshape(B, Sq, Hq, Dh), 0, 2, 1, 3)
        k = (lambda x, n, axis: torch.repeat_interleave(x, n, dim=axis))(k_cache, rep, axis=1)
        v = (lambda x, n, axis: torch.repeat_interleave(x, n, dim=axis))(v_cache, rep, axis=1)
        scores = torch.einsum("bhqd,bhkd->bhqk", q, k) * (Dh ** -0.5)
        p = _softmax(scores, axis=-1)
        o = torch.einsum("bhqk,bhkd->bhqd", p, v)
        return (lambda x, *d: x.permute(*d))(o, 0, 2, 1, 3).reshape(B, Sq, Hq * Dh) @ wo

    a = _A_ref(A_x, A_k_cache, A_v_cache, A_wq, A_wo)
    b = _B_ref(B_x, B_k_cache, B_v_cache, B_wq, B_wo)
    return torch.concatenate([a, b], axis=-1)
```

```cpp
#include <hip/hip_runtime.h>
#include <hip/hip_bf16.h>
#include <stdint.h>

typedef unsigned u32x4 __attribute__((ext_vector_type(4)));
typedef float f32x4 __attribute__((ext_vector_type(4)));
typedef __bf16 bf16x2 __attribute__((ext_vector_type(2)));
typedef short bf16x8 __attribute__((ext_vector_type(8)));
typedef __bf16 bf16x8b __attribute__((ext_vector_type(8)));

constexpr int D = 576, HQ = 32, HKV = 16, SK = 8192, DH = 128, W = 2048, NQ = HQ * DH;

__device__ __forceinline__ float bflo(unsigned u) { return __builtin_bit_cast(float, u << 16); }
__device__ __forceinline__ float bfhi(unsigned u) { return __builtin_bit_cast(float, u & 0xffff0000u); }
__device__ __forceinline__ unsigned cvtpk(float lo, float hi) {
  bf16x2 v = {(__bf16)lo, (__bf16)hi}; return __builtin_bit_cast(unsigned, v);
}
__device__ __forceinline__ float dot2(unsigned k, unsigned q, float acc) {
  return __builtin_amdgcn_fdot2_f32_bf16(__builtin_bit_cast(bf16x2, k), __builtin_bit_cast(bf16x2, q), acc, false);
}
template <int CTRL> __device__ __forceinline__ float dpp(float x) {
  return __builtin_bit_cast(float, __builtin_amdgcn_mov_dpp(__builtin_bit_cast(int, x), CTRL, 0xf, 0xf, true));
}
constexpr int XOR1 = 0xB1, XOR2 = 0x4E, XOR7 = 0x141, XOR8 = 0x128;
__device__ __forceinline__ f32x4 mfma(u32x4 a, u32x4 b, f32x4 c) {
  return __builtin_amdgcn_mfma_f32_16x16x32_bf16(__builtin_bit_cast(bf16x8b, a), __builtin_bit_cast(bf16x8b, b), c, 0, 0, 0);
}

__global__ void __launch_bounds__(256) k_q(const uint16_t* xA, const uint16_t* wqA, const uint16_t* xB, const uint16_t* wqB, float* qpart) {
  const int w = blockIdx.x, br = w >> 7, cb = (w & 127) >> 1, half = w & 1;
  const uint16_t* x = br ? xB : xA; const uint16_t* wq = br ? wqB : wqA;
  const int tid = threadIdx.x, rr = tid >> 3, c8 = tid & 7;
  float acc[8] = {0,0,0,0,0,0,0,0};
#pragma unroll
  for (int p = 0; p < 9; ++p) {
    const int r = half * 288 + p * 32 + rr;
    const u32x4 v = *(const u32x4*)(wq + (size_t)r * NQ + cb * 64 + c8 * 8);
    const float xv = bflo(x[r]);
#pragma unroll
    for (int k = 0; k < 4; ++k) { acc[2*k] += xv * bflo(v[k]); acc[2*k+1] += xv * bfhi(v[k]); }
  }
  __shared__ float red[32][65];
#pragma unroll
  for (int k = 0; k < 8; ++k) red[rr][c8 * 8 + k] = acc[k];
  __syncthreads();
  if (tid < 64) { float s = 0; for (int i = 0; i < 32; ++i) s += red[i][tid];
    qpart[(size_t)half * 2 * NQ + br * NQ + cb * 64 + tid] = s; }
}

template <int NL> __device__ __forceinline__ void attn_part(const uint16_t* kp, const uint16_t* vp, const float* qpart, int qoff, float* sm, int wave, int lane, float* dst) {
  const int g = lane >> 4, i = lane & 15;
  u32x4 K[NL], V[NL];
#pragma unroll
  for (int t = 0; t < NL; ++t) K[t] = __builtin_nontemporal_load((const u32x4*)(kp + (size_t)(t * 4 + g) * DH + i * 8));
#pragma unroll
  for (int t = 0; t < NL; ++t) V[t] = __builtin_nontemporal_load((const u32x4*)(vp + (size_t)(t * 4 + g) * DH + i * 8));
  unsigned q0[4], q1[4];
#pragma unroll
  for (int k = 0; k < 4; ++k) {
    const float* a = qpart + qoff + i * 8 + 2 * k; const float* b = a + 2 * NQ;
    q0[k] = cvtpk(a[0] + b[0], a[1] + b[1]);
    q1[k] = cvtpk(a[128] + b[128], a[129] + b[129]);
  }
  float s[NL];
  constexpr float C2 = 0.088388347648318440f * 1.4426950408889634f;
  float m = -INFINITY;
#pragma unroll
  for (int t = 0; t < NL; ++t) {
    float d0 = 0, d1 = 0;
#pragma unroll
    for (int k = 0; k < 4; ++k) { d0 = dot2(K[t][k], q0[k], d0); d1 = dot2(K[t][k], q1[k], d1); }
    d0 += dpp<XOR8>(d0); d1 += dpp<XOR8>(d1);
    float d = (i & 8) ? d1 : d0;
    d += dpp<XOR7>(d); d += dpp<XOR2>(d); d += dpp<XOR1>(d);
    s[t] = d * C2; m = fmaxf(m, s[t]);
  }
  m = fmaxf(m, __shfl_xor(m, 16)); m = fmaxf(m, __shfl_xor(m, 32));
  unsigned msk[4];
#pragma unroll
  for (int k = 0; k < 4; ++k) msk[k] = (((i & 7) >> 1) == k) ? ((i & 1) ? 0xffff0000u : 0x0000ffffu) : 0u;
  float l = 0; f32x4 acc = {0, 0, 0, 0};
#pragma unroll
  for (int t = 0; t < NL; ++t) {
    const float p = __builtin_amdgcn_exp2f(s[t] - m);
    const unsigned pp = cvtpk(p, p);
    l += bflo(pp);
    u32x4 a = {pp & msk[0], pp & msk[1], pp & msk[2], pp & msk[3]};
    acc = mfma(a, V[t], acc);
  }
  l += __shfl_xor(l, 16); l += __shfl_xor(l, 32);
  float* rec = sm + (wave * 2 + (g >> 1)) * 132;
  *(f32x4*)(rec + 4 + 8 * i + 4 * (g & 1)) = acc;
  if (lane == 0 || lane == 8) { float* r2 = sm + (wave * 2 + (lane >> 3)) * 132; r2[0] = m; r2[1] = l; }
  __syncthreads();
  const int tid = wave * 64 + lane;
  if (tid < 256) {
    const int hd = tid >> 7, d = tid & 127;
    float M = -INFINITY;
    for (int w2 = 0; w2 < 8; ++w2) M = fmaxf(M, sm[(w2 * 2 + hd) * 132]);
    float L = 0, o = 0;
    for (int w2 = 0; w2 < 8; ++w2) { const float* r2 = sm + (w2 * 2 + hd) * 132; const float e = __builtin_amdgcn_exp2f(r2[0] - M); L += e * r2[1]; o += e * r2[4 + d]; }
    dst[hd * 132 + 4 + d] = o;
    if (d == 0) { dst[hd * 132] = M; dst[hd * 132 + 1] = L; }
  }
  __syncthreads();
}

__global__ void __launch_bounds__(512) k_attn(const uint16_t* kA, const uint16_t* vA, const uint16_t* kB, const uint16_t* vB, const float* qpart, float* part) {
  __shared__ __attribute__((aligned(16))) float sm[16 * 132];
  const int w = blockIdx.x, h = w >> 4, s = w & 15;
  const int wave = __builtin_amdgcn_readfirstlane(threadIdx.x >> 6), lane = threadIdx.x & 63;
  {
    const size_t off = ((size_t)h * SK + (SK - W) + s * 128 + wave * 16) * DH;
    attn_part<4>(kA + off, vA + off, qpart, h * 256, sm, wave, lane, part + ((size_t)(0 * 16 + h) * 16 + s) * 264);
  }
  {
    const size_t off = ((size_t)h * SK + s * 512 + wave * 64) * DH;
    attn_part<16>(kB + off, vB + off, qpart, NQ + h * 256, sm, wave, lane, part + ((size_t)(1 * 16 + h) * 16 + s) * 264);
  }
}

__global__ void __launch_bounds__(256) k_out(const float* part, const uint16_t* woA, const uint16_t* woB, float* ypart) {
  const int w = blockIdx.x, br = w >> 7, rb = w & 127, h = rb >> 3, hd = (rb >> 2) & 1, d0 = (rb & 3) * 32;
  const uint16_t* wo = (br ? woB : woA) + (size_t)rb * 32 * D;
  __shared__ float o[32];
  __shared__ float red[4][576];
  const int tid = threadIdx.x;
  if (tid < 32) {
    const float* p = part + ((size_t)(br * 16 + h) * 16) * 264 + hd * 132;
    float M = -INFINITY;
    for (int s = 0; s < 16; ++s) M = fmaxf(M, p[s * 264]);
    float L = 0, a = 0;
    for (int s = 0; s < 16; ++s) { const float e = __builtin_amdgcn_exp2f(p[s * 264] - M); L += e * p[s * 264 + 1]; a += e * p[s * 264 + 4 + d0 + tid]; }
    o[tid] = bflo(cvtpk(a / L, 0.f));
  }
  __syncthreads();
  if (tid < 216) {
    const int rg = tid / 72, c = tid % 72;
    float acc[8] = {0,0,0,0,0,0,0,0};
    for (int r = rg; r < 32; r += 3) {
      const u32x4 v = *(const u32x4*)(wo + (size_t)r * D + c * 8);
      const float xv = o[r];
#pragma unroll
      for (int k = 0; k < 4; ++k) { acc[2*k] += xv * bflo(v[k]); acc[2*k+1] += xv * bfhi(v[k]); }
    }
#pragma unroll
    for (int k = 0; k < 8; ++k) red[rg][c * 8 + k] = acc[k];
  }
  __syncthreads();
  for (int c = tid; c < 576; c += 256) ypart[(size_t)w * 576 + c] = red[0][c] + red[1][c] + red[2][c];
}

__global__ void __launch_bounds__(256) k_fin(const float* ypart, uint16_t* out) {
  const int br = blockIdx.x / 9, c = (blockIdx.x % 9) * 64 + (threadIdx.x & 63), part = threadIdx.x >> 6;
  float s = 0;
  for (int i = part; i < 128; i += 4) s += ypart[(size_t)(br * 128 + i) * 576 + c];
  __shared__ float red[4][64];
  red[part][threadIdx.x & 63] = s;
  __syncthreads();
  if (threadIdx.x < 64) out[br * 576 + c] = (uint16_t)(cvtpk(red[0][threadIdx.x] + red[1][threadIdx.x] + red[2][threadIdx.x] + red[3][threadIdx.x], 0.f) & 0xffff);
}

extern "C" void kernel_launch(void* const* d_in, const int* in_sizes, int n_in, void* d_out, int out_size, void* d_ws, size_t ws_size, hipStream_t stream) {
  const uint16_t* xA = (const uint16_t*)d_in[0]; const uint16_t* kA = (const uint16_t*)d_in[1]; const uint16_t* vA = (const uint16_t*)d_in[2];
  const uint16_t* wqA = (const uint16_t*)d_in[3]; const uint16_t* woA = (const uint16_t*)d_in[4];
  const uint16_t* xB = (const uint16_t*)d_in[5]; const uint16_t* kB = (const uint16_t*)d_in[6]; const uint16_t* vB = (const uint16_t*)d_in[7];
  const uint16_t* wqB = (const uint16_t*)d_in[8]; const uint16_t* woB = (const uint16_t*)d_in[9];
  float* ws = (float*)d_ws;
  float* qpart = ws;
  float* part = ws + 16384;
  float* ypart = part + 32 * 16 * 264;
  k_q<<<256, 256, 0, stream>>>(xA, wqA, xB, wqB, qpart);
  k_attn<<<256, 512, 0, stream>>>(kA, vA, kB, vB, qpart, part);
  k_out<<<256, 256, 0, stream>>>(part, woA, woB, ypart);
  k_fin<<<18, 256, 0, stream>>>(ypart, (uint16_t*)d_out);
}
```

```cpp
#include <hip/hip_runtime.h>
#include <hip/hip_bf16.h>
#include <stdint.h>

#define GAS __attribute__((address_space(1)))
#define LAS __attribute__((address_space(3)))
#define RLX_AGENT __ATOMIC_RELAXED, __HIP_MEMORY_SCOPE_AGENT
#define RLX_WG __ATOMIC_RELAXED, __HIP_MEMORY_SCOPE_WORKGROUP
typedef unsigned u32x4 __attribute__((ext_vector_type(4)));
typedef unsigned u32x2 __attribute__((ext_vector_type(2)));
typedef float f32x4 __attribute__((ext_vector_type(4)));
typedef __bf16 bf16x2 __attribute__((ext_vector_type(2)));
typedef __bf16 bf16x8 __attribute__((ext_vector_type(8)));
typedef unsigned long long u64;
typedef GAS const unsigned char* gptr;

constexpr int DM = 576, SK = 8192, DH = 128, WIN = 2048, NQ = 4096;
constexpr int NWAVE = 11, NTHR = NWAVE * 64;
constexpr size_t WS_EPOCH = 0, WS_Q = 4096, WS_O = WS_Q + 16 * 2 * 2 * 256 * 8, WS_Y = WS_O + 16 * 16 * 16 * 384, WS_END = WS_Y + 144 * 8192;
constexpr size_t WS_STAMP = 8u << 20;
constexpr unsigned SPIN_CAP = 1u << 17;

__device__ __forceinline__ float bflo(unsigned u) { return __builtin_bit_cast(float, u << 16); }
__device__ __forceinline__ float bfhi(unsigned u) { return __builtin_bit_cast(float, u & 0xffff0000u); }
__device__ __forceinline__ unsigned cvtpk(float lo, float hi) { bf16x2 v = {(__bf16)lo, (__bf16)hi}; return __builtin_bit_cast(unsigned, v); }
__device__ __forceinline__ float dot2(unsigned k, unsigned q, float acc) {
  return __builtin_amdgcn_fdot2_f32_bf16(__builtin_bit_cast(bf16x2, k), __builtin_bit_cast(bf16x2, q), acc, false);
}
template <int CTRL> __device__ __forceinline__ float dpp(float x) {
  return __builtin_bit_cast(float, __builtin_amdgcn_mov_dpp(__builtin_bit_cast(int, x), CTRL, 0xf, 0xf, true));
}
constexpr int XOR1 = 0xB1, XOR2 = 0x4E, XOR7 = 0x141, XOR8 = 0x128;
__device__ __forceinline__ f32x4 mfma(u32x4 a, u32x4 b, f32x4 c) {
  return __builtin_amdgcn_mfma_f32_16x16x32_bf16(__builtin_bit_cast(bf16x8, a), __builtin_bit_cast(bf16x8, b), c, 0, 0, 0);
}
__device__ __forceinline__ float fold32(float a, float b) {
  auto r = __builtin_amdgcn_permlane32_swap(__float_as_uint(a), __float_as_uint(b), false, false);
  return __uint_as_float(r[0]) + __uint_as_float(r[1]);
}
__device__ __forceinline__ float fold16(float a, float b) {
  auto r = __builtin_amdgcn_permlane16_swap(__float_as_uint(a), __float_as_uint(b), false, false);
  return __uint_as_float(r[0]) + __uint_as_float(r[1]);
}
__device__ __forceinline__ float max16_32(float x) {
  auto s = __builtin_amdgcn_permlane16_swap(__float_as_uint(x), __float_as_uint(x), false, false);
  x = fmaxf(__uint_as_float(s[0]), __uint_as_float(s[1]));
  auto t = __builtin_amdgcn_permlane32_swap(__float_as_uint(x), __float_as_uint(x), false, false);
  return fmaxf(__uint_as_float(t[0]), __uint_as_float(t[1]));
}
__device__ __forceinline__ float tr_reduce8(const float (&a)[8], int lane) {
  const float b0 = fold32(a[0], a[4]), b1 = fold32(a[1], a[5]), b2 = fold32(a[2], a[6]), b3 = fold32(a[3], a[7]);
  const float c0 = fold16(b0, b2), c1 = fold16(b1, b3);
  const float d0 = c0 + dpp<XOR8>(c0), d1 = c1 + dpp<XOR8>(c1);
  return (lane & 8) ? d1 : d0;
}
typedef __amdgpu_buffer_rsrc_t rsrc_t;
__device__ __forceinline__ rsrc_t mkrs(const void* p, unsigned bytes) { return __builtin_amdgcn_make_buffer_rsrc((void*)p, 0, bytes, 0x00020000); }
#define AUXKV 18
#define AUXW 18
template <int AUX> __device__ __forceinline__ u32x4 ldb(rsrc_t r, unsigned voff, unsigned soff) { return __builtin_amdgcn_raw_buffer_load_b128(r, voff, soff, AUX); }
__device__ __forceinline__ u64 ldg(GAS u64* p) { return __hip_atomic_load(p, RLX_AGENT); }
__device__ __forceinline__ void stg(GAS u64* p, unsigned tag, float v) { __hip_atomic_store(p, ((u64)tag << 32) | __float_as_uint(v), RLX_AGENT); }
#define SB() __builtin_amdgcn_sched_barrier(0)
#define ST(i)
#define STDUMP()
#define STDECL()

struct Lds {
  float part[10][2][132];
  unsigned q[2][2][64];
  unsigned x[144];
  float o[32];
  float fin[8][8];
  unsigned qready, issued;
};

__global__ void __launch_bounds__(NTHR, 3) fused_decode(const uint16_t* xA, const uint16_t* kA, const uint16_t* vA, const uint16_t* wqA, const uint16_t* woA,
                                                         const uint16_t* xB, const uint16_t* kB, const uint16_t* vB, const uint16_t* wqB, const uint16_t* woB,
                                                         uint16_t* out, unsigned char* wsp) {
  __shared__ __attribute__((aligned(16))) Lds L;
  GAS unsigned char* ws = (GAS unsigned char*)wsp;
  const int tid = threadIdx.x, lane = tid & 63, wave = __builtin_amdgcn_readfirstlane(tid >> 6);
  const int w = blockIdx.x, h = (w & 7) * 2 + (w >> 7), s = (w >> 3) & 15;
  const int pbr = s >> 3;
  STDECL();
  ST(0);
  unsigned epoch = __hip_atomic_load((GAS unsigned*)(ws + WS_EPOCH), RLX_AGENT), tag;

  if (wave == 0) {
    const int half = s & 1, cb = (s >> 1) & 3, rs = lane >> 3, c = lane & 7;
    const unsigned xoff = (lane < 36 ? lane : 0) * 16;
    const u32x4 xr = *(GAS const u32x4*)((gptr)(pbr ? xB : xA) + half * 576 + xoff);
    const rsrc_t wr = mkrs((pbr ? wqB : wqA) + (half * 288) * NQ + h * 256 + cb * 64, 288 * NQ * 2);
    const unsigned woff = (unsigned)(rs * 36 * NQ + c * 8) * 2;
    u32x4 wv[36];
#pragma unroll
    for (int t = 0; t < 36; ++t) wv[t] = ldb<AUXW>(wr, woff, t * NQ * 2);
    SB();
    __builtin_amdgcn_s_barrier();
    SB();
    ST(8);
    if (lane < 36) *(u32x4*)&L.x[lane * 4] = xr;
    float acc[8] = {0, 0, 0, 0, 0, 0, 0, 0};
#pragma unroll
    for (int t4 = 0; t4 < 9; ++t4) {
      const u32x2 xw = *(const u32x2*)&L.x[rs * 18 + t4 * 2];
#pragma unroll
      for (int u = 0; u < 4; ++u) {
        const int t = t4 * 4 + u;
        const float xv = (u & 1) ? bfhi(xw[u >> 1]) : bflo(xw[u >> 1]);
#pragma unroll
        for (int k = 0; k < 4; ++k) { acc[2 * k] = fmaf(xv, bflo(wv[t][k]), acc[2 * k]); acc[2 * k + 1] = fmaf(xv, bfhi(wv[t][k]), acc[2 * k + 1]); }
      }
      SB();
    }
    const float qv = tr_reduce8(acc, lane);
    asm volatile("" : "+v"(epoch));
    tag = __builtin_amdgcn_readfirstlane(epoch) + 1u;
    GAS u64* qg = (GAS u64*)(ws + WS_Q) + h * 1024;
    stg(qg + (pbr * 2 + half) * 256 + cb * 64 + c * 8 + (lane >> 3), tag, qv);
    ST(1);
    u64 g[16];
    for (unsigned spins = 0;; ++spins) {
      bool ok = true;
#pragma unroll
      for (int i = 0; i < 16; ++i) { g[i] = ldg(qg + (i >> 2) * 256 + ((i >> 1) & 1) * 128 + 2 * lane + (i & 1)); ok &= (unsigned)(g[i] >> 32) == tag; }
      if (__all(ok) || spins > SPIN_CAP) break;
    }
#pragma unroll
    for (int b = 0; b < 2; ++b)
#pragma unroll
      for (int hd = 0; hd < 2; ++hd) {
        const int i = b * 8 + hd * 2;
        L.q[b][hd][lane] = cvtpk(__uint_as_float((unsigned)g[i]) + __uint_as_float((unsigned)g[i + 4]), __uint_as_float((unsigned)g[i + 1]) + __uint_as_float((unsigned)g[i + 5]));
      }
    __builtin_amdgcn_fence(__ATOMIC_RELEASE, "workgroup");
    if (lane == 0) __hip_atomic_store(&L.qready, 1u, RLX_WG);
    ST(2);
  } else {
    if (tid == 64) { L.qready = 0u; L.issued = 0u; }
    const int sw = wave - 1, br = sw >= 2, pair = br ? (sw - 2) >> 1 : 3;
    const int g = lane >> 4, i = lane & 15;
    const size_t off = ((size_t)h * 2048 + pair * 512 + s * 8 + (sw & 1) * 4) * 1024;
    const rsrc_t kr = mkrs((const char*)(br ? kB : kA) + off, 512 * 1024), vr = mkrs((const char*)(br ? vB : vA) + off, 512 * 1024);
    const unsigned loff = lane * 16;
    asm volatile("s_waitcnt lgkmcnt(0)" ::: "memory");
    __builtin_amdgcn_s_barrier();
    SB();
    ST(8);
    u32x4 K[16], V[16];
#pragma unroll
    for (int t = 0; t < 16; ++t) K[t] = ldb<AUXKV>(kr, loff + (t & 3) * 1024, (t >> 2) * 131072);
    ST(9);
#pragma unroll
    for (int t = 0; t < 16; ++t) V[t] = ldb<AUXKV>(vr, loff + (t & 3) * 1024, (t >> 2) * 131072);
    SB();
    if (lane == 0) __hip_atomic_fetch_add(&L.issued, 1u, RLX_WG);
    ST(1);
    for (unsigned spins = 0; __hip_atomic_load(&L.qready, RLX_WG) == 0u && spins < (SPIN_CAP << 3); ++spins) __builtin_amdgcn_s_sleep(2);
    __builtin_amdgcn_fence(__ATOMIC_ACQUIRE, "workgroup");
    const u32x4 q0 = *(const u32x4*)&L.q[br][0][4 * i], q1 = *(const u32x4*)&L.q[br][1][4 * i];
    constexpr float C2 = 0.088388347648318440f * 1.4426950408889634f;
    float sc[16], m = -INFINITY;
#pragma unroll
    for (int t = 0; t < 16; ++t) {
      float d0 = 0, d1 = 0;
#pragma unroll
      for (int k = 0; k < 4; ++k) { d0 = dot2(K[t][k], q0[k], d0); d1 = dot2(K[t][k], q1[k], d1); }
      d0 += dpp<XOR8>(d0); d1 += dpp<XOR8>(d1);
      float d = (i & 8) ? d1 : d0;
      d += dpp<XOR7>(d); d += dpp<XOR2>(d); d += dpp<XOR1>(d);
      sc[t] = d; m = fmaxf(m, d);
    }
    m = max16_32(m) * C2;
    asm volatile("" : "+v"(epoch));
    tag = __builtin_amdgcn_readfirstlane(epoch) + 1u;
    ST(2);
    unsigned msk[4];
#pragma unroll
    for (int k = 0; k < 4; ++k) msk[k] = (((i & 7) >> 1) == k) ? ((i & 1) ? 0xffff0000u : 0x0000ffffu) : 0u;
    float l = 0; f32x4 acc = {0, 0, 0, 0};
#pragma unroll
    for (int t = 0; t < 16; ++t) {
      const float p = __builtin_amdgcn_exp2f(fmaf(sc[t], C2, -m));
      const unsigned pp = cvtpk(p, p);
      l += bflo(pp);
      const u32x4 a = {pp & msk[0], pp & msk[1], pp & msk[2], pp & msk[3]};
      acc = mfma(a, V[t], acc);
    }
    l = fold16(l, l); l = fold32(l, l);
    *(f32x4*)&L.part[sw][g >> 1][4 + 8 * i + 4 * (g & 1)] = acc;
    if ((lane & 55) == 0) { L.part[sw][lane >> 3][0] = m; L.part[sw][lane >> 3][1] = l; }
    ST(3);
  }
  u32x4 wo[4];
  if (wave < 9) {
    for (unsigned spins = 0; __hip_atomic_load(&L.issued, RLX_WG) < 10u && spins < (SPIN_CAP << 3); ++spins) __builtin_amdgcn_s_sleep(2);
    const rsrc_t wr = mkrs((pbr ? woB : woA) + (h * 256 + (s & 7) * 32) * DM + wave * 64, 32 * DM * 2);
    const unsigned woff = ((lane >> 3) * 4 * DM + (lane & 7) * 8) * 2;
#pragma unroll
    for (int t = 0; t < 4; ++t) wo[t] = ldb<AUXW>(wr, woff + t * DM * 2, 0);
  }
  SB();
  asm volatile("s_waitcnt lgkmcnt(0)" :: "s"(tag) : "memory");
  __builtin_amdgcn_s_barrier();
  SB();
  ST(4);
  if (wave < 8) {
    const int br = wave >> 2, hd = (wave >> 1) & 1, d = tid & 127;
    float M, Lsum, o;
    if (br == 0) {
      const float m0 = L.part[0][hd][0], m1 = L.part[1][hd][0];
      M = fmaxf(m0, m1);
      const float e0 = __builtin_amdgcn_exp2f(m0 - M), e1 = __builtin_amdgcn_exp2f(m1 - M);
      Lsum = e0 * L.part[0][hd][1] + e1 * L.part[1][hd][1];
      o = e0 * L.part[0][hd][4 + d] + e1 * L.part[1][hd][4 + d];
    } else {
      float mm[8]; M = -INFINITY;
#pragma unroll
      for (int v = 0; v < 8; ++v) { mm[v] = L.part[2 + v][hd][0]; M = fmaxf(M, mm[v]); }
      Lsum = 0; o = 0;
#pragma unroll
      for (int v = 0; v < 8; ++v) { const float e = __builtin_amdgcn_exp2f(mm[v] - M); Lsum = fmaf(e, L.part[2 + v][hd][1], Lsum); o = fmaf(e, L.part[2 + v][hd][4 + d], o); }
    }
    GAS u64* og = (GAS u64*)(ws + WS_O) + (size_t)h * (16 * 16 * 48);
    GAS u64* dst = og + ((tid >> 5) * 16 + s) * 48;
    stg(dst + (tid & 31), tag, o * __builtin_amdgcn_rcpf(Lsum));
    if ((tid & 31) == 0) stg(dst + 32, tag, M + __builtin_amdgcn_logf(Lsum));
    GAS u64* src = og + (s * 16 + (lane & 15)) * 48;
    u64 go, gl;
    for (unsigned spins = 0;; ++spins) {
      go = ldg(src + 4 * wave + (lane >> 4)); gl = ldg(src + 32);
      if (__all((unsigned)(go >> 32) == tag && (unsigned)(gl >> 32) == tag) || spins > SPIN_CAP) break;
    }
    const float lse = __uint_as_float((unsigned)gl);
    float mx = fmaxf(lse, dpp<XOR8>(lse)); mx = fmaxf(mx, dpp<XOR7>(mx)); mx = fmaxf(mx, dpp<XOR2>(mx)); mx = fmaxf(mx, dpp<XOR1>(mx));
    float e = __builtin_amdgcn_exp2f(lse - mx), a = e * __uint_as_float((unsigned)go);
    e += dpp<XOR8>(e); a += dpp<XOR8>(a); e += dpp<XOR7>(e); a += dpp<XOR7>(a); e += dpp<XOR2>(e); a += dpp<XOR2>(a); e += dpp<XOR1>(e); a += dpp<XOR1>(a);
    if ((lane & 15) == 0) L.o[4 * wave + (lane >> 4)] = a * __builtin_amdgcn_rcpf(e);
  }
  ST(5);
  asm volatile("s_waitcnt lgkmcnt(0)" ::: "memory");
  __builtin_amdgcn_s_barrier();
  SB();
  if (wave < 9) {
    const f32x4 ov = *(const f32x4*)&L.o[(lane >> 3) * 4];
    float acc[8] = {0, 0, 0, 0, 0, 0, 0, 0};
#pragma unroll
    for (int t = 0; t < 4; ++t)
#pragma unroll
      for (int k = 0; k < 4; ++k) { acc[2 * k] = fmaf(ov[t], bflo(wo[t][k]), acc[2 * k]); acc[2 * k + 1] = fmaf(ov[t], bfhi(wo[t][k]), acc[2 * k + 1]); }
    const float y = tr_reduce8(acc, lane);
    GAS u64* yg = (GAS u64*)(ws + WS_Y);
    stg(yg + ((size_t)(pbr * 72 + wave * 8 + (lane & 7)) * 128 + h * 8 + (s & 7)) * 8 + (lane >> 3), tag, y);
  }
  ST(6);
  if (w >= 144) { STDUMP(); return; }
  if (wave < 8) {
    GAS u64* src = (GAS u64*)(ws + WS_Y) + (size_t)w * 1024 + wave * 128 + lane;
    u64 g0, g1;
    for (unsigned spins = 0;; ++spins) {
      g0 = ldg(src); g1 = ldg(src + 64);
      if (__all((unsigned)(g0 >> 32) == tag && (unsigned)(g1 >> 32) == tag) || spins > SPIN_CAP) break;
    }
    float y = __uint_as_float((unsigned)g0) + __uint_as_float((unsigned)g1);
    y += dpp<XOR8>(y); y = fold16(y, y); y = fold32(y, y);
    if (lane < 8) L.fin[wave][lane] = y;
  }
  asm volatile("s_waitcnt lgkmcnt(0)" ::: "memory");
  __builtin_amdgcn_s_barrier();
  SB();
  if (wave == 0) {
    float y = L.fin[lane >> 3][lane & 7];
    y += dpp<XOR8>(y); y = fold16(y, y); y = fold32(y, y);
    const float yn = dpp<XOR1>(y);
    if (lane < 8 && !(lane & 1)) ((GAS unsigned*)out)[w * 4 + (lane >> 1)] = cvtpk(y, yn);
    if (w == 0 && lane == 0) __hip_atomic_store((GAS unsigned*)(ws + WS_EPOCH), tag, RLX_AGENT);
    ST(7);
  }
  STDUMP();
}

extern "C" void kernel_launch(void* const* d_in, const int* in_sizes, int n_in, void* d_out, int out_size, void* d_ws, size_t ws_size, hipStream_t stream) {
  fused_decode<<<256, NTHR, 0, stream>>>((const uint16_t*)d_in[0], (const uint16_t*)d_in[1], (const uint16_t*)d_in[2], (const uint16_t*)d_in[3], (const uint16_t*)d_in[4],
                                         (const uint16_t*)d_in[5], (const uint16_t*)d_in[6], (const uint16_t*)d_in[7], (const uint16_t*)d_in[8], (const uint16_t*)d_in[9],
                                         (uint16_t*)d_out, (unsigned char*)d_ws);
}
```

```cpp
#define PROBE 0
#define PW 0
#define PK 0
#include <hip/hip_runtime.h>
#include <hip/hip_bf16.h>
#include <stdint.h>

#define GAS __attribute__((address_space(1)))
#define LAS __attribute__((address_space(3)))
#define RLX_AGENT __ATOMIC_RELAXED, __HIP_MEMORY_SCOPE_AGENT
#define RLX_WG __ATOMIC_RELAXED, __HIP_MEMORY_SCOPE_WORKGROUP
typedef unsigned u32x4 __attribute__((ext_vector_type(4)));
typedef unsigned u32x2 __attribute__((ext_vector_type(2)));
typedef float f32x4 __attribute__((ext_vector_type(4)));
typedef __bf16 bf16x2 __attribute__((ext_vector_type(2)));
typedef __bf16 bf16x8 __attribute__((ext_vector_type(8)));
typedef unsigned long long u64;
typedef GAS const unsigned char* gptr;

constexpr int DM = 576, SK = 8192, DH = 128, WIN = 2048, NQ = 4096;
constexpr int NWAVE = 11, NTHR = NWAVE * 64;
constexpr size_t WS_EPOCH = 0, WS_Q = 4096, WS_O = WS_Q + 16 * 2 * 2 * 256 * 8, WS_Y = WS_O + 16 * 16 * 16 * 384, WS_END = WS_Y + 144 * 8192;
constexpr size_t WS_STAMP = 8u << 20;
constexpr unsigned SPIN_CAP = 1u << 17;

__device__ __forceinline__ float bflo(unsigned u) { return __builtin_bit_cast(float, u << 16); }
__device__ __forceinline__ float bfhi(unsigned u) { return __builtin_bit_cast(float, u & 0xffff0000u); }
__device__ __forceinline__ unsigned cvtpk(float lo, float hi) { bf16x2 v = {(__bf16)lo, (__bf16)hi}; return __builtin_bit_cast(unsigned, v); }
__device__ __forceinline__ float dot2(unsigned k, unsigned q, float acc) {
  return __builtin_amdgcn_fdot2_f32_bf16(__builtin_bit_cast(bf16x2, k), __builtin_bit_cast(bf16x2, q), acc, false);
}
template <int CTRL> __device__ __forceinline__ float dpp(float x) {
  return __builtin_bit_cast(float, __builtin_amdgcn_mov_dpp(__builtin_bit_cast(int, x), CTRL, 0xf, 0xf, true));
}
constexpr int XOR1 = 0xB1, XOR2 = 0x4E, XOR7 = 0x141, XOR8 = 0x128;
__device__ __forceinline__ f32x4 mfma(u32x4 a, u32x4 b, f32x4 c) {
  return __builtin_amdgcn_mfma_f32_16x16x32_bf16(__builtin_bit_cast(bf16x8, a), __builtin_bit_cast(bf16x8, b), c, 0, 0, 0);
}
__device__ __forceinline__ float fold32(float a, float b) {
  auto r = __builtin_amdgcn_permlane32_swap(__float_as_uint(a), __float_as_uint(b), false, false);
  return __uint_as_float(r[0]) + __uint_as_float(r[1]);
}
__device__ __forceinline__ float fold16(float a, float b) {
  auto r = __builtin_amdgcn_permlane16_swap(__float_as_uint(a), __float_as_uint(b), false, false);
  return __uint_as_float(r[0]) + __uint_as_float(r[1]);
}
__device__ __forceinline__ float max16_32(float x) {
  auto s = __builtin_amdgcn_permlane16_swap(__float_as_uint(x), __float_as_uint(x), false, false);
  x = fmaxf(__uint_as_float(s[0]), __uint_as_float(s[1]));
  auto t = __builtin_amdgcn_permlane32_swap(__float_as_uint(x), __float_as_uint(x), false, false);
  return fmaxf(__uint_as_float(t[0]), __uint_as_float(t[1]));
}
__device__ __forceinline__ float tr_reduce8(const float (&a)[8], int lane) {
  const float b0 = fold32(a[0], a[4]), b1 = fold32(a[1], a[5]), b2 = fold32(a[2], a[6]), b3 = fold32(a[3], a[7]);
  const float c0 = fold16(b0, b2), c1 = fold16(b1, b3);
  const float d0 = c0 + dpp<XOR8>(c0), d1 = c1 + dpp<XOR8>(c1);
  return (lane & 8) ? d1 : d0;
}
typedef __amdgpu_buffer_rsrc_t rsrc_t;
__device__ __forceinline__ rsrc_t mkrs(const void* p, unsigned bytes) { return __builtin_amdgcn_make_buffer_rsrc((void*)p, 0, bytes, 0x00020000); }
#define AUXKV 2
#define AUXW 2
template <int AUX> __device__ __forceinline__ u32x4 ldb(rsrc_t r, unsigned voff, unsigned soff) { return __builtin_amdgcn_raw_buffer_load_b128(r, voff, soff, AUX); }
__device__ __forceinline__ u64 ldg(GAS u64* p) { return __hip_atomic_load(p, RLX_AGENT); }
__device__ __forceinline__ void stg(GAS u64* p, unsigned tag, float v) { __hip_atomic_store(p, ((u64)tag << 32) | __float_as_uint(v), RLX_AGENT); }
#define SB() __builtin_amdgcn_sched_barrier(0)
#define STAMP
#define ST(i) do { SB(); u64 t_; asm volatile("s_memrealtime %0\n\ts_waitcnt lgkmcnt(0)" : "=s"(t_) :: "memory"); stp[i] = (unsigned)t_; SB(); } while (0)
#define STDUMP() do { if (wave == PW && lane == 0) { GAS u64* pg = (GAS u64*)(ws + WS_STAMP); \
    __hip_atomic_store(pg + blockIdx.x, ((u64)tag << 32) | stp[0], RLX_AGENT); __hip_atomic_store(pg + 256 + blockIdx.x, ((u64)tag << 32) | (stp[PK] ? stp[PK] : stp[0]), RLX_AGENT); } \
  if (blockIdx.x == 0 && wave == 0) { GAS u64* pg = (GAS u64*)(ws + WS_STAMP); u64 a[4], b[4]; \
    for (unsigned sp = 0; sp < SPIN_CAP; ++sp) { bool ok = true; for (int i_ = 0; i_ < 4; ++i_) { a[i_] = ldg(pg + lane + 64 * i_); b[i_] = ldg(pg + 256 + lane + 64 * i_); ok &= (unsigned)(a[i_] >> 32) == tag && (unsigned)(b[i_] >> 32) == tag; } if (__all(ok)) break; } \
    int mn = 0x7fffffff, mx = -0x7fffffff, sm = 0; for (int i_ = 0; i_ < 4; ++i_) { mn = min(mn, (int)((unsigned)a[i_] - stp[0])); mx = max(mx, (int)((unsigned)b[i_] - stp[0])); sm += (int)((unsigned)b[i_] - stp[0]); } \
    for (int o_ = 1; o_ < 64; o_ <<= 1) { mn = min(mn, __shfl_xor(mn, o_)); mx = max(mx, __shfl_xor(mx, o_)); sm += __shfl_xor(sm, o_); } \
    const int X = PROBE == 0 ? 0 : PROBE == 1 ? mx - mn : sm / 256 - mn; \
    const unsigned until = stp[0] + mn + 5000 + 4 * X; \
    if (lane == 0) *(GAS unsigned*)(ws + WS_STAMP + 65536) = until; } } while (0)
#define STDECL() unsigned stp[12] = {0, 0, 0, 0, 0, 0, 0, 0, 0, 0, 0, 0}

struct Lds {
  float part[10][2][132];
  unsigned q[2][2][64];
  unsigned x[144];
  float o[32];
  float fin[8][8];
  unsigned qready, issued;
};

__global__ void __launch_bounds__(NTHR, 3) fused_decode(const uint16_t* xA, const uint16_t* kA, const uint16_t* vA, const uint16_t* wqA, const uint16_t* woA,
                                                         const uint16_t* xB, const uint16_t* kB, const uint16_t* vB, const uint16_t* wqB, const uint16_t* woB,
                                                         uint16_t* out, unsigned char* wsp) {
  __shared__ __attribute__((aligned(16))) Lds L;
  GAS unsigned char* ws = (GAS unsigned char*)wsp;
  const int tid = threadIdx.x, lane = tid & 63, wave = __builtin_amdgcn_readfirstlane(tid >> 6);
  const int w = blockIdx.x, h = (w & 7) * 2 + (w >> 7), s = (w >> 3) & 15;
  const int pbr = s >> 3;
  STDECL();
  ST(0);
  unsigned epoch = __hip_atomic_load((GAS unsigned*)(ws + WS_EPOCH), RLX_AGENT), tag;

  if (wave == 0) {
    const int half = s & 1, cb = (s >> 1) & 3, rs = lane >> 3, c = lane & 7;
    const unsigned xoff = (lane < 36 ? lane : 0) * 16;
    const u32x4 xr = *(GAS const u32x4*)((gptr)(pbr ? xB : xA) + half * 576 + xoff);
    const rsrc_t wr = mkrs((pbr ? wqB : wqA) + (half * 288) * NQ + h * 256 + cb * 64, 288 * NQ * 2);
    const unsigned woff = (unsigned)(rs * 36 * NQ + c * 8) * 2;
    u32x4 wv[36];
#pragma unroll
    for (int t = 0; t < 36; ++t) wv[t] = ldb<AUXW>(wr, woff, t * NQ * 2);
    SB();
    __builtin_amdgcn_s_barrier();
    SB();
    ST(8);
    if (lane < 36) *(u32x4*)&L.x[lane * 4] = xr;
    float acc[8] = {0, 0, 0, 0, 0, 0, 0, 0};
#pragma unroll
    for (int t4 = 0; t4 < 9; ++t4) {
      const u32x2 xw = *(const u32x2*)&L.x[rs * 18 + t4 * 2];
#pragma unroll
      for (int u = 0; u < 4; ++u) {
        const int t = t4 * 4 + u;
        const float xv = (u & 1) ? bfhi(xw[u >> 1]) : bflo(xw[u >> 1]);
#pragma unroll
        for (int k = 0; k < 4; ++k) { acc[2 * k] = fmaf(xv, bflo(wv[t][k]), acc[2 * k]); acc[2 * k + 1] = fmaf(xv, bfhi(wv[t][k]), acc[2 * k + 1]); }
      }
      SB();
    }
    const float qv = tr_reduce8(acc, lane);
    asm volatile("" : "+v"(epoch));
    tag = __builtin_amdgcn_readfirstlane(epoch) + 1u;
    GAS u64* qg = (GAS u64*)(ws + WS_Q) + h * 1024;
    stg(qg + (pbr * 2 + half) * 256 + cb * 64 + c * 8 + (lane >> 3), tag, qv);
    ST(1);
    u64 g[16];
    for (unsigned spins = 0;; ++spins) {
      bool ok = true;
#pragma unroll
      for (int i = 0; i < 16; ++i) { g[i] = ldg(qg + (i >> 2) * 256 + ((i >> 1) & 1) * 128 + 2 * lane + (i & 1)); ok &= (unsigned)(g[i] >> 32) == tag; }
      if (__all(ok) || spins > SPIN_CAP) break;
    }
#pragma unroll
    for (int b = 0; b < 2; ++b)
#pragma unroll
      for (int hd = 0; hd < 2; ++hd) {
        const int i = b * 8 + hd * 2;
        L.q[b][hd][lane] = cvtpk(__uint_as_float((unsigned)g[i]) + __uint_as_float((unsigned)g[i + 4]), __uint_as_float((unsigned)g[i + 1]) + __uint_as_float((unsigned)g[i + 5]));
      }
    __builtin_amdgcn_fence(__ATOMIC_RELEASE, "workgroup");
    if (lane == 0) __hip_atomic_store(&L.qready, 1u, RLX_WG);
    ST(2);
  } else {
    if (tid == 64) { L.qready = 0u; L.issued = 0u; }
    const int sw = wave - 1, br = sw >= 2, pair = br ? (sw - 2) >> 1 : 3;
    const int g = lane >> 4, i = lane & 15;
    const size_t off = ((size_t)h * 2048 + pair * 512 + s * 8 + (sw & 1) * 4) * 1024;
    const rsrc_t kr = mkrs((const char*)(br ? kB : kA) + off, 512 * 1024), vr = mkrs((const char*)(br ? vB : vA) + off, 512 * 1024);
    const unsigned loff = lane * 16;
    asm volatile("s_waitcnt lgkmcnt(0)" ::: "memory");
    __builtin_amdgcn_s_barrier();
    SB();
    ST(8);
    u32x4 K[16], V[16];
#pragma unroll
    for (int t = 0; t < 16; ++t) K[t] = ldb<AUXKV>(kr, loff + (t & 3) * 1024, (t >> 2) * 131072);
    ST(9);
#pragma unroll
    for (int t = 0; t < 16; ++t) V[t] = ldb<AUXKV>(vr, loff + (t & 3) * 1024, (t >> 2) * 131072);
    SB();
    if (lane == 0) __hip_atomic_fetch_add(&L.issued, 1u, RLX_WG);
    ST(1);
    for (unsigned spins = 0; __hip_atomic_load(&L.qready, RLX_WG) == 0u && spins < (SPIN_CAP << 3); ++spins) __builtin_amdgcn_s_sleep(2);
    __builtin_amdgcn_fence(__ATOMIC_ACQUIRE, "workgroup");
    const u32x4 q0 = *(const u32x4*)&L.q[br][0][4 * i], q1 = *(const u32x4*)&L.q[br][1][4 * i];
    constexpr float C2 = 0.088388347648318440f * 1.4426950408889634f;
    float sc[16], m = -INFINITY;
#pragma unroll
    for (int t = 0; t < 16; ++t) {
      float d0 = 0, d1 = 0;
#pragma unroll
      for (int k = 0; k < 4; ++k) { d0 = dot2(K[t][k], q0[k], d0); d1 = dot2(K[t][k], q1[k], d1); }
      d0 += dpp<XOR8>(d0); d1 += dpp<XOR8>(d1);
      float d = (i & 8) ? d1 : d0;
      d += dpp<XOR7>(d); d += dpp<XOR2>(d); d += dpp<XOR1>(d);
      sc[t] = d; m = fmaxf(m, d);
    }
    m = max16_32(m) * C2;
    asm volatile("" : "+v"(epoch));
    tag = __builtin_amdgcn_readfirstlane(epoch) + 1u;
    ST(2);
    unsigned msk[4];
#pragma unroll
    for (int k = 0; k < 4; ++k) msk[k] = (((i & 7) >> 1) == k) ? ((i & 1) ? 0xffff0000u : 0x0000ffffu) : 0u;
    float l = 0; f32x4 acc = {0, 0, 0, 0};
#pragma unroll
    for (int t = 0; t < 16; ++t) {
      const float p = __builtin_amdgcn_exp2f(fmaf(sc[t], C2, -m));
      const unsigned pp = cvtpk(p, p);
      l += bflo(pp);
      const u32x4 a = {pp & msk[0], pp & msk[1], pp & msk[2], pp & msk[3]};
      acc = mfma(a, V[t], acc);
    }
    l = fold16(l, l); l = fold32(l, l);
    *(f32x4*)&L.part[sw][g >> 1][4 + 8 * i + 4 * (g & 1)] = acc;
    if ((lane & 55) == 0) { L.part[sw][lane >> 3][0] = m; L.part[sw][lane >> 3][1] = l; }
    ST(3);
  }
  u32x4 wo[4];
  if (wave < 9) {
    for (unsigned spins = 0; __hip_atomic_load(&L.issued, RLX_WG) < 10u && spins < (SPIN_CAP << 3); ++spins) __builtin_amdgcn_s_sleep(2);
    const rsrc_t wr = mkrs((pbr ? woB : woA) + (h * 256 + (s & 7) * 32) * DM + wave * 64, 32 * DM * 2);
    const unsigned woff = ((lane >> 3) * 4 * DM + (lane & 7) * 8) * 2;
#pragma unroll
    for (int t = 0; t < 4; ++t) wo[t] = ldb<AUXW>(wr, woff + t * DM * 2, 0);
  }
  SB();
  asm volatile("s_waitcnt lgkmcnt(0)" :: "s"(tag) : "memory");
  __builtin_amdgcn_s_barrier();
  SB();
  ST(4);
  if (wave < 8) {
    const int br = wave >> 2, hd = (wave >> 1) & 1, d = tid & 127;
    float M, Lsum, o;
    if (br == 0) {
      const float m0 = L.part[0][hd][0], m1 = L.part[1][hd][0];
      M = fmaxf(m0, m1);
      const float e0 = __builtin_amdgcn_exp2f(m0 - M), e1 = __builtin_amdgcn_exp2f(m1 - M);
      Lsum = e0 * L.part[0][hd][1] + e1 * L.part[1][hd][1];
      o = e0 * L.part[0][hd][4 + d] + e1 * L.part[1][hd][4 + d];
    } else {
      float mm[8]; M = -INFINITY;
#pragma unroll
      for (int v = 0; v < 8; ++v) { mm[v] = L.part[2 + v][hd][0]; M = fmaxf(M, mm[v]); }
      Lsum = 0; o = 0;
#pragma unroll
      for (int v = 0; v < 8; ++v) { const float e = __builtin_amdgcn_exp2f(mm[v] - M); Lsum = fmaf(e, L.part[2 + v][hd][1], Lsum); o = fmaf(e, L.part[2 + v][hd][4 + d], o); }
    }
    GAS u64* og = (GAS u64*)(ws + WS_O) + (size_t)h * (16 * 16 * 48);
    GAS u64* dst = og + ((tid >> 5) * 16 + s) * 48;
    stg(dst + (tid & 31), tag, o * __builtin_amdgcn_rcpf(Lsum));
    if ((tid & 31) == 0) stg(dst + 32, tag, M + __builtin_amdgcn_logf(Lsum));
    GAS u64* src = og + (s * 16 + (lane & 15)) * 48;
    u64 go, gl;
    for (unsigned spins = 0;; ++spins) {
      go = ldg(src + 4 * wave + (lane >> 4)); gl = ldg(src + 32);
      if (__all((unsigned)(go >> 32) == tag && (unsigned)(gl >> 32) == tag) || spins > SPIN_CAP) break;
    }
    const float lse = __uint_as_float((unsigned)gl);
    float mx = fmaxf(lse, dpp<XOR8>(lse)); mx = fmaxf(mx, dpp<XOR7>(mx)); mx = fmaxf(mx, dpp<XOR2>(mx)); mx = fmaxf(mx, dpp<XOR1>(mx));
    float e = __builtin_amdgcn_exp2f(lse - mx), a = e * __uint_as_float((unsigned)go);
    e += dpp<XOR8>(e); a += dpp<XOR8>(a); e += dpp<XOR7>(e); a += dpp<XOR7>(a); e += dpp<XOR2>(e); a += dpp<XOR2>(a); e += dpp<XOR1>(e); a += dpp<XOR1>(a);
    if ((lane & 15) == 0) L.o[4 * wave + (lane >> 4)] = a * __builtin_amdgcn_rcpf(e);
  }
  ST(5);
  asm volatile("s_waitcnt lgkmcnt(0)" ::: "memory");
  __builtin_amdgcn_s_barrier();
  SB();
  if (wave < 9) {
    const f32x4 ov = *(const f32x4*)&L.o[(lane >> 3) * 4];
    float acc[8] = {0, 0, 0, 0, 0, 0, 0, 0};
#pragma unroll
    for (int t = 0; t < 4; ++t)
#pragma unroll
      for (int k = 0; k < 4; ++k) { acc[2 * k] = fmaf(ov[t], bflo(wo[t][k]), acc[2 * k]); acc[2 * k + 1] = fmaf(ov[t], bfhi(wo[t][k]), acc[2 * k + 1]); }
    const float y = tr_reduce8(acc, lane);
    GAS u64* yg = (GAS u64*)(ws + WS_Y);
    stg(yg + ((size_t)(pbr * 72 + wave * 8 + (lane & 7)) * 128 + h * 8 + (s & 7)) * 8 + (lane >> 3), tag, y);
  }
  ST(6);
  if (w >= 144) { STDUMP(); return; }
  if (wave < 8) {
    GAS u64* src = (GAS u64*)(ws + WS_Y) + (size_t)w * 1024 + wave * 128 + lane;
    u64 g0, g1;
    for (unsigned spins = 0;; ++spins) {
      g0 = ldg(src); g1 = ldg(src + 64);
      if (__all((unsigned)(g0 >> 32) == tag && (unsigned)(g1 >> 32) == tag) || spins > SPIN_CAP) break;
    }
    float y = __uint_as_float((unsigned)g0) + __uint_as_float((unsigned)g1);
    y += dpp<XOR8>(y); y = fold16(y, y); y = fold32(y, y);
    if (lane < 8) L.fin[wave][lane] = y;
  }
  asm volatile("s_waitcnt lgkmcnt(0)" ::: "memory");
  __builtin_amdgcn_s_barrier();
  SB();
  if (wave == 0) {
    float y = L.fin[lane >> 3][lane & 7];
    y += dpp<XOR8>(y); y = fold16(y, y); y = fold32(y, y);
    const float yn = dpp<XOR1>(y);
    if (lane < 8 && !(lane & 1)) ((GAS unsigned*)out)[w * 4 + (lane >> 1)] = cvtpk(y, yn);
    if (w == 0 && lane == 0) __hip_atomic_store((GAS unsigned*)(ws + WS_EPOCH), tag, RLX_AGENT);
    ST(7);
  }
  STDUMP();
}

__global__ void tinyk(unsigned char* ws) { const unsigned until = __hip_atomic_load((unsigned*)(ws + WS_STAMP + 65536), RLX_AGENT); for (unsigned sp = 0; sp < 1000000; ++sp) { if ((int)((unsigned)__builtin_amdgcn_s_memrealtime() - until) >= 0) break; } }
extern "C" void kernel_launch(void* const* d_in, const int* in_sizes, int n_in, void* d_out, int out_size, void* d_ws, size_t ws_size, hipStream_t stream) {
  fused_decode<<<256, NTHR, 0, stream>>>((const uint16_t*)d_in[0], (const uint16_t*)d_in[1], (const uint16_t*)d_in[2], (const uint16_t*)d_in[3], (const uint16_t*)d_in[4],
                                         (const uint16_t*)d_in[5], (const uint16_t*)d_in[6], (const uint16_t*)d_in[7], (const uint16_t*)d_in[8], (const uint16_t*)d_in[9],
                                         (uint16_t*)d_out, (unsigned char*)d_ws);
  tinyk<<<1, 64, 0, stream>>>((unsigned char*)d_ws);
}
```

```cpp
#include <hip/hip_runtime.h>
#include <hip/hip_bf16.h>
#include <stdint.h>

#define GAS __attribute__((address_space(1)))
#define LAS __attribute__((address_space(3)))
#define RLX_AGENT __ATOMIC_RELAXED, __HIP_MEMORY_SCOPE_AGENT
#define RLX_WG __ATOMIC_RELAXED, __HIP_MEMORY_SCOPE_WORKGROUP
typedef unsigned u32x4 __attribute__((ext_vector_type(4)));
typedef unsigned u32x2 __attribute__((ext_vector_type(2)));
typedef float f32x4 __attribute__((ext_vector_type(4)));
typedef short s16x4 __attribute__((ext_vector_type(4)));
typedef __bf16 bf16x2 __attribute__((ext_vector_type(2)));
typedef __bf16 bf16x8 __attribute__((ext_vector_type(8)));
typedef unsigned long long u64;
typedef __amdgpu_buffer_rsrc_t rsrc_t;

constexpr int DM = 576, NQ = 4096;
constexpr int NWAVE = 11, NTHR = NWAVE * 64, NRED = 36;
constexpr unsigned WS_EPOCH = 0, WS_Q = 4096, WS_O = WS_Q + 16 * 4096, WS_Y = WS_O + 256 * 2048, WS_END = WS_Y + NRED * 16384;
constexpr size_t WS_STAMP = 8u << 20;
constexpr unsigned SPIN_CAP = 1u << 17;

#define AUXKV 2
#define AUXW 2
#define FIRST1 20
#define FIRST2 12
#define PS1 0
#define PS2 0
#define QSLEEP 64
#define PRETOUCH 2
#define WOGATE 0
#define XPOS 7
#define MID 6
#define ABL 0

__device__ __forceinline__ float bflo(unsigned u) { return __builtin_bit_cast(float, u << 16); }
__device__ __forceinline__ float bfhi(unsigned u) { return __builtin_bit_cast(float, u & 0xffff0000u); }
__device__ __forceinline__ unsigned fu(float f) { return __float_as_uint(f); }
__device__ __forceinline__ float uf(unsigned u) { return __uint_as_float(u); }
__device__ __forceinline__ unsigned cvtpk(float lo, float hi) { bf16x2 v = {(__bf16)lo, (__bf16)hi}; return __builtin_bit_cast(unsigned, v); }
__device__ __forceinline__ float dot2(unsigned k, unsigned q, float acc) {
  return __builtin_amdgcn_fdot2_f32_bf16(__builtin_bit_cast(bf16x2, k), __builtin_bit_cast(bf16x2, q), acc, false);
}
template <int CTRL> __device__ __forceinline__ float dpp(float x) {
  return __builtin_bit_cast(float, __builtin_amdgcn_mov_dpp(__builtin_bit_cast(int, x), CTRL, 0xf, 0xf, true));
}
constexpr int XOR1 = 0xB1, XOR2 = 0x4E, XOR7 = 0x141, XOR8 = 0x128;
__device__ __forceinline__ f32x4 mfma(u32x4 a, u32x4 b, f32x4 c) {
  return __builtin_amdgcn_mfma_f32_16x16x32_bf16(__builtin_bit_cast(bf16x8, a), __builtin_bit_cast(bf16x8, b), c, 0, 0, 0);
}
__device__ __forceinline__ float fold32(float a, float b) {
  auto r = __builtin_amdgcn_permlane32_swap(fu(a), fu(b), false, false);
  return uf(r[0]) + uf(r[1]);
}
__device__ __forceinline__ float fold16(float a, float b) {
  auto r = __builtin_amdgcn_permlane16_swap(fu(a), fu(b), false, false);
  return uf(r[0]) + uf(r[1]);
}
__device__ __forceinline__ float max16_32(float x) {
  auto s = __builtin_amdgcn_permlane16_swap(fu(x), fu(x), false, false);
  x = fmaxf(uf(s[0]), uf(s[1]));
  auto t = __builtin_amdgcn_permlane32_swap(fu(x), fu(x), false, false);
  return fmaxf(uf(t[0]), uf(t[1]));
}
__device__ __forceinline__ rsrc_t mkrs(const void* p, unsigned bytes) { return __builtin_amdgcn_make_buffer_rsrc((void*)p, 0, bytes, 0x00020000); }
template <int AUX> __device__ __forceinline__ u32x4 ldb(rsrc_t r, unsigned voff, unsigned soff) { return __builtin_amdgcn_raw_buffer_load_b128(r, voff, soff, AUX); }
constexpr int SC1 = 16;
__device__ __forceinline__ u32x4 ldx(rsrc_t r, unsigned voff, unsigned soff) { asm volatile("" ::: "memory"); return __builtin_amdgcn_raw_buffer_load_b128(r, voff, soff, SC1); }
__device__ __forceinline__ void stx(rsrc_t r, unsigned voff, unsigned soff, u32x4 v) { __builtin_amdgcn_raw_buffer_store_b128(v, r, voff, soff, SC1); }
__device__ __forceinline__ u32x4 seal(unsigned a, unsigned b, unsigned c, unsigned tag) { return u32x4{a, b, c, tag ^ a ^ b ^ c}; }
__device__ __forceinline__ bool valid(u32x4 g, unsigned tag) { return (g[0] ^ g[1] ^ g[2] ^ g[3]) == tag; }
__device__ __forceinline__ u32x4 seal4(float a, float b, float c, float d, unsigned tag) {
  return seal(__builtin_amdgcn_perm(fu(a), fu(d), 0x07060501u), __builtin_amdgcn_perm(fu(b), fu(d), 0x07060502u), __builtin_amdgcn_perm(fu(c), fu(d), 0x07060503u), tag);
}
__device__ __forceinline__ float fourth(u32x4 g) { return uf(__builtin_amdgcn_perm(g[2], __builtin_amdgcn_perm(g[1], g[0], 0x0c04000cu), 0x04020100u)); }
#define SB() __builtin_amdgcn_sched_barrier(0)
#define ST(i)
#define STDUMP()
#define STDECL()
#define STPAR
#define STARG

struct Lds {
  unsigned char wo[9][4096];
  float part[11][2][132];
  unsigned q[2][128];
  unsigned x[144];
  float fin[8][32];
  unsigned short o[32];
  unsigned qready, issued, done;
};

__device__ __forceinline__ void lds_wait(unsigned* p, unsigned n) {
  for (unsigned spins = 0; __hip_atomic_load(p, RLX_WG) < n && spins < (SPIN_CAP << 3); ++spins) __builtin_amdgcn_s_sleep(1);
}

template <bool LATE>
__device__ __forceinline__ unsigned stream_wave(Lds& L, rsrc_t kr, rsrc_t vr, unsigned rot, int wave, int br, int lane, rsrc_t wsr, unsigned pt0, unsigned pt1, unsigned pub STPAR) {
  const int g = lane >> 4, i = lane & 15;
  const unsigned loff = lane * 16;
  u32x4 K[16], V[16];
  unsigned so = rot & 0x7000u;
#define LOAD16(X, R) _Pragma("unroll") for (int t = 0; t < 16; t += 2) { \
    X[t] = ldb<AUXKV>(R, loff, so); SB(); unsigned nx = so + 4096u; asm volatile("" : "+s"(nx)); SB(); \
    X[t + 1] = ldb<AUXKV>(R, loff + 1024, so); SB(); so = nx & 0x7000u; asm volatile("" : "+s"(so)); SB(); }
  LOAD16(K, kr)
  ST(9);
  if (!LATE) {
    LOAD16(V, vr)
    if (lane == 0) __hip_atomic_fetch_add(&L.issued, 1u, RLX_WG);
    ST(1);
  }
  lds_wait(&L.qready, 1u);
  const unsigned tag = __builtin_amdgcn_readfirstlane(__hip_atomic_load(&L.qready, RLX_WG));
  __builtin_amdgcn_fence(__ATOMIC_ACQUIRE, "workgroup");
  const u32x4 q0 = *(const u32x4*)&L.q[br][4 * i], q1 = *(const u32x4*)&L.q[br][64 + 4 * i];
  constexpr float C2 = 0.088388347648318440f * 1.4426950408889634f;
  float sc[16], m = -INFINITY;
#pragma unroll
  for (int t = 0; t < 16; ++t) {
    float d0 = 0, d1 = 0;
#pragma unroll
    for (int k = 0; k < 4; ++k) { d0 = dot2(K[t][k], q0[k], d0); d1 = dot2(K[t][k], q1[k], d1); }
    d0 += dpp<XOR8>(d0); d1 += dpp<XOR8>(d1);
    float d = (i & 8) ? d1 : d0;
    d += dpp<XOR7>(d); d += dpp<XOR2>(d); d += dpp<XOR1>(d);
    sc[t] = d; m = fmaxf(m, d);
  }
  m = max16_32(m) * C2;
  unsigned msk[4];
#pragma unroll
  for (int k = 0; k < 4; ++k) msk[k] = (((i & 7) >> 1) == k) ? ((i & 1) ? 0xffff0000u : 0x0000ffffu) : 0u;
  float l = 0;
  u32x4 a[16];
#pragma unroll
  for (int t = 0; t < 16; ++t) {
    const float p = __builtin_amdgcn_exp2f(fmaf(sc[t], C2, -m));
    const unsigned pp = cvtpk(p, p);
    l += p;
    a[t] = u32x4{pp & msk[0], pp & msk[1], pp & msk[2], pp & msk[3]};
  }
  l = fold16(l, l); l = fold32(l, l);
  if ((lane & 55) == 0) { L.part[wave][lane >> 3][0] = m; L.part[wave][lane >> 3][1] = l; }
  SB();
  ST(2);
  if (!LATE) {
    f32x4 acc = {0, 0, 0, 0};
#pragma unroll
    for (int t = 0; t < 16; ++t) acc = mfma(a[t], V[t], acc);
    *(f32x4*)&L.part[wave][g >> 1][4 + 8 * i + 4 * (g & 1)] = acc;
    __builtin_amdgcn_fence(__ATOMIC_RELEASE, "workgroup");
    if (lane == 0) __hip_atomic_fetch_add(&L.done, 1u, RLX_WG);
    ST(3);
  } else {
#pragma unroll
    for (int t = 0; t < 16; ++t) asm volatile("" : "+v"(a[t]));
    lds_wait(&L.issued, 8u);
    SB();
    unsigned pre0 = 0, pre1 = 0;
    if (PRETOUCH == 1 || PRETOUCH == 2) pre0 = __builtin_amdgcn_raw_buffer_load_b32(wsr, lane * 128 + pt0, 0, SC1);
    if (PRETOUCH == 1) pre1 = __builtin_amdgcn_raw_buffer_load_b32(wsr, lane * 128 + pt1, 0, SC1);
    SB();
    LOAD16(V, vr)
    if (lane == 0) __hip_atomic_fetch_add(&L.issued, 1u, RLX_WG);
    ST(1);
    f32x4 acc = {0, 0, 0, 0};
#pragma unroll
    for (int t = 0; t < MID; ++t) acc = mfma(a[t], V[t], acc);
    asm volatile("" : "+v"(acc));
    SB();
    lds_wait(&L.done, 8u);
    __builtin_amdgcn_fence(__ATOMIC_ACQUIRE, "workgroup");
    const int hd = g >> 1;
    const float* rec = &L.part[br ? 4 : 3][hd][0];
    const float2 own = *(const float2*)&L.part[wave][hd][0];
    float2 r[7]; f32x4 o[7];
#pragma unroll
    for (int u = 0; u < 7; ++u) { r[u] = *(const float2*)(rec + u * 264); o[u] = *(const f32x4*)(rec + u * 264 + 4 + 8 * i + 4 * (g & 1)); }
    float M = fmaxf(own.x, r[0].x);
#pragma unroll
    for (int u = 1; u < 7; ++u) { r[u].x = br ? r[u].x : -INFINITY; M = fmaxf(M, r[u].x); }
    float f = __builtin_amdgcn_exp2f(own.x - M), Ls = f * own.y;
    f32x4 base = {0, 0, 0, 0};
#pragma unroll
    for (int u = 0; u < 7; ++u) { const float e = __builtin_amdgcn_exp2f(r[u].x - M); Ls = fmaf(e, r[u].y, Ls); base += e * o[u]; }
    const float inv = __builtin_amdgcn_rcpf(Ls);
    f *= inv; base *= inv;
    unsigned lse = fu(M + __builtin_amdgcn_logf(Ls)), poff = (4 * hd + (i >> 2)) * 2048 + (4 * (g & 1) + (i & 3)) * 16;
    asm volatile("" : "+v"(f), "+v"(base), "+v"(lse), "+v"(poff));
    SB();
    ST(4);
#pragma unroll
    for (int t = MID; t < 16; ++t) acc = mfma(a[t], V[t], acc);
    acc = acc * f + base;
    if (!(ABL & 2)) stx(wsr, poff, pub, seal(cvtpk(acc[0], acc[1]), cvtpk(acc[2], acc[3]), lse, tag));
    else asm volatile("" :: "v"(acc));
    asm volatile("" :: "v"(pre0), "v"(pre1));
    ST(3);
  }
  return tag;
}

__global__ void __launch_bounds__(NTHR, 3) fused_decode(const uint16_t* xA, const uint16_t* kA, const uint16_t* vA, const uint16_t* wqA, const uint16_t* woA,
                                                         const uint16_t* xB, const uint16_t* kB, const uint16_t* vB, const uint16_t* wqB, const uint16_t* woB,
                                                         uint16_t* out, unsigned char* wsp) {
  __shared__ __attribute__((aligned(16))) Lds L;
  GAS unsigned char* ws = (GAS unsigned char*)wsp;
  const int tid = threadIdx.x, lane = tid & 63, wave = __builtin_amdgcn_readfirstlane(tid >> 6);
  const int w = blockIdx.x, h = (w & 7) * 2 + (w >> 7), s = (w >> 3) & 15;
  const int pbr = s >> 3;
  STDECL();
  ST(0);
  const rsrc_t wsr = mkrs(wsp, WS_END);
  unsigned tag;
  if (tid == 64) { L.qready = 0u; L.issued = 0u; L.done = 0u; }

  if (__builtin_expect(wave == 0, 1)) {
    const int rs = lane >> 3, c = lane & 7;
    const int qbr = (w >> 1) & 1, half = (w >> 2) & 1, j = w >> 3, blk = (j >> 1) * 4 + (j & 1) * 2 + ((j ^ w) & 1);
    const rsrc_t wr = mkrs((qbr ? wqB : wqA) + (half * 288) * NQ + blk * 64, 288 * NQ * 2);
    const unsigned woff = (unsigned)(rs * 36 * NQ + c * 8) * 2;
    u32x4 wv[36], xr;
    unsigned epoch, so = 0;
#pragma unroll
    for (int t = 0; t < 36; ++t) {
      wv[t] = ldb<AUXW>(wr, woff, so); SB();
      so += NQ * 2; asm volatile("" : "+s"(so)); SB();
      if (t == XPOS) {
        xr = ldb<0>(mkrs((qbr ? xB : xA) + half * 288, 576), lane * 16, 0);
        epoch = __hip_atomic_load((GAS unsigned*)(ws + WS_EPOCH), RLX_AGENT); SB();
      }
    }
    __builtin_amdgcn_s_barrier();
    SB();
    ST(8);
    if (lane < 36) *(u32x4*)&L.x[lane * 4] = xr;
    float acc[8] = {0, 0, 0, 0, 0, 0, 0, 0};
#pragma unroll
    for (int t4 = 0; t4 < 9; ++t4) {
      const u32x2 xw = *(const u32x2*)&L.x[rs * 18 + t4 * 2];
#pragma unroll
      for (int u = 0; u < 4; ++u) {
        const int t = t4 * 4 + u;
        const unsigned xl = (u & 1) ? xw[u >> 1] >> 16 : xw[u >> 1] & 0xffffu, xh = (u & 1) ? xw[u >> 1] & 0xffff0000u : xw[u >> 1] << 16;
#pragma unroll
        for (int k = 0; k < 4; ++k) { acc[2 * k] = dot2(wv[t][k], xl, acc[2 * k]); acc[2 * k + 1] = dot2(wv[t][k], xh, acc[2 * k + 1]); }
      }
      SB();
    }
    float b[4];
#pragma unroll
    for (int k = 0; k < 4; ++k) { b[k] = fold32(acc[k], acc[k + 4]); b[k] = fold16(b[k], b[k]); b[k] += dpp<XOR8>(b[k]); }
    asm volatile("" : "+v"(epoch));
    tag = __builtin_amdgcn_readfirstlane(epoch) + 1u;
    tag += tag == 0u;
    if (!(lane & 24)) stx(wsr, (c * 2 + (lane >> 5)) * 16, WS_Q + (blk >> 2) * 4096 + (qbr * 2 + half) * 1024 + (blk & 3) * 256, seal4(b[0], b[1], b[2], b[3], tag));
    if (PRETOUCH == 3) {
      const u32x4 z = {0u, 0u, 0u, 0u};
      stx(wsr, (lane >> 3) * 2048 + (lane & 7) * 16, WS_O + h * 32768 + s * 128, z);
      stx(wsr, (lane >> 3) * 2048 + (lane & 7) * 16, WS_O + h * 32768 + 16384 + s * 128, z);
#pragma unroll
      for (int k = 0; k < 3; ++k) if (k < 2 || lane < 16) stx(wsr, (lane >> 3) * 16384 + (lane & 7) * 16, WS_Y + (pbr * 18 + 8 * k) * 16384 + (h * 8 + (s & 7)) * 128, z);
    }
    ST(1);
    __builtin_amdgcn_s_sleep(QSLEEP);
    u32x4 g[4];
    if (ABL & 1) { for (int i = 0; i < 4; ++i) g[i] = seal4(b[0], b[1], b[2], b[3], tag); asm volatile("s_waitcnt vmcnt(0)" ::: "memory"); } else
    for (unsigned spins = 0;; ++spins) {
      bool ok = true;
#pragma unroll
      for (int i = 0; i < 4; ++i) g[i] = ldx(wsr, lane * 16 + i * 1024, WS_Q + h * 4096);
#pragma unroll
      for (int i = 0; i < 4; ++i) ok &= valid(g[i], tag);
      if (__all(ok) || spins > SPIN_CAP) break;
    }
#pragma unroll
    for (int bb = 0; bb < 2; ++bb) {
      const u32x4 p0 = g[2 * bb], p1 = g[2 * bb + 1];
      *(u32x2*)&L.q[bb][2 * lane] = u32x2{cvtpk(uf(p0[0]) + uf(p1[0]), uf(p0[1]) + uf(p1[1])), cvtpk(uf(p0[2]) + uf(p1[2]), fourth(p0) + fourth(p1))};
    }
    __builtin_amdgcn_fence(__ATOMIC_RELEASE, "workgroup");
    if (lane == 0) __hip_atomic_store(&L.qready, tag, RLX_WG);
    ST(2);
  } else {
    const int br = wave == 1 || wave == 3 ? 0 : 1;
    const int pair = wave < 5 ? 3 : (wave - 5) >> 1, odd = wave < 5 ? (wave - 1) >> 1 : (wave - 5) & 1;
    const size_t off = ((size_t)h * 2048 + pair * 512 + s * 32 + odd * 2) * 1024;
    const rsrc_t kr = mkrs((const char*)(br ? kB : kA) + off, 32 * 1024), vr = mkrs((const char*)(br ? vB : vA) + off, 32 * 1024);
    asm volatile("s_waitcnt lgkmcnt(0)" ::: "memory");
    __builtin_amdgcn_s_barrier();
    SB();
    ST(8);
    const unsigned OOR = 0x40000000u;
    const unsigned pt0 = lane < 8 ? WS_O + (h * 16 + s) * 2048 + br * 1024 : OOR, pt1 = w < NRED ? WS_Y + w * 16384 + br * 8192 : OOR;
    if (wave < 3) tag = stream_wave<true>(L, kr, vr, (s + h) * 4096, wave, br, lane, wsr, pt0, pt1, WS_O + h * 32768 + br * 16384 + s * 128 STARG);
    else tag = stream_wave<false>(L, kr, vr, (s + h) * 4096, wave, br, lane, wsr, 0, 0, 0 STARG);
  }
  const int pw = wave == 0 ? 8 : wave - 3;
  u32x4 wf[4];
  if (pw >= 0) {
    lds_wait(&L.issued, 10u);
    const rsrc_t wr = mkrs((pbr ? woB : woA) + (h * 256 + (s & 7) * 32) * DM + pw * 64, 32 * DM * 2);
    const unsigned woff = ((lane >> 3) * DM + (lane & 7) * 8) * 2;
    LAS unsigned char* dst = (LAS unsigned char*)&L.wo[pw][0];
#pragma unroll
    for (int t = 0; t < 4; ++t) __builtin_amdgcn_raw_ptr_buffer_load_lds(wr, dst + t * 1024, 16, woff, t * 8 * DM * 2, 0, AUXW);
    asm volatile("s_waitcnt vmcnt(0)" ::: "memory");
    LAS unsigned char* src = dst + (lane >> 4) * 1024 + ((lane >> 2) & 3) * 128 + (lane & 3) * 8;
#pragma unroll
    for (int b = 0; b < 4; ++b) {
      const u32x2 lo = __builtin_bit_cast(u32x2, __builtin_amdgcn_ds_read_tr16_b64_v4i16((LAS s16x4*)(src + b * 32)));
      const u32x2 hi = __builtin_bit_cast(u32x2, __builtin_amdgcn_ds_read_tr16_b64_v4i16((LAS s16x4*)(src + b * 32 + 512)));
      wf[b] = u32x4{lo[0], lo[1], hi[0], hi[1]};
    }
    asm volatile("" : "+v"(wf[0]), "+v"(wf[1]), "+v"(wf[2]), "+v"(wf[3]));
    ST(5);
  } else if (ABL & 2) {
  } else if (wave == 2) {
    if (PRETOUCH == 2 && w < NRED) { const unsigned pre = __builtin_amdgcn_raw_buffer_load_b32(wsr, lane * 128, WS_Y + w * 16384, SC1) ^ __builtin_amdgcn_raw_buffer_load_b32(wsr, lane * 128 + 8192, WS_Y + w * 16384, SC1); asm volatile("" :: "v"(pre)); }
  } else {
    __builtin_amdgcn_s_sleep(FIRST1);
    u32x4 g0, g1;
    for (unsigned spins = 0;; ++spins) {
      g0 = ldx(wsr, lane * 16, WS_O + (h * 16 + s) * 2048); g1 = ldx(wsr, lane * 16 + 1024, WS_O + (h * 16 + s) * 2048);
      if (__all(valid(g0, tag) && valid(g1, tag)) || spins > SPIN_CAP) break;
      if (PS1) __builtin_amdgcn_s_sleep(PS1);
    }
    ST(10);
    float mx = fmaxf(uf(g0[2]), uf(g1[2]));
    mx = max16_32(fmaxf(mx, dpp<XOR8>(mx)));
    const float t0 = __builtin_amdgcn_exp2f(uf(g0[2]) - mx), t1 = __builtin_amdgcn_exp2f(uf(g1[2]) - mx);
    float e = t0 + t1;
    e += dpp<XOR8>(e); e = fold16(e, e); e = fold32(e, e);
    const float a0 = fmaf(t0, bflo(g0[0]), t1 * bflo(g1[0])), a1 = fmaf(t0, bfhi(g0[0]), t1 * bfhi(g1[0]));
    const float a2 = fmaf(t0, bflo(g0[1]), t1 * bflo(g1[1])), a3 = fmaf(t0, bfhi(g0[1]), t1 * bfhi(g1[1]));
    float a = fold16(fold32(a0, a2), fold32(a1, a3));
    a += dpp<XOR8>(a);
    if (!(lane & 8)) L.o[8 * (lane & 3) + (lane & 4) + (lane >> 4)] = (unsigned short)cvtpk(a * __builtin_amdgcn_rcpf(e), 0.f);
    ST(5);
  }
  if (ABL & 2) { STDUMP(); return; }
  asm volatile("s_waitcnt lgkmcnt(0)" ::: "memory");
  __builtin_amdgcn_s_barrier();
  SB();
  ST(11);
  if (pw >= 0) {
    const u32x4 ov = *(const u32x4*)&L.o[(lane >> 4) * 8];
    const f32x4 z = {0, 0, 0, 0};
    const float y0 = mfma(ov, wf[0], z)[0], y1 = mfma(ov, wf[1], z)[0], y2 = mfma(ov, wf[2], z)[0], y3 = mfma(ov, wf[3], z)[0];
    if (lane < 16) stx(wsr, (lane >> 3) * 16384 + (lane & 7) * 16, WS_Y + (pbr * 18 + pw * 2) * 16384 + (h * 8 + (s & 7)) * 128, seal4(y0, y1, y2, y3, tag));
  }
  ST(6);
  if (w >= NRED) { STDUMP(); return; }
  if (wave < 8) {
    __builtin_amdgcn_s_sleep(FIRST2);
    u32x4 g0, g1;
    for (unsigned spins = 0;; ++spins) {
      g0 = ldx(wsr, lane * 16, WS_Y + w * 16384 + wave * 2048); g1 = ldx(wsr, lane * 16 + 1024, WS_Y + w * 16384 + wave * 2048);
      if (__all(valid(g0, tag) && valid(g1, tag)) || spins > SPIN_CAP) break;
      if (PS2) __builtin_amdgcn_s_sleep(PS2);
    }
    float y = fold16(fold32(uf(g0[0]) + uf(g1[0]), uf(g0[2]) + uf(g1[2])), fold32(uf(g0[1]) + uf(g1[1]), fourth(g0) + fourth(g1)));
    y += dpp<XOR8>(y);
    if (!(lane & 8)) L.fin[wave][(lane >> 4) * 8 + (lane & 7)] = y;
  }
  asm volatile("s_waitcnt lgkmcnt(0)" ::: "memory");
  __builtin_amdgcn_s_barrier();
  SB();
  if (wave == 0) {
    const float* f = &L.fin[(lane >> 5) * 4][lane & 31];
    float y = (f[0] + f[32]) + (f[64] + f[96]);
    y = fold32(y, y);
    const float yn = dpp<XOR1>(y);
    if (lane < 32 && !(lane & 1)) ((GAS unsigned*)out)[(w >> 1) * 32 + (w & 1) * 4 + (lane >> 3) * 8 + ((lane & 7) >> 1)] = cvtpk(y, yn);
    if (w == 0 && lane == 0) __hip_atomic_store((GAS unsigned*)(ws + WS_EPOCH), tag, RLX_AGENT);
    ST(7);
  }
  STDUMP();
}

extern "C" void kernel_launch(void* const* d_in, const int* in_sizes, int n_in, void* d_out, int out_size, void* d_ws, size_t ws_size, hipStream_t stream) {
  fused_decode<<<256, NTHR, 0, stream>>>((const uint16_t*)d_in[0], (const uint16_t*)d_in[1], (const uint16_t*)d_in[2], (const uint16_t*)d_in[3], (const uint16_t*)d_in[4],
                                         (const uint16_t*)d_in[5], (const uint16_t*)d_in[6], (const uint16_t*)d_in[7], (const uint16_t*)d_in[8], (const uint16_t*)d_in[9],
                                         (uint16_t*)d_out, (unsigned char*)d_ws);
}
```

```cpp
#define STAMP
#define PM1 0
#define PM2 0
#define PW1 1
#define PK1 10
#define PW2 8
#define PK2 6
#define PSC 8
#include <hip/hip_runtime.h>
#include <hip/hip_bf16.h>
#include <stdint.h>

#define GAS __attribute__((address_space(1)))
#define LAS __attribute__((address_space(3)))
#define RLX_AGENT __ATOMIC_RELAXED, __HIP_MEMORY_SCOPE_AGENT
#define RLX_WG __ATOMIC_RELAXED, __HIP_MEMORY_SCOPE_WORKGROUP
typedef unsigned u32x4 __attribute__((ext_vector_type(4)));
typedef unsigned u32x2 __attribute__((ext_vector_type(2)));
typedef float f32x4 __attribute__((ext_vector_type(4)));
typedef short s16x4 __attribute__((ext_vector_type(4)));
typedef __bf16 bf16x2 __attribute__((ext_vector_type(2)));
typedef __bf16 bf16x8 __attribute__((ext_vector_type(8)));
typedef unsigned long long u64;
typedef __amdgpu_buffer_rsrc_t rsrc_t;

constexpr int DM = 576, NQ = 4096;
constexpr int NWAVE = 11, NTHR = NWAVE * 64, NRED = 36;
constexpr unsigned WS_EPOCH = 0, WS_Q = 4096, WS_O = WS_Q + 16 * 4096, WS_Y = WS_O + 256 * 2048, WS_END = WS_Y + NRED * 16384;
constexpr size_t WS_STAMP = 8u << 20;
constexpr unsigned SPIN_CAP = 1u << 17;

#define AUXKV 2
#define AUXW 2
#define FIRST1 20
#define FIRST2 12
#define PS1 0
#define PS2 0
#define QSLEEP 64
#define PRETOUCH 2
#define WOGATE 0
#define XPOS 7
#define MID 6
#define ABL 0

__device__ __forceinline__ float bflo(unsigned u) { return __builtin_bit_cast(float, u << 16); }
__device__ __forceinline__ float bfhi(unsigned u) { return __builtin_bit_cast(float, u & 0xffff0000u); }
__device__ __forceinline__ unsigned fu(float f) { return __float_as_uint(f); }
__device__ __forceinline__ float uf(unsigned u) { return __uint_as_float(u); }
__device__ __forceinline__ unsigned cvtpk(float lo, float hi) { bf16x2 v = {(__bf16)lo, (__bf16)hi}; return __builtin_bit_cast(unsigned, v); }
__device__ __forceinline__ float dot2(unsigned k, unsigned q, float acc) {
  return __builtin_amdgcn_fdot2_f32_bf16(__builtin_bit_cast(bf16x2, k), __builtin_bit_cast(bf16x2, q), acc, false);
}
template <int CTRL> __device__ __forceinline__ float dpp(float x) {
  return __builtin_bit_cast(float, __builtin_amdgcn_mov_dpp(__builtin_bit_cast(int, x), CTRL, 0xf, 0xf, true));
}
constexpr int XOR1 = 0xB1, XOR2 = 0x4E, XOR7 = 0x141, XOR8 = 0x128;
__device__ __forceinline__ f32x4 mfma(u32x4 a, u32x4 b, f32x4 c) {
  return __builtin_amdgcn_mfma_f32_16x16x32_bf16(__builtin_bit_cast(bf16x8, a), __builtin_bit_cast(bf16x8, b), c, 0, 0, 0);
}
__device__ __forceinline__ float fold32(float a, float b) {
  auto r = __builtin_amdgcn_permlane32_swap(fu(a), fu(b), false, false);
  return uf(r[0]) + uf(r[1]);
}
__device__ __forceinline__ float fold16(float a, float b) {
  auto r = __builtin_amdgcn_permlane16_swap(fu(a), fu(b), false, false);
  return uf(r[0]) + uf(r[1]);
}
__device__ __forceinline__ float max16_32(float x) {
  auto s = __builtin_amdgcn_permlane16_swap(fu(x), fu(x), false, false);
  x = fmaxf(uf(s[0]), uf(s[1]));
  auto t = __builtin_amdgcn_permlane32_swap(fu(x), fu(x), false, false);
  return fmaxf(uf(t[0]), uf(t[1]));
}
__device__ __forceinline__ rsrc_t mkrs(const void* p, unsigned bytes) { return __builtin_amdgcn_make_buffer_rsrc((void*)p, 0, bytes, 0x00020000); }
template <int AUX> __device__ __forceinline__ u32x4 ldb(rsrc_t r, unsigned voff, unsigned soff) { return __builtin_amdgcn_raw_buffer_load_b128(r, voff, soff, AUX); }
constexpr int SC1 = 16;
__device__ __forceinline__ u32x4 ldx(rsrc_t r, unsigned voff, unsigned soff) { asm volatile("" ::: "memory"); return __builtin_amdgcn_raw_buffer_load_b128(r, voff, soff, SC1); }
__device__ __forceinline__ void stx(rsrc_t r, unsigned voff, unsigned soff, u32x4 v) { __builtin_amdgcn_raw_buffer_store_b128(v, r, voff, soff, SC1); }
__device__ __forceinline__ u32x4 seal(unsigned a, unsigned b, unsigned c, unsigned tag) { return u32x4{a, b, c, tag ^ a ^ b ^ c}; }
__device__ __forceinline__ bool valid(u32x4 g, unsigned tag) { return (g[0] ^ g[1] ^ g[2] ^ g[3]) == tag; }
__device__ __forceinline__ u32x4 seal4(float a, float b, float c, float d, unsigned tag) {
  return seal(__builtin_amdgcn_perm(fu(a), fu(d), 0x07060501u), __builtin_amdgcn_perm(fu(b), fu(d), 0x07060502u), __builtin_amdgcn_perm(fu(c), fu(d), 0x07060503u), tag);
}
__device__ __forceinline__ float fourth(u32x4 g) { return uf(__builtin_amdgcn_perm(g[2], __builtin_amdgcn_perm(g[1], g[0], 0x0c04000cu), 0x04020100u)); }
#define SB() __builtin_amdgcn_sched_barrier(0)
#define ST(i) do { SB(); u64 t_; asm volatile("s_memrealtime %0\n\ts_waitcnt lgkmcnt(0)" : "=s"(t_) :: "memory"); stp[i] = (unsigned)t_; SB(); } while (0)
#define PAGG(M, mn, mx, sm, c) (M == 0 ? mx : M == 1 ? sm / max(c, 1) : mn)
#define STDUMP() do { GAS u64* pg = (GAS u64*)(ws + WS_STAMP); \
  if (lane == 0) { if (wave == 0) __hip_atomic_store(pg + blockIdx.x, ((u64)tag << 32) | stp[0], RLX_AGENT); \
    if (wave == PW1) __hip_atomic_store(pg + 256 + blockIdx.x, ((u64)tag << 32) | stp[PK1], RLX_AGENT); \
    if (wave == PW2) __hip_atomic_store(pg + 512 + blockIdx.x, ((u64)tag << 32) | stp[PK2], RLX_AGENT); } \
  if (blockIdx.x == 0 && wave == 0) { u64 a[12]; \
    for (unsigned sp = 0; sp < SPIN_CAP; ++sp) { bool ok = true; for (int i_ = 0; i_ < 12; ++i_) { a[i_] = __hip_atomic_load(pg + lane + 64 * i_, RLX_AGENT); ok &= (unsigned)(a[i_] >> 32) == tag; } if (__all(ok)) break; } \
    int mn[3], mx[3], sm[3], cn[3]; \
    for (int q_ = 0; q_ < 3; ++q_) { mn[q_] = 0x7fffffff; mx[q_] = -0x7fffffff; sm[q_] = 0; cn[q_] = 0; \
      for (int i_ = 0; i_ < 4; ++i_) { const unsigned v_ = (unsigned)a[q_ * 4 + i_]; if (v_) { const int d_ = (int)(v_ - stp[0]); mn[q_] = min(mn[q_], d_); mx[q_] = max(mx[q_], d_); sm[q_] += d_; cn[q_]++; } } \
      for (int o_ = 1; o_ < 64; o_ <<= 1) { mn[q_] = min(mn[q_], __shfl_xor(mn[q_], o_)); mx[q_] = max(mx[q_], __shfl_xor(mx[q_], o_)); sm[q_] += __shfl_xor(sm[q_], o_); cn[q_] += __shfl_xor(cn[q_], o_); } } \
    const int X = PAGG(PM2, mn[2], mx[2], sm[2], cn[2]) - PAGG(PM1, mn[1], mx[1], sm[1], cn[1]); \
    const unsigned until = stp[0] + mn[0] + 4000 + PSC * X; \
    for (unsigned sp = 0; sp < 1000000; ++sp) { if ((int)((unsigned)__builtin_amdgcn_s_memrealtime() - until) >= 0) break; } } } while (0)
#define STDECL() unsigned stp[12] = {0, 0, 0, 0, 0, 0, 0, 0, 0, 0, 0, 0}
#define STPAR , unsigned (&stp)[12]
#define STARG , stp

struct Lds {
  unsigned char wo[9][4096];
  float part[11][2][132];
  unsigned q[2][128];
  unsigned x[144];
  float fin[8][32];
  unsigned short o[32];
  unsigned qready, issued, done;
};

__device__ __forceinline__ void lds_wait(unsigned* p, unsigned n) {
  for (unsigned spins = 0; __hip_atomic_load(p, RLX_WG) < n && spins < (SPIN_CAP << 3); ++spins) __builtin_amdgcn_s_sleep(1);
}

template <bool LATE>
__device__ __forceinline__ unsigned stream_wave(Lds& L, rsrc_t kr, rsrc_t vr, unsigned rot, int wave, int br, int lane, rsrc_t wsr, unsigned pt0, unsigned pt1, unsigned pub STPAR) {
  const int g = lane >> 4, i = lane & 15;
  const unsigned loff = lane * 16;
  u32x4 K[16], V[16];
  unsigned so = rot & 0x7000u;
#define LOAD16(X, R) _Pragma("unroll") for (int t = 0; t < 16; t += 2) { \
    X[t] = ldb<AUXKV>(R, loff, so); SB(); unsigned nx = so + 4096u; asm volatile("" : "+s"(nx)); SB(); \
    X[t + 1] = ldb<AUXKV>(R, loff + 1024, so); SB(); so = nx & 0x7000u; asm volatile("" : "+s"(so)); SB(); }
  LOAD16(K, kr)
  ST(9);
  if (!LATE) {
    LOAD16(V, vr)
    if (lane == 0) __hip_atomic_fetch_add(&L.issued, 1u, RLX_WG);
    ST(1);
  }
  lds_wait(&L.qready, 1u);
  const unsigned tag = __builtin_amdgcn_readfirstlane(__hip_atomic_load(&L.qready, RLX_WG));
  __builtin_amdgcn_fence(__ATOMIC_ACQUIRE, "workgroup");
  const u32x4 q0 = *(const u32x4*)&L.q[br][4 * i], q1 = *(const u32x4*)&L.q[br][64 + 4 * i];
  constexpr float C2 = 0.088388347648318440f * 1.4426950408889634f;
  float sc[16], m = -INFINITY;
#pragma unroll
  for (int t = 0; t < 16; ++t) {
    float d0 = 0, d1 = 0;
#pragma unroll
    for (int k = 0; k < 4; ++k) { d0 = dot2(K[t][k], q0[k], d0); d1 = dot2(K[t][k], q1[k], d1); }
    d0 += dpp<XOR8>(d0); d1 += dpp<XOR8>(d1);
    float d = (i & 8) ? d1 : d0;
    d += dpp<XOR7>(d); d += dpp<XOR2>(d); d += dpp<XOR1>(d);
    sc[t] = d; m = fmaxf(m, d);
  }
  m = max16_32(m) * C2;
  unsigned msk[4];
#pragma unroll
  for (int k = 0; k < 4; ++k) msk[k] = (((i & 7) >> 1) == k) ? ((i & 1) ? 0xffff0000u : 0x0000ffffu) : 0u;
  float l = 0;
  u32x4 a[16];
#pragma unroll
  for (int t = 0; t < 16; ++t) {
    const float p = __builtin_amdgcn_exp2f(fmaf(sc[t], C2, -m));
    const unsigned pp = cvtpk(p, p);
    l += p;
    a[t] = u32x4{pp & msk[0], pp & msk[1], pp & msk[2], pp & msk[3]};
  }
  l = fold16(l, l); l = fold32(l, l);
  if ((lane & 55) == 0) { L.part[wave][lane >> 3][0] = m; L.part[wave][lane >> 3][1] = l; }
  SB();
  ST(2);
  if (!LATE) {
    f32x4 acc = {0, 0, 0, 0};
#pragma unroll
    for (int t = 0; t < 16; ++t) acc = mfma(a[t], V[t], acc);
    *(f32x4*)&L.part[wave][g >> 1][4 + 8 * i + 4 * (g & 1)] = acc;
    __builtin_amdgcn_fence(__ATOMIC_RELEASE, "workgroup");
    if (lane == 0) __hip_atomic_fetch_add(&L.done, 1u, RLX_WG);
    ST(3);
  } else {
#pragma unroll
    for (int t = 0; t < 16; ++t) asm volatile("" : "+v"(a[t]));
    lds_wait(&L.issued, 8u);
    SB();
    unsigned pre0 = 0, pre1 = 0;
    if (PRETOUCH == 1 || PRETOUCH == 2) pre0 = __builtin_amdgcn_raw_buffer_load_b32(wsr, lane * 128 + pt0, 0, SC1);
    if (PRETOUCH == 1) pre1 = __builtin_amdgcn_raw_buffer_load_b32(wsr, lane * 128 + pt1, 0, SC1);
    SB();
    LOAD16(V, vr)
    if (lane == 0) __hip_atomic_fetch_add(&L.issued, 1u, RLX_WG);
    ST(1);
    f32x4 acc = {0, 0, 0, 0};
#pragma unroll
    for (int t = 0; t < MID; ++t) acc = mfma(a[t], V[t], acc);
    asm volatile("" : "+v"(acc));
    SB();
    lds_wait(&L.done, 8u);
    __builtin_amdgcn_fence(__ATOMIC_ACQUIRE, "workgroup");
    const int hd = g >> 1;
    const float* rec = &L.part[br ? 4 : 3][hd][0];
    const float2 own = *(const float2*)&L.part[wave][hd][0];
    float2 r[7]; f32x4 o[7];
#pragma unroll
    for (int u = 0; u < 7; ++u) { r[u] = *(const float2*)(rec + u * 264); o[u] = *(const f32x4*)(rec + u * 264 + 4 + 8 * i + 4 * (g & 1)); }
    float M = fmaxf(own.x, r[0].x);
#pragma unroll
    for (int u = 1; u < 7; ++u) { r[u].x = br ? r[u].x : -INFINITY; M = fmaxf(M, r[u].x); }
    float f = __builtin_amdgcn_exp2f(own.x - M), Ls = f * own.y;
    f32x4 base = {0, 0, 0, 0};
#pragma unroll
    for (int u = 0; u < 7; ++u) { const float e = __builtin_amdgcn_exp2f(r[u].x - M); Ls = fmaf(e, r[u].y, Ls); base += e * o[u]; }
    const float inv = __builtin_amdgcn_rcpf(Ls);
    f *= inv; base *= inv;
    unsigned lse = fu(M + __builtin_amdgcn_logf(Ls)), poff = (4 * hd + (i >> 2)) * 2048 + (4 * (g & 1) + (i & 3)) * 16;
    asm volatile("" : "+v"(f), "+v"(base), "+v"(lse), "+v"(poff));
    SB();
    ST(4);
#pragma unroll
    for (int t = MID; t < 16; ++t) acc = mfma(a[t], V[t], acc);
    acc = acc * f + base;
    if (!(ABL & 2)) stx(wsr, poff, pub, seal(cvtpk(acc[0], acc[1]), cvtpk(acc[2], acc[3]), lse, tag));
    else asm volatile("" :: "v"(acc));
    asm volatile("" :: "v"(pre0), "v"(pre1));
    ST(3);
  }
  return tag;
}

__global__ void __launch_bounds__(NTHR, 3) fused_decode(const uint16_t* xA, const uint16_t* kA, const uint16_t* vA, const uint16_t* wqA, const uint16_t* woA,
                                                         const uint16_t* xB, const uint16_t* kB, const uint16_t* vB, const uint16_t* wqB, const uint16_t* woB,
                                                         uint16_t* out, unsigned char* wsp) {
  __shared__ __attribute__((aligned(16))) Lds L;
  GAS unsigned char* ws = (GAS unsigned char*)wsp;
  const int tid = threadIdx.x, lane = tid & 63, wave = __builtin_amdgcn_readfirstlane(tid >> 6);
  const int w = blockIdx.x, h = (w & 7) * 2 + (w >> 7), s = (w >> 3) & 15;
  const int pbr = s >> 3;
  STDECL();
  ST(0);
  const rsrc_t wsr = mkrs(wsp, WS_END);
  unsigned tag;
  if (tid == 64) { L.qready = 0u; L.issued = 0u; L.done = 0u; }

  if (__builtin_expect(wave == 0, 1)) {
    const int rs = lane >> 3, c = lane & 7;
    const int qbr = (w >> 1) & 1, half = (w >> 2) & 1, j = w >> 3, blk = (j >> 1) * 4 + (j & 1) * 2 + ((j ^ w) & 1);
    const rsrc_t wr = mkrs((qbr ? wqB : wqA) + (half * 288) * NQ + blk * 64, 288 * NQ * 2);
    const unsigned woff = (unsigned)(rs * 36 * NQ + c * 8) * 2;
    u32x4 wv[36], xr;
    unsigned epoch, so = 0;
#pragma unroll
    for (int t = 0; t < 36; ++t) {
      wv[t] = ldb<AUXW>(wr, woff, so); SB();
      so += NQ * 2; asm volatile("" : "+s"(so)); SB();
      if (t == XPOS) {
        xr = ldb<0>(mkrs((qbr ? xB : xA) + half * 288, 576), lane * 16, 0);
        epoch = __hip_atomic_load((GAS unsigned*)(ws + WS_EPOCH), RLX_AGENT); SB();
      }
    }
    __builtin_amdgcn_s_barrier();
    SB();
    ST(8);
    if (lane < 36) *(u32x4*)&L.x[lane * 4] = xr;
    float acc[8] = {0, 0, 0, 0, 0, 0, 0, 0};
#pragma unroll
    for (int t4 = 0; t4 < 9; ++t4) {
      const u32x2 xw = *(const u32x2*)&L.x[rs * 18 + t4 * 2];
#pragma unroll
      for (int u = 0; u < 4; ++u) {
        const int t = t4 * 4 + u;
        const unsigned xl = (u & 1) ? xw[u >> 1] >> 16 : xw[u >> 1] & 0xffffu, xh = (u & 1) ? xw[u >> 1] & 0xffff0000u : xw[u >> 1] << 16;
#pragma unroll
        for (int k = 0; k < 4; ++k) { acc[2 * k] = dot2(wv[t][k], xl, acc[2 * k]); acc[2 * k + 1] = dot2(wv[t][k], xh, acc[2 * k + 1]); }
      }
      SB();
    }
    float b[4];
#pragma unroll
    for (int k = 0; k < 4; ++k) { b[k] = fold32(acc[k], acc[k + 4]); b[k] = fold16(b[k], b[k]); b[k] += dpp<XOR8>(b[k]); }
    asm volatile("" : "+v"(epoch));
    tag = __builtin_amdgcn_readfirstlane(epoch) + 1u;
    tag += tag == 0u;
    if (!(lane & 24)) stx(wsr, (c * 2 + (lane >> 5)) * 16, WS_Q + (blk >> 2) * 4096 + (qbr * 2 + half) * 1024 + (blk & 3) * 256, seal4(b[0], b[1], b[2], b[3], tag));
    if (PRETOUCH == 3) {
      const u32x4 z = {0u, 0u, 0u, 0u};
      stx(wsr, (lane >> 3) * 2048 + (lane & 7) * 16, WS_O + h * 32768 + s * 128, z);
      stx(wsr, (lane >> 3) * 2048 + (lane & 7) * 16, WS_O + h * 32768 + 16384 + s * 128, z);
#pragma unroll
      for (int k = 0; k < 3; ++k) if (k < 2 || lane < 16) stx(wsr, (lane >> 3) * 16384 + (lane & 7) * 16, WS_Y + (pbr * 18 + 8 * k) * 16384 + (h * 8 + (s & 7)) * 128, z);
    }
    ST(1);
    __builtin_amdgcn_s_sleep(QSLEEP);
    u32x4 g[4];
    if (ABL & 1) { for (int i = 0; i < 4; ++i) g[i] = seal4(b[0], b[1], b[2], b[3], tag); asm volatile("s_waitcnt vmcnt(0)" ::: "memory"); } else
    for (unsigned spins = 0;; ++spins) {
      bool ok = true;
#pragma unroll
      for (int i = 0; i < 4; ++i) g[i] = ldx(wsr, lane * 16 + i * 1024, WS_Q + h * 4096);
#pragma unroll
      for (int i = 0; i < 4; ++i) ok &= valid(g[i], tag);
      if (__all(ok) || spins > SPIN_CAP) break;
    }
#pragma unroll
    for (int bb = 0; bb < 2; ++bb) {
      const u32x4 p0 = g[2 * bb], p1 = g[2 * bb + 1];
      *(u32x2*)&L.q[bb][2 * lane] = u32x2{cvtpk(uf(p0[0]) + uf(p1[0]), uf(p0[1]) + uf(p1[1])), cvtpk(uf(p0[2]) + uf(p1[2]), fourth(p0) + fourth(p1))};
    }
    __builtin_amdgcn_fence(__ATOMIC_RELEASE, "workgroup");
    if (lane == 0) __hip_atomic_store(&L.qready, tag, RLX_WG);
    ST(2);
  } else {
    const int br = wave == 1 || wave == 3 ? 0 : 1;
    const int pair = wave < 5 ? 3 : (wave - 5) >> 1, odd = wave < 5 ? (wave - 1) >> 1 : (wave - 5) & 1;
    const size_t off = ((size_t)h * 2048 + pair * 512 + s * 32 + odd * 2) * 1024;
    const rsrc_t kr = mkrs((const char*)(br ? kB : kA) + off, 32 * 1024), vr = mkrs((const char*)(br ? vB : vA) + off, 32 * 1024);
    asm volatile("s_waitcnt lgkmcnt(0)" ::: "memory");
    __builtin_amdgcn_s_barrier();
    SB();
    ST(8);
    const unsigned OOR = 0x40000000u;
    const unsigned pt0 = lane < 8 ? WS_O + (h * 16 + s) * 2048 + br * 1024 : OOR, pt1 = w < NRED ? WS_Y + w * 16384 + br * 8192 : OOR;
    if (wave < 3) tag = stream_wave<true>(L, kr, vr, (s + h) * 4096, wave, br, lane, wsr, pt0, pt1, WS_O + h * 32768 + br * 16384 + s * 128 STARG);
    else tag = stream_wave<false>(L, kr, vr, (s + h) * 4096, wave, br, lane, wsr, 0, 0, 0 STARG);
  }
  const int pw = wave == 0 ? 8 : wave - 3;
  u32x4 wf[4];
  if (pw >= 0) {
    lds_wait(&L.issued, 10u);
    const rsrc_t wr = mkrs((pbr ? woB : woA) + (h * 256 + (s & 7) * 32) * DM + pw * 64, 32 * DM * 2);
    const unsigned woff = ((lane >> 3) * DM + (lane & 7) * 8) * 2;
    LAS unsigned char* dst = (LAS unsigned char*)&L.wo[pw][0];
#pragma unroll
    for (int t = 0; t < 4; ++t) __builtin_amdgcn_raw_ptr_buffer_load_lds(wr, dst + t * 1024, 16, woff, t * 8 * DM * 2, 0, AUXW);
    asm volatile("s_waitcnt vmcnt(0)" ::: "memory");
    LAS unsigned char* src = dst + (lane >> 4) * 1024 + ((lane >> 2) & 3) * 128 + (lane & 3) * 8;
#pragma unroll
    for (int b = 0; b < 4; ++b) {
      const u32x2 lo = __builtin_bit_cast(u32x2, __builtin_amdgcn_ds_read_tr16_b64_v4i16((LAS s16x4*)(src + b * 32)));
      const u32x2 hi = __builtin_bit_cast(u32x2, __builtin_amdgcn_ds_read_tr16_b64_v4i16((LAS s16x4*)(src + b * 32 + 512)));
      wf[b] = u32x4{lo[0], lo[1], hi[0], hi[1]};
    }
    asm volatile("" : "+v"(wf[0]), "+v"(wf[1]), "+v"(wf[2]), "+v"(wf[3]));
    ST(5);
  } else if (ABL & 2) {
  } else if (wave == 2) {
    if (PRETOUCH == 2 && w < NRED) { const unsigned pre = __builtin_amdgcn_raw_buffer_load_b32(wsr, lane * 128, WS_Y + w * 16384, SC1) ^ __builtin_amdgcn_raw_buffer_load_b32(wsr, lane * 128 + 8192, WS_Y + w * 16384, SC1); asm volatile("" :: "v"(pre)); }
  } else {
    __builtin_amdgcn_s_sleep(FIRST1);
    u32x4 g0, g1;
    for (unsigned spins = 0;; ++spins) {
      g0 = ldx(wsr, lane * 16, WS_O + (h * 16 + s) * 2048); g1 = ldx(wsr, lane * 16 + 1024, WS_O + (h * 16 + s) * 2048);
      if (__all(valid(g0, tag) && valid(g1, tag)) || spins > SPIN_CAP) break;
      if (PS1) __builtin_amdgcn_s_sleep(PS1);
    }
    ST(10);
    float mx = fmaxf(uf(g0[2]), uf(g1[2]));
    mx = max16_32(fmaxf(mx, dpp<XOR8>(mx)));
    const float t0 = __builtin_amdgcn_exp2f(uf(g0[2]) - mx), t1 = __builtin_amdgcn_exp2f(uf(g1[2]) - mx);
    float e = t0 + t1;
    e += dpp<XOR8>(e); e = fold16(e, e); e = fold32(e, e);
    const float a0 = fmaf(t0, bflo(g0[0]), t1 * bflo(g1[0])), a1 = fmaf(t0, bfhi(g0[0]), t1 * bfhi(g1[0]));
    const float a2 = fmaf(t0, bflo(g0[1]), t1 * bflo(g1[1])), a3 = fmaf(t0, bfhi(g0[1]), t1 * bfhi(g1[1]));
    float a = fold16(fold32(a0, a2), fold32(a1, a3));
    a += dpp<XOR8>(a);
    if (!(lane & 8)) L.o[8 * (lane & 3) + (lane & 4) + (lane >> 4)] = (unsigned short)cvtpk(a * __builtin_amdgcn_rcpf(e), 0.f);
    ST(5);
  }
  if (ABL & 2) { STDUMP(); return; }
  asm volatile("s_waitcnt lgkmcnt(0)" ::: "memory");
  __builtin_amdgcn_s_barrier();
  SB();
  ST(11);
  if (pw >= 0) {
    const u32x4 ov = *(const u32x4*)&L.o[(lane >> 4) * 8];
    const f32x4 z = {0, 0, 0, 0};
    const float y0 = mfma(ov, wf[0], z)[0], y1 = mfma(ov, wf[1], z)[0], y2 = mfma(ov, wf[2], z)[0], y3 = mfma(ov, wf[3], z)[0];
    if (lane < 16) stx(wsr, (lane >> 3) * 16384 + (lane & 7) * 16, WS_Y + (pbr * 18 + pw * 2) * 16384 + (h * 8 + (s & 7)) * 128, seal4(y0, y1, y2, y3, tag));
  }
  ST(6);
  if (w >= NRED) { STDUMP(); return; }
  if (wave < 8) {
    __builtin_amdgcn_s_sleep(FIRST2);
    u32x4 g0, g1;
    for (unsigned spins = 0;; ++spins) {
      g0 = ldx(wsr, lane * 16, WS_Y + w * 16384 + wave * 2048); g1 = ldx(wsr, lane * 16 + 1024, WS_Y + w * 16384 + wave * 2048);
      if (__all(valid(g0, tag) && valid(g1, tag)) || spins > SPIN_CAP) break;
      if (PS2) __builtin_amdgcn_s_sleep(PS2);
    }
    float y = fold16(fold32(uf(g0[0]) + uf(g1[0]), uf(g0[2]) + uf(g1[2])), fold32(uf(g0[1]) + uf(g1[1]), fourth(g0) + fourth(g1)));
    y += dpp<XOR8>(y);
    if (!(lane & 8)) L.fin[wave][(lane >> 4) * 8 + (lane & 7)] = y;
  }
  asm volatile("s_waitcnt lgkmcnt(0)" ::: "memory");
  __builtin_amdgcn_s_barrier();
  SB();
  if (wave == 0) {
    const float* f = &L.fin[(lane >> 5) * 4][lane & 31];
    float y = (f[0] + f[32]) + (f[64] + f[96]);
    y = fold32(y, y);
    const float yn = dpp<XOR1>(y);
    if (lane < 32 && !(lane & 1)) ((GAS unsigned*)out)[(w >> 1) * 32 + (w & 1) * 4 + (lane >> 3) * 8 + ((lane & 7) >> 1)] = cvtpk(y, yn);
    if (w == 0 && lane == 0) __hip_atomic_store((GAS unsigned*)(ws + WS_EPOCH), tag, RLX_AGENT);
    ST(7);
  }
  STDUMP();
}

extern "C" void kernel_launch(void* const* d_in, const int* in_sizes, int n_in, void* d_out, int out_size, void* d_ws, size_t ws_size, hipStream_t stream) {
  fused_decode<<<256, NTHR, 0, stream>>>((const uint16_t*)d_in[0], (const uint16_t*)d_in[1], (const uint16_t*)d_in[2], (const uint16_t*)d_in[3], (const uint16_t*)d_in[4],
                                         (const uint16_t*)d_in[5], (const uint16_t*)d_in[6], (const uint16_t*)d_in[7], (const uint16_t*)d_in[8], (const uint16_t*)d_in[9],
                                         (uint16_t*)d_out, (unsigned char*)d_ws);
}
```

```cpp
#define STAMP
#define PM1 2
#define PM2 1
#define PW1 0
#define PK1 0
#define PW2 0
#define PK2 8
#define PSC 8
#include <hip/hip_runtime.h>
#include <hip/hip_bf16.h>
#include <stdint.h>
#include <stdio.h>

#define GAS __attribute__((address_space(1)))
#define LAS __attribute__((address_space(3)))
#define RLX_AGENT __ATOMIC_RELAXED, __HIP_MEMORY_SCOPE_AGENT
#define RLX_WG __ATOMIC_RELAXED, __HIP_MEMORY_SCOPE_WORKGROUP
typedef unsigned u32x4 __attribute__((ext_vector_type(4)));
typedef unsigned u32x2 __attribute__((ext_vector_type(2)));
typedef float f32x4 __attribute__((ext_vector_type(4)));
typedef short s16x4 __attribute__((ext_vector_type(4)));
typedef __bf16 bf16x2 __attribute__((ext_vector_type(2)));
typedef __bf16 bf16x8 __attribute__((ext_vector_type(8)));
typedef unsigned long long u64;
typedef __amdgpu_buffer_rsrc_t rsrc_t;

constexpr int DM = 576, NQ = 4096;
constexpr int NWAVE = 11, NTHR = NWAVE * 64, NRED = 36;
constexpr unsigned WS_EPOCH = 0, WS_Q = 4096, WS_O = WS_Q + 16 * 4096, WS_Y = WS_O + 256 * 2048, WS_END = WS_Y + NRED * 16384;
constexpr size_t WS_STAMP = 8u << 20;
constexpr unsigned SPIN_CAP = 1u << 17;

#define AUXKV 2
#define AUXW 2
#define FIRST1 20
#define FIRST2 12
#define PS1 0
#define PS2 0
#define QSLEEP 64
#define PRETOUCH 2
#define WOGATE 0
#define XPOS 7
#define MID 6
#define ABL 0

__device__ __forceinline__ float bflo(unsigned u) { return __builtin_bit_cast(float, u << 16); }
__device__ __forceinline__ float bfhi(unsigned u) { return __builtin_bit_cast(float, u & 0xffff0000u); }
__device__ __forceinline__ unsigned fu(float f) { return __float_as_uint(f); }
__device__ __forceinline__ float uf(unsigned u) { return __uint_as_float(u); }
__device__ __forceinline__ unsigned cvtpk(float lo, float hi) { bf16x2 v = {(__bf16)lo, (__bf16)hi}; return __builtin_bit_cast(unsigned, v); }
__device__ __forceinline__ float dot2(unsigned k, unsigned q, float acc) {
  return __builtin_amdgcn_fdot2_f32_bf16(__builtin_bit_cast(bf16x2, k), __builtin_bit_cast(bf16x2, q), acc, false);
}
template <int CTRL> __device__ __forceinline__ float dpp(float x) {
  return __builtin_bit_cast(float, __builtin_amdgcn_mov_dpp(__builtin_bit_cast(int, x), CTRL, 0xf, 0xf, true));
}
constexpr int XOR1 = 0xB1, XOR2 = 0x4E, XOR7 = 0x141, XOR8 = 0x128;
__device__ __forceinline__ f32x4 mfma(u32x4 a, u32x4 b, f32x4 c) {
  return __builtin_amdgcn_mfma_f32_16x16x32_bf16(__builtin_bit_cast(bf16x8, a), __builtin_bit_cast(bf16x8, b), c, 0, 0, 0);
}
__device__ __forceinline__ float fold32(float a, float b) {
  auto r = __builtin_amdgcn_permlane32_swap(fu(a), fu(b), false, false);
  return uf(r[0]) + uf(r[1]);
}
__device__ __forceinline__ float fold16(float a, float b) {
  auto r = __builtin_amdgcn_permlane16_swap(fu(a), fu(b), false, false);
  return uf(r[0]) + uf(r[1]);
}
__device__ __forceinline__ float max16_32(float x) {
  auto s = __builtin_amdgcn_permlane16_swap(fu(x), fu(x), false, false);
  x = fmaxf(uf(s[0]), uf(s[1]));
  auto t = __builtin_amdgcn_permlane32_swap(fu(x), fu(x), false, false);
  return fmaxf(uf(t[0]), uf(t[1]));
}
__device__ __forceinline__ rsrc_t mkrs(const void* p, unsigned bytes) { return __builtin_amdgcn_make_buffer_rsrc((void*)p, 0, bytes, 0x00020000); }
template <int AUX> __device__ __forceinline__ u32x4 ldb(rsrc_t r, unsigned voff, unsigned soff) { return __builtin_amdgcn_raw_buffer_load_b128(r, voff, soff, AUX); }
constexpr int SC1 = 16;
__device__ __forceinline__ u32x4 ldx(rsrc_t r, unsigned voff, unsigned soff) { asm volatile("" ::: "memory"); return __builtin_amdgcn_raw_buffer_load_b128(r, voff, soff, SC1); }
__device__ __forceinline__ void stx(rsrc_t r, unsigned voff, unsigned soff, u32x4 v) { __builtin_amdgcn_raw_buffer_store_b128(v, r, voff, soff, SC1); }
__device__ __forceinline__ u32x4 seal(unsigned a, unsigned b, unsigned c, unsigned tag) { return u32x4{a, b, c, tag ^ a ^ b ^ c}; }
__device__ __forceinline__ bool valid(u32x4 g, unsigned tag) { return (g[0] ^ g[1] ^ g[2] ^ g[3]) == tag; }
__device__ __forceinline__ u32x4 seal4(float a, float b, float c, float d, unsigned tag) {
  return seal(__builtin_amdgcn_perm(fu(a), fu(d), 0x07060501u), __builtin_amdgcn_perm(fu(b), fu(d), 0x07060502u), __builtin_amdgcn_perm(fu(c), fu(d), 0x07060503u), tag);
}
__device__ __forceinline__ float fourth(u32x4 g) { return uf(__builtin_amdgcn_perm(g[2], __builtin_amdgcn_perm(g[1], g[0], 0x0c04000cu), 0x04020100u)); }
#define SB() __builtin_amdgcn_sched_barrier(0)
#define ST(i) do { SB(); u64 t_; asm volatile("s_memrealtime %0\n\ts_waitcnt lgkmcnt(0)" : "=s"(t_) :: "memory"); stp[i] = (unsigned)t_; SB(); } while (0)
#define PAGG(M, mn, mx, sm, c) (M == 0 ? mx : M == 1 ? sm / max(c, 1) : mn)
#define STDUMP() do { GAS u64* pg = (GAS u64*)(ws + WS_STAMP); \
  if (lane == 0) { if (wave == 0) __hip_atomic_store(pg + blockIdx.x, ((u64)tag << 32) | stp[0], RLX_AGENT); \
    if (wave == PW1) __hip_atomic_store(pg + 256 + blockIdx.x, ((u64)tag << 32) | stp[PK1], RLX_AGENT); \
    if (wave == PW2) __hip_atomic_store(pg + 512 + blockIdx.x, ((u64)tag << 32) | stp[PK2], RLX_AGENT); } \
  if (blockIdx.x == 0 && wave == 0) { u64 a[12]; \
    for (unsigned sp = 0; sp < SPIN_CAP; ++sp) { bool ok = true; for (int i_ = 0; i_ < 12; ++i_) { a[i_] = __hip_atomic_load(pg + lane + 64 * i_, RLX_AGENT); ok &= (unsigned)(a[i_] >> 32) == tag; } if (__all(ok)) break; } \
    int mn[3], mx[3], sm[3], cn[3]; \
    for (int q_ = 0; q_ < 3; ++q_) { mn[q_] = 0x7fffffff; mx[q_] = -0x7fffffff; sm[q_] = 0; cn[q_] = 0; \
      for (int i_ = 0; i_ < 4; ++i_) { const unsigned v_ = (unsigned)a[q_ * 4 + i_]; if (v_) { const int d_ = (int)(v_ - stp[0]); mn[q_] = min(mn[q_], d_); mx[q_] = max(mx[q_], d_); sm[q_] += d_; cn[q_]++; } } \
      for (int o_ = 1; o_ < 64; o_ <<= 1) { mn[q_] = min(mn[q_], __shfl_xor(mn[q_], o_)); mx[q_] = max(mx[q_], __shfl_xor(mx[q_], o_)); sm[q_] += __shfl_xor(sm[q_], o_); cn[q_] += __shfl_xor(cn[q_], o_); } } \
    const int X = PAGG(PM2, mn[2], mx[2], sm[2], cn[2]) - PAGG(PM1, mn[1], mx[1], sm[1], cn[1]); \
    const unsigned until = stp[0] + mn[0] + 4000 + 0 * X + (unsigned)((__builtin_amdgcn_s_getreg(20 | (3 << 11)) & 0xf) * 500); \
    for (unsigned sp = 0; sp < 1000000; ++sp) { if ((int)((unsigned)__builtin_amdgcn_s_memrealtime() - until) >= 0) break; } } } while (0)
#define STDECL() unsigned stp[12] = {0, 0, 0, 0, 0, 0, 0, 0, 0, 0, 0, 0}
#define STPAR , unsigned (&stp)[12]
#define STARG , stp

struct Lds {
  unsigned char wo[9][4096];
  float part[11][2][132];
  unsigned q[2][128];
  unsigned x[144];
  float fin[8][32];
  unsigned short o[32];
  unsigned qready, issued, done;
};

__device__ __forceinline__ void lds_wait(unsigned* p, unsigned n) {
  for (unsigned spins = 0; __hip_atomic_load(p, RLX_WG) < n && spins < (SPIN_CAP << 3); ++spins) __builtin_amdgcn_s_sleep(1);
}

template <bool LATE>
__device__ __forceinline__ unsigned stream_wave(Lds& L, rsrc_t kr, rsrc_t vr, unsigned rot, int wave, int br, int lane, rsrc_t wsr, unsigned pt0, unsigned pt1, unsigned pub STPAR) {
  const int g = lane >> 4, i = lane & 15;
  const unsigned loff = lane * 16;
  u32x4 K[16], V[16];
  unsigned so = rot & 0x7000u;
#define LOAD16(X, R) _Pragma("unroll") for (int t = 0; t < 16; t += 2) { \
    X[t] = ldb<AUXKV>(R, loff, so); SB(); unsigned nx = so + 4096u; asm volatile("" : "+s"(nx)); SB(); \
    X[t + 1] = ldb<AUXKV>(R, loff + 1024, so); SB(); so = nx & 0x7000u; asm volatile("" : "+s"(so)); SB(); }
  LOAD16(K, kr)
  ST(9);
  if (!LATE) {
    LOAD16(V, vr)
    if (lane == 0) __hip_atomic_fetch_add(&L.issued, 1u, RLX_WG);
    ST(1);
  }
  lds_wait(&L.qready, 1u);
  const unsigned tag = __builtin_amdgcn_readfirstlane(__hip_atomic_load(&L.qready, RLX_WG));
  __builtin_amdgcn_fence(__ATOMIC_ACQUIRE, "workgroup");
  const u32x4 q0 = *(const u32x4*)&L.q[br][4 * i], q1 = *(const u32x4*)&L.q[br][64 + 4 * i];
  constexpr float C2 = 0.088388347648318440f * 1.4426950408889634f;
  float sc[16], m = -INFINITY;
#pragma unroll
  for (int t = 0; t < 16; ++t) {
    float d0 = 0, d1 = 0;
#pragma unroll
    for (int k = 0; k < 4; ++k) { d0 = dot2(K[t][k], q0[k], d0); d1 = dot2(K[t][k], q1[k], d1); }
    d0 += dpp<XOR8>(d0); d1 += dpp<XOR8>(d1);
    float d = (i & 8) ? d1 : d0;
    d += dpp<XOR7>(d); d += dpp<XOR2>(d); d += dpp<XOR1>(d);
    sc[t] = d; m = fmaxf(m, d);
  }
  m = max16_32(m) * C2;
  unsigned msk[4];
#pragma unroll
  for (int k = 0; k < 4; ++k) msk[k] = (((i & 7) >> 1) == k) ? ((i & 1) ? 0xffff0000u : 0x0000ffffu) : 0u;
  float l = 0;
  u32x4 a[16];
#pragma unroll
  for (int t = 0; t < 16; ++t) {
    const float p = __builtin_amdgcn_exp2f(fmaf(sc[t], C2, -m));
    const unsigned pp = cvtpk(p, p);
    l += p;
    a[t] = u32x4{pp & msk[0], pp & msk[1], pp & msk[2], pp & msk[3]};
  }
  l = fold16(l, l); l = fold32(l, l);
  if ((lane & 55) == 0) { L.part[wave][lane >> 3][0] = m; L.part[wave][lane >> 3][1] = l; }
  SB();
  ST(2);
  if (!LATE) {
    f32x4 acc = {0, 0, 0, 0};
#pragma unroll
    for (int t = 0; t < 16; ++t) acc = mfma(a[t], V[t], acc);
    *(f32x4*)&L.part[wave][g >> 1][4 + 8 * i + 4 * (g & 1)] = acc;
    __builtin_amdgcn_fence(__ATOMIC_RELEASE, "workgroup");
    if (lane == 0) __hip_atomic_fetch_add(&L.done, 1u, RLX_WG);
    ST(3);
  } else {
#pragma unroll
    for (int t = 0; t < 16; ++t) asm volatile("" : "+v"(a[t]));
    lds_wait(&L.issued, 8u);
    SB();
    unsigned pre0 = 0, pre1 = 0;
    if (PRETOUCH == 1 || PRETOUCH == 2) pre0 = __builtin_amdgcn_raw_buffer_load_b32(wsr, lane * 128 + pt0, 0, SC1);
    if (PRETOUCH == 1) pre1 = __builtin_amdgcn_raw_buffer_load_b32(wsr, lane * 128 + pt1, 0, SC1);
    SB();
    LOAD16(V, vr)
    if (lane == 0) __hip_atomic_fetch_add(&L.issued, 1u, RLX_WG);
    ST(1);
    f32x4 acc = {0, 0, 0, 0};
#pragma unroll
    for (int t = 0; t < MID; ++t) acc = mfma(a[t], V[t], acc);
    asm volatile("" : "+v"(acc));
    SB();
    lds_wait(&L.done, 8u);
    __builtin_amdgcn_fence(__ATOMIC_ACQUIRE, "workgroup");
    const int hd = g >> 1;
    const float* rec = &L.part[br ? 4 : 3][hd][0];
    const float2 own = *(const float2*)&L.part[wave][hd][0];
    float2 r[7]; f32x4 o[7];
#pragma unroll
    for (int u = 0; u < 7; ++u) { r[u] = *(const float2*)(rec + u * 264); o[u] = *(const f32x4*)(rec + u * 264 + 4 + 8 * i + 4 * (g & 1)); }
    float M = fmaxf(own.x, r[0].x);
#pragma unroll
    for (int u = 1; u < 7; ++u) { r[u].x = br ? r[u].x : -INFINITY; M = fmaxf(M, r[u].x); }
    float f = __builtin_amdgcn_exp2f(own.x - M), Ls = f * own.y;
    f32x4 base = {0, 0, 0, 0};
#pragma unroll
    for (int u = 0; u < 7; ++u) { const float e = __builtin_amdgcn_exp2f(r[u].x - M); Ls = fmaf(e, r[u].y, Ls); base += e * o[u]; }
    const float inv = __builtin_amdgcn_rcpf(Ls);
    f *= inv; base *= inv;
    unsigned lse = fu(M + __builtin_amdgcn_logf(Ls)), poff = (4 * hd + (i >> 2)) * 2048 + (4 * (g & 1) + (i & 3)) * 16;
    asm volatile("" : "+v"(f), "+v"(base), "+v"(lse), "+v"(poff));
    SB();
    ST(4);
#pragma unroll
    for (int t = MID; t < 16; ++t) acc = mfma(a[t], V[t], acc);
    acc = acc * f + base;
    if (!(ABL & 2)) stx(wsr, poff, pub, seal(cvtpk(acc[0], acc[1]), cvtpk(acc[2], acc[3]), lse, tag));
    else asm volatile("" :: "v"(acc));
    asm volatile("" :: "v"(pre0), "v"(pre1));
    ST(3);
  }
  return tag;
}

__global__ void __launch_bounds__(NTHR, 3) fused_decode(const uint16_t* xA, const uint16_t* kA, const uint16_t* vA, const uint16_t* wqA, const uint16_t* woA,
                                                         const uint16_t* xB, const uint16_t* kB, const uint16_t* vB, const uint16_t* wqB, const uint16_t* woB,
                                                         uint16_t* out, unsigned char* wsp) {
  __shared__ __attribute__((aligned(16))) Lds L;
  GAS unsigned char* ws = (GAS unsigned char*)wsp;
  const int tid = threadIdx.x, lane = tid & 63, wave = __builtin_amdgcn_readfirstlane(tid >> 6);
  const int w = blockIdx.x, h = (w & 7) * 2 + (w >> 7), s = (w >> 3) & 15;
  const int pbr = s >> 3;
  STDECL();
  ST(0);
  const rsrc_t wsr = mkrs(wsp, WS_END);
  unsigned tag;
  if (tid == 64) { L.qready = 0u; L.issued = 0u; L.done = 0u; }

  if (__builtin_expect(wave == 0, 1)) {
    const int rs = lane >> 3, c = lane & 7;
    const int qbr = (w >> 1) & 1, half = (w >> 2) & 1, j = w >> 3, blk = (j >> 1) * 4 + (j & 1) * 2 + ((j ^ w) & 1);
    const rsrc_t wr = mkrs((qbr ? wqB : wqA) + (half * 288) * NQ + blk * 64, 288 * NQ * 2);
    const unsigned woff = (unsigned)(rs * 36 * NQ + c * 8) * 2;
    u32x4 wv[36], xr;
    unsigned epoch, so = 0;
#pragma unroll
    for (int t = 0; t < 36; ++t) {
      wv[t] = ldb<AUXW>(wr, woff, so); SB();
      so += NQ * 2; asm volatile("" : "+s"(so)); SB();
      if (t == XPOS) {
        xr = ldb<0>(mkrs((qbr ? xB : xA) + half * 288, 576), lane * 16, 0);
        epoch = __hip_atomic_load((GAS unsigned*)(ws + WS_EPOCH), RLX_AGENT); SB();
      }
    }
    __builtin_amdgcn_s_barrier();
    SB();
    ST(8);
    if (lane < 36) *(u32x4*)&L.x[lane * 4] = xr;
    float acc[8] = {0, 0, 0, 0, 0, 0, 0, 0};
#pragma unroll
    for (int t4 = 0; t4 < 9; ++t4) {
      const u32x2 xw = *(const u32x2*)&L.x[rs * 18 + t4 * 2];
#pragma unroll
      for (int u = 0; u < 4; ++u) {
        const int t = t4 * 4 + u;
        const unsigned xl = (u & 1) ? xw[u >> 1] >> 16 : xw[u >> 1] & 0xffffu, xh = (u & 1) ? xw[u >> 1] & 0xffff0000u : xw[u >> 1] << 16;
#pragma unroll
        for (int k = 0; k < 4; ++k) { acc[2 * k] = dot2(wv[t][k], xl, acc[2 * k]); acc[2 * k + 1] = dot2(wv[t][k], xh, acc[2 * k + 1]); }
      }
      SB();
    }
    float b[4];
#pragma unroll
    for (int k = 0; k < 4; ++k) { b[k] = fold32(acc[k], acc[k + 4]); b[k] = fold16(b[k], b[k]); b[k] += dpp<XOR8>(b[k]); }
    asm volatile("" : "+v"(epoch));
    tag = __builtin_amdgcn_readfirstlane(epoch) + 1u;
    tag += tag == 0u;
    if (!(lane & 24)) stx(wsr, (c * 2 + (lane >> 5)) * 16, WS_Q + (blk >> 2) * 4096 + (qbr * 2 + half) * 1024 + (blk & 3) * 256, seal4(b[0], b[1], b[2], b[3], tag));
    if (PRETOUCH == 3) {
      const u32x4 z = {0u, 0u, 0u, 0u};
      stx(wsr, (lane >> 3) * 2048 + (lane & 7) * 16, WS_O + h * 32768 + s * 128, z);
      stx(wsr, (lane >> 3) * 2048 + (lane & 7) * 16, WS_O + h * 32768 + 16384 + s * 128, z);
#pragma unroll
      for (int k = 0; k < 3; ++k) if (k < 2 || lane < 16) stx(wsr, (lane >> 3) * 16384 + (lane & 7) * 16, WS_Y + (pbr * 18 + 8 * k) * 16384 + (h * 8 + (s & 7)) * 128, z);
    }
    ST(1);
    __builtin_amdgcn_s_sleep(QSLEEP);
    u32x4 g[4];
    if (ABL & 1) { for (int i = 0; i < 4; ++i) g[i] = seal4(b[0], b[1], b[2], b[3], tag); asm volatile("s_waitcnt vmcnt(0)" ::: "memory"); } else
    for (unsigned spins = 0;; ++spins) {
      bool ok = true;
#pragma unroll
      for (int i = 0; i < 4; ++i) g[i] = ldx(wsr, lane * 16 + i * 1024, WS_Q + h * 4096);
#pragma unroll
      for (int i = 0; i < 4; ++i) ok &= valid(g[i], tag);
      if (__all(ok) || spins > SPIN_CAP) break;
    }
#pragma unroll
    for (int bb = 0; bb < 2; ++bb) {
      const u32x4 p0 = g[2 * bb], p1 = g[2 * bb + 1];
      *(u32x2*)&L.q[bb][2 * lane] = u32x2{cvtpk(uf(p0[0]) + uf(p1[0]), uf(p0[1]) + uf(p1[1])), cvtpk(uf(p0[2]) + uf(p1[2]), fourth(p0) + fourth(p1))};
    }
    __builtin_amdgcn_fence(__ATOMIC_RELEASE, "workgroup");
    if (lane == 0) __hip_atomic_store(&L.qready, tag, RLX_WG);
    ST(2);
  } else {
    const int br = wave == 1 || wave == 3 ? 0 : 1;
    const int pair = wave < 5 ? 3 : (wave - 5) >> 1, odd = wave < 5 ? (wave - 1) >> 1 : (wave - 5) & 1;
    const size_t off = ((size_t)h * 2048 + pair * 512 + s * 32 + odd * 2) * 1024;
    const rsrc_t kr = mkrs((const char*)(br ? kB : kA) + off, 32 * 1024), vr = mkrs((const char*)(br ? vB : vA) + off, 32 * 1024);
    asm volatile("s_waitcnt lgkmcnt(0)" ::: "memory");
    __builtin_amdgcn_s_barrier();
    SB();
    ST(8);
    const unsigned OOR = 0x40000000u;
    const unsigned pt0 = lane < 8 ? WS_O + (h * 16 + s) * 2048 + br * 1024 : OOR, pt1 = w < NRED ? WS_Y + w * 16384 + br * 8192 : OOR;
    if (wave < 3) tag = stream_wave<true>(L, kr, vr, (s + h) * 4096, wave, br, lane, wsr, pt0, pt1, WS_O + h * 32768 + br * 16384 + s * 128 STARG);
    else tag = stream_wave<false>(L, kr, vr, (s + h) * 4096, wave, br, lane, wsr, 0, 0, 0 STARG);
  }
  const int pw = wave == 0 ? 8 : wave - 3;
  u32x4 wf[4];
  if (pw >= 0) {
    lds_wait(&L.issued, 10u);
    const rsrc_t wr = mkrs((pbr ? woB : woA) + (h * 256 + (s & 7) * 32) * DM + pw * 64, 32 * DM * 2);
    const unsigned woff = ((lane >> 3) * DM + (lane & 7) * 8) * 2;
    LAS unsigned char* dst = (LAS unsigned char*)&L.wo[pw][0];
#pragma unroll
    for (int t = 0; t < 4; ++t) __builtin_amdgcn_raw_ptr_buffer_load_lds(wr, dst + t * 1024, 16, woff, t * 8 * DM * 2, 0, AUXW);
    asm volatile("s_waitcnt vmcnt(0)" ::: "memory");
    LAS unsigned char* src = dst + (lane >> 4) * 1024 + ((lane >> 2) & 3) * 128 + (lane & 3) * 8;
#pragma unroll
    for (int b = 0; b < 4; ++b) {
      const u32x2 lo = __builtin_bit_cast(u32x2, __builtin_amdgcn_ds_read_tr16_b64_v4i16((LAS s16x4*)(src + b * 32)));
      const u32x2 hi = __builtin_bit_cast(u32x2, __builtin_amdgcn_ds_read_tr16_b64_v4i16((LAS s16x4*)(src + b * 32 + 512)));
      wf[b] = u32x4{lo[0], lo[1], hi[0], hi[1]};
    }
    asm volatile("" : "+v"(wf[0]), "+v"(wf[1]), "+v"(wf[2]), "+v"(wf[3]));
    ST(5);
  } else if (ABL & 2) {
  } else if (wave == 2) {
    if (PRETOUCH == 2 && w < NRED) { const unsigned pre = __builtin_amdgcn_raw_buffer_load_b32(wsr, lane * 128, WS_Y + w * 16384, SC1) ^ __builtin_amdgcn_raw_buffer_load_b32(wsr, lane * 128 + 8192, WS_Y + w * 16384, SC1); asm volatile("" :: "v"(pre)); }
  } else {
    __builtin_amdgcn_s_sleep(FIRST1);
    u32x4 g0, g1;
    for (unsigned spins = 0;; ++spins) {
      g0 = ldx(wsr, lane * 16, WS_O + (h * 16 + s) * 2048); g1 = ldx(wsr, lane * 16 + 1024, WS_O + (h * 16 + s) * 2048);
      if (__all(valid(g0, tag) && valid(g1, tag)) || spins > SPIN_CAP) break;
      if (PS1) __builtin_amdgcn_s_sleep(PS1);
    }
    ST(10);
    float mx = fmaxf(uf(g0[2]), uf(g1[2]));
    mx = max16_32(fmaxf(mx, dpp<XOR8>(mx)));
    const float t0 = __builtin_amdgcn_exp2f(uf(g0[2]) - mx), t1 = __builtin_amdgcn_exp2f(uf(g1[2]) - mx);
    float e = t0 + t1;
    e += dpp<XOR8>(e); e = fold16(e, e); e = fold32(e, e);
    const float a0 = fmaf(t0, bflo(g0[0]), t1 * bflo(g1[0])), a1 = fmaf(t0, bfhi(g0[0]), t1 * bfhi(g1[0]));
    const float a2 = fmaf(t0, bflo(g0[1]), t1 * bflo(g1[1])), a3 = fmaf(t0, bfhi(g0[1]), t1 * bfhi(g1[1]));
    float a = fold16(fold32(a0, a2), fold32(a1, a3));
    a += dpp<XOR8>(a);
    if (!(lane & 8)) L.o[8 * (lane & 3) + (lane & 4) + (lane >> 4)] = (unsigned short)cvtpk(a * __builtin_amdgcn_rcpf(e), 0.f);
    ST(5);
  }
  if (ABL & 2) { STDUMP(); return; }
  asm volatile("s_waitcnt lgkmcnt(0)" ::: "memory");
  __builtin_amdgcn_s_barrier();
  SB();
  ST(11);
  if (pw >= 0) {
    const u32x4 ov = *(const u32x4*)&L.o[(lane >> 4) * 8];
    const f32x4 z = {0, 0, 0, 0};
    const float y0 = mfma(ov, wf[0], z)[0], y1 = mfma(ov, wf[1], z)[0], y2 = mfma(ov, wf[2], z)[0], y3 = mfma(ov, wf[3], z)[0];
    if (lane < 16) stx(wsr, (lane >> 3) * 16384 + (lane & 7) * 16, WS_Y + (pbr * 18 + pw * 2) * 16384 + (h * 8 + (s & 7)) * 128, seal4(y0, y1, y2, y3, tag));
  }
  ST(6);
  if (w >= NRED) { STDUMP(); return; }
  if (wave < 8) {
    __builtin_amdgcn_s_sleep(FIRST2);
    u32x4 g0, g1;
    for (unsigned spins = 0;; ++spins) {
      g0 = ldx(wsr, lane * 16, WS_Y + w * 16384 + wave * 2048); g1 = ldx(wsr, lane * 16 + 1024, WS_Y + w * 16384 + wave * 2048);
      if (__all(valid(g0, tag) && valid(g1, tag)) || spins > SPIN_CAP) break;
      if (PS2) __builtin_amdgcn_s_sleep(PS2);
    }
    float y = fold16(fold32(uf(g0[0]) + uf(g1[0]), uf(g0[2]) + uf(g1[2])), fold32(uf(g0[1]) + uf(g1[1]), fourth(g0) + fourth(g1)));
    y += dpp<XOR8>(y);
    if (!(lane & 8)) L.fin[wave][(lane >> 4) * 8 + (lane & 7)] = y;
  }
  asm volatile("s_waitcnt lgkmcnt(0)" ::: "memory");
  __builtin_amdgcn_s_barrier();
  SB();
  if (wave == 0) {
    const float* f = &L.fin[(lane >> 5) * 4][lane & 31];
    float y = (f[0] + f[32]) + (f[64] + f[96]);
    y = fold32(y, y);
    const float yn = dpp<XOR1>(y);
    if (lane < 32 && !(lane & 1)) ((GAS unsigned*)out)[(w >> 1) * 32 + (w & 1) * 4 + (lane >> 3) * 8 + ((lane & 7) >> 1)] = cvtpk(y, yn);
    if (w == 0 && lane == 0) __hip_atomic_store((GAS unsigned*)(ws + WS_EPOCH), tag, RLX_AGENT);
    ST(7);
  }
  STDUMP();
}

extern "C" void kernel_launch(void* const* d_in, const int* in_sizes, int n_in, void* d_out, int out_size, void* d_ws, size_t ws_size, hipStream_t stream) {
  for (int i = 0; i < 10; ++i) fprintf(stderr, "in%d %p\n", i, d_in[i]); fprintf(stderr, "out %p ws %p\n", d_out, d_ws);
  fused_decode<<<256, NTHR, 0, stream>>>((const uint16_t*)d_in[0], (const uint16_t*)d_in[1], (const uint16_t*)d_in[2], (const uint16_t*)d_in[3], (const uint16_t*)d_in[4],
                                         (const uint16_t*)d_in[5], (const uint16_t*)d_in[6], (const uint16_t*)d_in[7], (const uint16_t*)d_in[8], (const uint16_t*)d_in[9],
                                         (uint16_t*)d_out, (unsigned char*)d_ws);
}
```

```cpp
#define PF1 (w&7)<4
#define PF2 (w&7)>=4
#define STAMP
#define PM1 1
#define PM2 1
#define PW1 1
#define PK1 3
#define PW2 1
#define PK2 3
#define PSC 8
#include <hip/hip_runtime.h>
#include <hip/hip_bf16.h>
#include <stdint.h>

#define GAS __attribute__((address_space(1)))
#define LAS __attribute__((address_space(3)))
#define RLX_AGENT __ATOMIC_RELAXED, __HIP_MEMORY_SCOPE_AGENT
#define RLX_WG __ATOMIC_RELAXED, __HIP_MEMORY_SCOPE_WORKGROUP
typedef unsigned u32x4 __attribute__((ext_vector_type(4)));
typedef unsigned u32x2 __attribute__((ext_vector_type(2)));
typedef float f32x4 __attribute__((ext_vector_type(4)));
typedef short s16x4 __attribute__((ext_vector_type(4)));
typedef __bf16 bf16x2 __attribute__((ext_vector_type(2)));
typedef __bf16 bf16x8 __attribute__((ext_vector_type(8)));
typedef unsigned long long u64;
typedef __amdgpu_buffer_rsrc_t rsrc_t;

constexpr int DM = 576, NQ = 4096;
constexpr int NWAVE = 11, NTHR = NWAVE * 64, NRED = 36;
constexpr unsigned WS_EPOCH = 0, WS_Q = 4096, WS_O = WS_Q + 16 * 4096, WS_Y = WS_O + 256 * 2048, WS_END = WS_Y + NRED * 16384;
constexpr size_t WS_STAMP = 8u << 20;
constexpr unsigned SPIN_CAP = 1u << 17;

#define AUXKV 2
#define AUXW 2
#define FIRST1 20
#define FIRST2 12
#define PS1 0
#define PS2 0
#define QSLEEP 64
#define PRETOUCH 2
#define WOGATE 0
#define XPOS 7
#define MID 6
#define ABL 0

__device__ __forceinline__ float bflo(unsigned u) { return __builtin_bit_cast(float, u << 16); }
__device__ __forceinline__ float bfhi(unsigned u) { return __builtin_bit_cast(float, u & 0xffff0000u); }
__device__ __forceinline__ unsigned fu(float f) { return __float_as_uint(f); }
__device__ __forceinline__ float uf(unsigned u) { return __uint_as_float(u); }
__device__ __forceinline__ unsigned cvtpk(float lo, float hi) { bf16x2 v = {(__bf16)lo, (__bf16)hi}; return __builtin_bit_cast(unsigned, v); }
__device__ __forceinline__ float dot2(unsigned k, unsigned q, float acc) {
  return __builtin_amdgcn_fdot2_f32_bf16(__builtin_bit_cast(bf16x2, k), __builtin_bit_cast(bf16x2, q), acc, false);
}
template <int CTRL> __device__ __forceinline__ float dpp(float x) {
  return __builtin_bit_cast(float, __builtin_amdgcn_mov_dpp(__builtin_bit_cast(int, x), CTRL, 0xf, 0xf, true));
}
constexpr int XOR1 = 0xB1, XOR2 = 0x4E, XOR7 = 0x141, XOR8 = 0x128;
__device__ __forceinline__ f32x4 mfma(u32x4 a, u32x4 b, f32x4 c) {
  return __builtin_amdgcn_mfma_f32_16x16x32_bf16(__builtin_bit_cast(bf16x8, a), __builtin_bit_cast(bf16x8, b), c, 0, 0, 0);
}
__device__ __forceinline__ float fold32(float a, float b) {
  auto r = __builtin_amdgcn_permlane32_swap(fu(a), fu(b), false, false);
  return uf(r[0]) + uf(r[1]);
}
__device__ __forceinline__ float fold16(float a, float b) {
  auto r = __builtin_amdgcn_permlane16_swap(fu(a), fu(b), false, false);
  return uf(r[0]) + uf(r[1]);
}
__device__ __forceinline__ float max16_32(float x) {
  auto s = __builtin_amdgcn_permlane16_swap(fu(x), fu(x), false, false);
  x = fmaxf(uf(s[0]), uf(s[1]));
  auto t = __builtin_amdgcn_permlane32_swap(fu(x), fu(x), false, false);
  return fmaxf(uf(t[0]), uf(t[1]));
}
__device__ __forceinline__ rsrc_t mkrs(const void* p, unsigned bytes) { return __builtin_amdgcn_make_buffer_rsrc((void*)p, 0, bytes, 0x00020000); }
template <int AUX> __device__ __forceinline__ u32x4 ldb(rsrc_t r, unsigned voff, unsigned soff) { return __builtin_amdgcn_raw_buffer_load_b128(r, voff, soff, AUX); }
constexpr int SC1 = 16;
__device__ __forceinline__ u32x4 ldx(rsrc_t r, unsigned voff, unsigned soff) { asm volatile("" ::: "memory"); return __builtin_amdgcn_raw_buffer_load_b128(r, voff, soff, SC1); }
__device__ __forceinline__ void stx(rsrc_t r, unsigned voff, unsigned soff, u32x4 v) { __builtin_amdgcn_raw_buffer_store_b128(v, r, voff, soff, SC1); }
__device__ __forceinline__ u32x4 seal(unsigned a, unsigned b, unsigned c, unsigned tag) { return u32x4{a, b, c, tag ^ a ^ b ^ c}; }
__device__ __forceinline__ bool valid(u32x4 g, unsigned tag) { return (g[0] ^ g[1] ^ g[2] ^ g[3]) == tag; }
__device__ __forceinline__ u32x4 seal4(float a, float b, float c, float d, unsigned tag) {
  return seal(__builtin_amdgcn_perm(fu(a), fu(d), 0x07060501u), __builtin_amdgcn_perm(fu(b), fu(d), 0x07060502u), __builtin_amdgcn_perm(fu(c), fu(d), 0x07060503u), tag);
}
__device__ __forceinline__ float fourth(u32x4 g) { return uf(__builtin_amdgcn_perm(g[2], __builtin_amdgcn_perm(g[1], g[0], 0x0c04000cu), 0x04020100u)); }
#define SB() __builtin_amdgcn_sched_barrier(0)
#define ST(i) do { SB(); u64 t_; asm volatile("s_memrealtime %0\n\ts_waitcnt lgkmcnt(0)" : "=s"(t_) :: "memory"); stp[i] = (unsigned)t_; SB(); } while (0)
#define PAGG(M, mn, mx, sm, c) (M == 0 ? mx : M == 1 ? sm / max(c, 1) : mn)
#define STDUMP() do { GAS u64* pg = (GAS u64*)(ws + WS_STAMP); \
  if (lane == 0) { if (wave == 0) __hip_atomic_store(pg + blockIdx.x, ((u64)tag << 32) | stp[0], RLX_AGENT); \
    if (wave == PW1) __hip_atomic_store(pg + 256 + blockIdx.x, ((u64)tag << 32) | ((PF1) ? stp[PK1] : 0u), RLX_AGENT); \
    if (wave == PW2) __hip_atomic_store(pg + 512 + blockIdx.x, ((u64)tag << 32) | ((PF2) ? stp[PK2] : 0u), RLX_AGENT); } \
  if (blockIdx.x == 0 && wave == 0) { u64 a[12]; \
    for (unsigned sp = 0; sp < SPIN_CAP; ++sp) { bool ok = true; for (int i_ = 0; i_ < 12; ++i_) { a[i_] = __hip_atomic_load(pg + lane + 64 * i_, RLX_AGENT); ok &= (unsigned)(a[i_] >> 32) == tag; } if (__all(ok)) break; } \
    int mn[3], mx[3], sm[3], cn[3]; \
    for (int q_ = 0; q_ < 3; ++q_) { mn[q_] = 0x7fffffff; mx[q_] = -0x7fffffff; sm[q_] = 0; cn[q_] = 0; \
      for (int i_ = 0; i_ < 4; ++i_) { const unsigned v_ = (unsigned)a[q_ * 4 + i_]; if (v_) { const int d_ = (int)(v_ - stp[0]); mn[q_] = min(mn[q_], d_); mx[q_] = max(mx[q_], d_); sm[q_] += d_; cn[q_]++; } } \
      for (int o_ = 1; o_ < 64; o_ <<= 1) { mn[q_] = min(mn[q_], __shfl_xor(mn[q_], o_)); mx[q_] = max(mx[q_], __shfl_xor(mx[q_], o_)); sm[q_] += __shfl_xor(sm[q_], o_); cn[q_] += __shfl_xor(cn[q_], o_); } } \
    const int X = PAGG(PM2, mn[2], mx[2], sm[2], cn[2]) - PAGG(PM1, mn[1], mx[1], sm[1], cn[1]); \
    const unsigned until = stp[0] + mn[0] + 6000 + PSC * X; \
    for (unsigned sp = 0; sp < 1000000; ++sp) { if ((int)((unsigned)__builtin_amdgcn_s_memrealtime() - until) >= 0) break; } } } while (0)
#define STDECL() unsigned stp[12] = {0, 0, 0, 0, 0, 0, 0, 0, 0, 0, 0, 0}
#define STPAR , unsigned (&stp)[12]
#define STARG , stp

struct Lds {
  unsigned char wo[9][4096];
  float part[11][2][132];
  unsigned q[2][128];
  unsigned x[144];
  float fin[8][32];
  unsigned short o[32];
  unsigned qready, issued, done;
};

__device__ __forceinline__ void lds_wait(unsigned* p, unsigned n) {
  for (unsigned spins = 0; __hip_atomic_load(p, RLX_WG) < n && spins < (SPIN_CAP << 3); ++spins) __builtin_amdgcn_s_sleep(1);
}

template <bool LATE>
__device__ __forceinline__ unsigned stream_wave(Lds& L, rsrc_t kr, rsrc_t vr, unsigned rot, int wave, int br, int lane, rsrc_t wsr, unsigned pt0, unsigned pt1, unsigned pub STPAR) {
  const int g = lane >> 4, i = lane & 15;
  const unsigned loff = lane * 16;
  u32x4 K[16], V[16];
  unsigned so = rot & 0x7000u;
#define LOAD16(X, R) _Pragma("unroll") for (int t = 0; t < 16; t += 2) { \
    X[t] = ldb<AUXKV>(R, loff, so); SB(); unsigned nx = so + 4096u; asm volatile("" : "+s"(nx)); SB(); \
    X[t + 1] = ldb<AUXKV>(R, loff + 1024, so); SB(); so = nx & 0x7000u; asm volatile("" : "+s"(so)); SB(); }
  LOAD16(K, kr)
  ST(9);
  if (!LATE) {
    LOAD16(V, vr)
    if (lane == 0) __hip_atomic_fetch_add(&L.issued, 1u, RLX_WG);
    ST(1);
  }
  lds_wait(&L.qready, 1u);
  const unsigned tag = __builtin_amdgcn_readfirstlane(__hip_atomic_load(&L.qready, RLX_WG));
  __builtin_amdgcn_fence(__ATOMIC_ACQUIRE, "workgroup");
  const u32x4 q0 = *(const u32x4*)&L.q[br][4 * i], q1 = *(const u32x4*)&L.q[br][64 + 4 * i];
  constexpr float C2 = 0.088388347648318440f * 1.4426950408889634f;
  float sc[16], m = -INFINITY;
#pragma unroll
  for (int t = 0; t < 16; ++t) {
    float d0 = 0, d1 = 0;
#pragma unroll
    for (int k = 0; k < 4; ++k) { d0 = dot2(K[t][k], q0[k], d0); d1 = dot2(K[t][k], q1[k], d1); }
    d0 += dpp<XOR8>(d0); d1 += dpp<XOR8>(d1);
    float d = (i & 8) ? d1 : d0;
    d += dpp<XOR7>(d); d += dpp<XOR2>(d); d += dpp<XOR1>(d);
    sc[t] = d; m = fmaxf(m, d);
  }
  m = max16_32(m) * C2;
  unsigned msk[4];
#pragma unroll
  for (int k = 0; k < 4; ++k) msk[k] = (((i & 7) >> 1) == k) ? ((i & 1) ? 0xffff0000u : 0x0000ffffu) : 0u;
  float l = 0;
  u32x4 a[16];
#pragma unroll
  for (int t = 0; t < 16; ++t) {
    const float p = __builtin_amdgcn_exp2f(fmaf(sc[t], C2, -m));
    const unsigned pp = cvtpk(p, p);
    l += p;
    a[t] = u32x4{pp & msk[0], pp & msk[1], pp & msk[2], pp & msk[3]};
  }
  l = fold16(l, l); l = fold32(l, l);
  if ((lane & 55) == 0) { L.part[wave][lane >> 3][0] = m; L.part[wave][lane >> 3][1] = l; }
  SB();
  ST(2);
  if (!LATE) {
    f32x4 acc = {0, 0, 0, 0};
#pragma unroll
    for (int t = 0; t < 16; ++t) acc = mfma(a[t], V[t], acc);
    *(f32x4*)&L.part[wave][g >> 1][4 + 8 * i + 4 * (g & 1)] = acc;
    __builtin_amdgcn_fence(__ATOMIC_RELEASE, "workgroup");
    if (lane == 0) __hip_atomic_fetch_add(&L.done, 1u, RLX_WG);
    ST(3);
  } else {
#pragma unroll
    for (int t = 0; t < 16; ++t) asm volatile("" : "+v"(a[t]));
    lds_wait(&L.issued, 8u);
    SB();
    unsigned pre0 = 0, pre1 = 0;
    if (PRETOUCH == 1 || PRETOUCH == 2) pre0 = __builtin_amdgcn_raw_buffer_load_b32(wsr, lane * 128 + pt0, 0, SC1);
    if (PRETOUCH == 1) pre1 = __builtin_amdgcn_raw_buffer_load_b32(wsr, lane * 128 + pt1, 0, SC1);
    SB();
    LOAD16(V, vr)
    if (lane == 0) __hip_atomic_fetch_add(&L.issued, 1u, RLX_WG);
    ST(1);
    f32x4 acc = {0, 0, 0, 0};
#pragma unroll
    for (int t = 0; t < MID; ++t) acc = mfma(a[t], V[t], acc);
    asm volatile("" : "+v"(acc));
    SB();
    lds_wait(&L.done, 8u);
    __builtin_amdgcn_fence(__ATOMIC_ACQUIRE, "workgroup");
    const int hd = g >> 1;
    const float* rec = &L.part[br ? 4 : 3][hd][0];
    const float2 own = *(const float2*)&L.part[wave][hd][0];
    float2 r[7]; f32x4 o[7];
#pragma unroll
    for (int u = 0; u < 7; ++u) { r[u] = *(const float2*)(rec + u * 264); o[u] = *(const f32x4*)(rec + u * 264 + 4 + 8 * i + 4 * (g & 1)); }
    float M = fmaxf(own.x, r[0].x);
#pragma unroll
    for (int u = 1; u < 7; ++u) { r[u].x = br ? r[u].x : -INFINITY; M = fmaxf(M, r[u].x); }
    float f = __builtin_amdgcn_exp2f(own.x - M), Ls = f * own.y;
    f32x4 base = {0, 0, 0, 0};
#pragma unroll
    for (int u = 0; u < 7; ++u) { const float e = __builtin_amdgcn_exp2f(r[u].x - M); Ls = fmaf(e, r[u].y, Ls); base += e * o[u]; }
    const float inv = __builtin_amdgcn_rcpf(Ls);
    f *= inv; base *= inv;
    unsigned lse = fu(M + __builtin_amdgcn_logf(Ls)), poff = (4 * hd + (i >> 2)) * 2048 + (4 * (g & 1) + (i & 3)) * 16;
    asm volatile("" : "+v"(f), "+v"(base), "+v"(lse), "+v"(poff));
    SB();
    ST(4);
#pragma unroll
    for (int t = MID; t < 16; ++t) acc = mfma(a[t], V[t], acc);
    acc = acc * f + base;
    if (!(ABL & 2)) stx(wsr, poff, pub, seal(cvtpk(acc[0], acc[1]), cvtpk(acc[2], acc[3]), lse, tag));
    else asm volatile("" :: "v"(acc));
    asm volatile("" :: "v"(pre0), "v"(pre1));
    ST(3);
  }
  return tag;
}

__global__ void __launch_bounds__(NTHR, 3) fused_decode(const uint16_t* xA, const uint16_t* kA, const uint16_t* vA, const uint16_t* wqA, const uint16_t* woA,
                                                         const uint16_t* xB, const uint16_t* kB, const uint16_t* vB, const uint16_t* wqB, const uint16_t* woB,
                                                         uint16_t* out, unsigned char* wsp) {
  __shared__ __attribute__((aligned(16))) Lds L;
  GAS unsigned char* ws = (GAS unsigned char*)wsp;
  const int tid = threadIdx.x, lane = tid & 63, wave = __builtin_amdgcn_readfirstlane(tid >> 6);
  const int w = blockIdx.x, h = (w & 7) * 2 + (w >> 7), s = (w >> 3) & 15;
  const int pbr = s >> 3;
  STDECL();
  ST(0);
  const rsrc_t wsr = mkrs(wsp, WS_END);
  unsigned tag;
  if (tid == 64) { L.qready = 0u; L.issued = 0u; L.done = 0u; }

  if (__builtin_expect(wave == 0, 1)) {
    const int rs = lane >> 3, c = lane & 7;
    const int qbr = (w >> 1) & 1, half = (w >> 2) & 1, j = w >> 3, blk = (j >> 1) * 4 + (j & 1) * 2 + ((j ^ w) & 1);
    const rsrc_t wr = mkrs((qbr ? wqB : wqA) + (half * 288) * NQ + blk * 64, 288 * NQ * 2);
    const unsigned woff = (unsigned)(rs * 36 * NQ + c * 8) * 2;
    u32x4 wv[36], xr;
    unsigned epoch, so = 0;
#pragma unroll
    for (int t = 0; t < 36; ++t) {
      wv[t] = ldb<AUXW>(wr, woff, so); SB();
      so += NQ * 2; asm volatile("" : "+s"(so)); SB();
      if (t == XPOS) {
        xr = ldb<0>(mkrs((qbr ? xB : xA) + half * 288, 576), lane * 16, 0);
        epoch = __hip_atomic_load((GAS unsigned*)(ws + WS_EPOCH), RLX_AGENT); SB();
      }
    }
    __builtin_amdgcn_s_barrier();
    SB();
    ST(8);
    if (lane < 36) *(u32x4*)&L.x[lane * 4] = xr;
    float acc[8] = {0, 0, 0, 0, 0, 0, 0, 0};
#pragma unroll
    for (int t4 = 0; t4 < 9; ++t4) {
      const u32x2 xw = *(const u32x2*)&L.x[rs * 18 + t4 * 2];
#pragma unroll
      for (int u = 0; u < 4; ++u) {
        const int t = t4 * 4 + u;
        const unsigned xl = (u & 1) ? xw[u >> 1] >> 16 : xw[u >> 1] & 0xffffu, xh = (u & 1) ? xw[u >> 1] & 0xffff0000u : xw[u >> 1] << 16;
#pragma unroll
        for (int k = 0; k < 4; ++k) { acc[2 * k] = dot2(wv[t][k], xl, acc[2 * k]); acc[2 * k + 1] = dot2(wv[t][k], xh, acc[2 * k + 1]); }
      }
      SB();
    }
    float b[4];
#pragma unroll
    for (int k = 0; k < 4; ++k) { b[k] = fold32(acc[k], acc[k + 4]); b[k] = fold16(b[k], b[k]); b[k] += dpp<XOR8>(b[k]); }
    asm volatile("" : "+v"(epoch));
    tag = __builtin_amdgcn_readfirstlane(epoch) + 1u;
    tag += tag == 0u;
    if (!(lane & 24)) stx(wsr, (c * 2 + (lane >> 5)) * 16, WS_Q + (blk >> 2) * 4096 + (qbr * 2 + half) * 1024 + (blk & 3) * 256, seal4(b[0], b[1], b[2], b[3], tag));
    if (PRETOUCH == 3) {
      const u32x4 z = {0u, 0u, 0u, 0u};
      stx(wsr, (lane >> 3) * 2048 + (lane & 7) * 16, WS_O + h * 32768 + s * 128, z);
      stx(wsr, (lane >> 3) * 2048 + (lane & 7) * 16, WS_O + h * 32768 + 16384 + s * 128, z);
#pragma unroll
      for (int k = 0; k < 3; ++k) if (k < 2 || lane < 16) stx(wsr, (lane >> 3) * 16384 + (lane & 7) * 16, WS_Y + (pbr * 18 + 8 * k) * 16384 + (h * 8 + (s & 7)) * 128, z);
    }
    ST(1);
    __builtin_amdgcn_s_sleep(QSLEEP);
    u32x4 g[4];
    if (ABL & 1) { for (int i = 0; i < 4; ++i) g[i] = seal4(b[0], b[1], b[2], b[3], tag); asm volatile("s_waitcnt vmcnt(0)" ::: "memory"); } else
    for (unsigned spins = 0;; ++spins) {
      bool ok = true;
#pragma unroll
      for (int i = 0; i < 4; ++i) g[i] = ldx(wsr, lane * 16 + i * 1024, WS_Q + h * 4096);
#pragma unroll
      for (int i = 0; i < 4; ++i) ok &= valid(g[i], tag);
      if (__all(ok) || spins > SPIN_CAP) break;
    }
#pragma unroll
    for (int bb = 0; bb < 2; ++bb) {
      const u32x4 p0 = g[2 * bb], p1 = g[2 * bb + 1];
      *(u32x2*)&L.q[bb][2 * lane] = u32x2{cvtpk(uf(p0[0]) + uf(p1[0]), uf(p0[1]) + uf(p1[1])), cvtpk(uf(p0[2]) + uf(p1[2]), fourth(p0) + fourth(p1))};
    }
    __builtin_amdgcn_fence(__ATOMIC_RELEASE, "workgroup");
    if (lane == 0) __hip_atomic_store(&L.qready, tag, RLX_WG);
    ST(2);
  } else {
    const int br = wave == 1 || wave == 3 ? 0 : 1;
    const int pair = wave < 5 ? 3 : (wave - 5) >> 1, odd = wave < 5 ? (wave - 1) >> 1 : (wave - 5) & 1;
    const size_t off = ((size_t)h * 2048 + pair * 512 + s * 32 + odd * 2) * 1024;
    const rsrc_t kr = mkrs((const char*)(br ? kB : kA) + off, 32 * 1024), vr = mkrs((const char*)(br ? vB : vA) + off, 32 * 1024);
    asm volatile("s_waitcnt lgkmcnt(0)" ::: "memory");
    __builtin_amdgcn_s_barrier();
    SB();
    ST(8);
    const unsigned OOR = 0x40000000u;
    const unsigned pt0 = lane < 8 ? WS_O + (h * 16 + s) * 2048 + br * 1024 : OOR, pt1 = w < NRED ? WS_Y + w * 16384 + br * 8192 : OOR;
    if (wave < 3) tag = stream_wave<true>(L, kr, vr, (s + h) * 4096, wave, br, lane, wsr, pt0, pt1, WS_O + h * 32768 + br * 16384 + s * 128 STARG);
    else tag = stream_wave<false>(L, kr, vr, (s + h) * 4096, wave, br, lane, wsr, 0, 0, 0 STARG);
  }
  const int pw = wave == 0 ? 8 : wave - 3;
  u32x4 wf[4];
  if (pw >= 0) {
    lds_wait(&L.issued, 10u);
    const rsrc_t wr = mkrs((pbr ? woB : woA) + (h * 256 + (s & 7) * 32) * DM + pw * 64, 32 * DM * 2);
    const unsigned woff = ((lane >> 3) * DM + (lane & 7) * 8) * 2;
    LAS unsigned char* dst = (LAS unsigned char*)&L.wo[pw][0];
#pragma unroll
    for (int t = 0; t < 4; ++t) __builtin_amdgcn_raw_ptr_buffer_load_lds(wr, dst + t * 1024, 16, woff, t * 8 * DM * 2, 0, AUXW);
    asm volatile("s_waitcnt vmcnt(0)" ::: "memory");
    LAS unsigned char* src = dst + (lane >> 4) * 1024 + ((lane >> 2) & 3) * 128 + (lane & 3) * 8;
#pragma unroll
    for (int b = 0; b < 4; ++b) {
      const u32x2 lo = __builtin_bit_cast(u32x2, __builtin_amdgcn_ds_read_tr16_b64_v4i16((LAS s16x4*)(src + b * 32)));
      const u32x2 hi = __builtin_bit_cast(u32x2, __builtin_amdgcn_ds_read_tr16_b64_v4i16((LAS s16x4*)(src + b * 32 + 512)));
      wf[b] = u32x4{lo[0], lo[1], hi[0], hi[1]};
    }
    asm volatile("" : "+v"(wf[0]), "+v"(wf[1]), "+v"(wf[2]), "+v"(wf[3]));
    ST(5);
  } else if (ABL & 2) {
  } else if (wave == 2) {
    if (PRETOUCH == 2 && w < NRED) { const unsigned pre = __builtin_amdgcn_raw_buffer_load_b32(wsr, lane * 128, WS_Y + w * 16384, SC1) ^ __builtin_amdgcn_raw_buffer_load_b32(wsr, lane * 128 + 8192, WS_Y + w * 16384, SC1); asm volatile("" :: "v"(pre)); }
  } else {
    __builtin_amdgcn_s_sleep(FIRST1);
    u32x4 g0, g1;
    for (unsigned spins = 0;; ++spins) {
      g0 = ldx(wsr, lane * 16, WS_O + (h * 16 + s) * 2048); g1 = ldx(wsr, lane * 16 + 1024, WS_O + (h * 16 + s) * 2048);
      if (__all(valid(g0, tag) && valid(g1, tag)) || spins > SPIN_CAP) break;
      if (PS1) __builtin_amdgcn_s_sleep(PS1);
    }
    ST(10);
    float mx = fmaxf(uf(g0[2]), uf(g1[2]));
    mx = max16_32(fmaxf(mx, dpp<XOR8>(mx)));
    const float t0 = __builtin_amdgcn_exp2f(uf(g0[2]) - mx), t1 = __builtin_amdgcn_exp2f(uf(g1[2]) - mx);
    float e = t0 + t1;
    e += dpp<XOR8>(e); e = fold16(e, e); e = fold32(e, e);
    const float a0 = fmaf(t0, bflo(g0[0]), t1 * bflo(g1[0])), a1 = fmaf(t0, bfhi(g0[0]), t1 * bfhi(g1[0]));
    const float a2 = fmaf(t0, bflo(g0[1]), t1 * bflo(g1[1])), a3 = fmaf(t0, bfhi(g0[1]), t1 * bfhi(g1[1]));
    float a = fold16(fold32(a0, a2), fold32(a1, a3));
    a += dpp<XOR8>(a);
    if (!(lane & 8)) L.o[8 * (lane & 3) + (lane & 4) + (lane >> 4)] = (unsigned short)cvtpk(a * __builtin_amdgcn_rcpf(e), 0.f);
    ST(5);
  }
  if (ABL & 2) { STDUMP(); return; }
  asm volatile("s_waitcnt lgkmcnt(0)" ::: "memory");
  __builtin_amdgcn_s_barrier();
  SB();
  ST(11);
  if (pw >= 0) {
    const u32x4 ov = *(const u32x4*)&L.o[(lane >> 4) * 8];
    const f32x4 z = {0, 0, 0, 0};
    const float y0 = mfma(ov, wf[0], z)[0], y1 = mfma(ov, wf[1], z)[0], y2 = mfma(ov, wf[2], z)[0], y3 = mfma(ov, wf[3], z)[0];
    if (lane < 16) stx(wsr, (lane >> 3) * 16384 + (lane & 7) * 16, WS_Y + (pbr * 18 + pw * 2) * 16384 + (h * 8 + (s & 7)) * 128, seal4(y0, y1, y2, y3, tag));
  }
  ST(6);
  if (w >= NRED) { STDUMP(); return; }
  if (wave < 8) {
    __builtin_amdgcn_s_sleep(FIRST2);
    u32x4 g0, g1;
    for (unsigned spins = 0;; ++spins) {
      g0 = ldx(wsr, lane * 16, WS_Y + w * 16384 + wave * 2048); g1 = ldx(wsr, lane * 16 + 1024, WS_Y + w * 16384 + wave * 2048);
      if (__all(valid(g0, tag) && valid(g1, tag)) || spins > SPIN_CAP) break;
      if (PS2) __builtin_amdgcn_s_sleep(PS2);
    }
    float y = fold16(fold32(uf(g0[0]) + uf(g1[0]), uf(g0[2]) + uf(g1[2])), fold32(uf(g0[1]) + uf(g1[1]), fourth(g0) + fourth(g1)));
    y += dpp<XOR8>(y);
    if (!(lane & 8)) L.fin[wave][(lane >> 4) * 8 + (lane & 7)] = y;
  }
  asm volatile("s_waitcnt lgkmcnt(0)" ::: "memory");
  __builtin_amdgcn_s_barrier();
  SB();
  if (wave == 0) {
    const float* f = &L.fin[(lane >> 5) * 4][lane & 31];
    float y = (f[0] + f[32]) + (f[64] + f[96]);
    y = fold32(y, y);
    const float yn = dpp<XOR1>(y);
    if (lane < 32 && !(lane & 1)) ((GAS unsigned*)out)[(w >> 1) * 32 + (w & 1) * 4 + (lane >> 3) * 8 + ((lane & 7) >> 1)] = cvtpk(y, yn);
    if (w == 0 && lane == 0) __hip_atomic_store((GAS unsigned*)(ws + WS_EPOCH), tag, RLX_AGENT);
    ST(7);
  }
  STDUMP();
}

extern "C" void kernel_launch(void* const* d_in, const int* in_sizes, int n_in, void* d_out, int out_size, void* d_ws, size_t ws_size, hipStream_t stream) {
  fused_decode<<<256, NTHR, 0, stream>>>((const uint16_t*)d_in[0], (const uint16_t*)d_in[1], (const uint16_t*)d_in[2], (const uint16_t*)d_in[3], (const uint16_t*)d_in[4],
                                         (const uint16_t*)d_in[5], (const uint16_t*)d_in[6], (const uint16_t*)d_in[7], (const uint16_t*)d_in[8], (const uint16_t*)d_in[9],
                                         (uint16_t*)d_out, (unsigned char*)d_ws);
}
```

```cpp
#include <hip/hip_runtime.h>
#include <hip/hip_bf16.h>
#include <stdint.h>

#define GAS __attribute__((address_space(1)))
#define LAS __attribute__((address_space(3)))
#define RLX_AGENT __ATOMIC_RELAXED, __HIP_MEMORY_SCOPE_AGENT
#define RLX_WG __ATOMIC_RELAXED, __HIP_MEMORY_SCOPE_WORKGROUP
typedef unsigned u32x4 __attribute__((ext_vector_type(4)));
typedef unsigned u32x2 __attribute__((ext_vector_type(2)));
typedef float f32x4 __attribute__((ext_vector_type(4)));
typedef short s16x4 __attribute__((ext_vector_type(4)));
typedef __bf16 bf16x2 __attribute__((ext_vector_type(2)));
typedef __bf16 bf16x8 __attribute__((ext_vector_type(8)));
typedef unsigned long long u64;
typedef u64 u64x2 __attribute__((ext_vector_type(2)));
typedef u64 u64x4 __attribute__((ext_vector_type(4)));
typedef __amdgpu_buffer_rsrc_t rsrc_t;

constexpr int DM = 576, NQ = 4096;
constexpr int NWAVE = 11, NTHR = NWAVE * 64, NRED = 36;
constexpr unsigned WS_EPOCH = 0, WS_Q = 4096, WS_O = WS_Q + 16 * 4096, WS_Y = WS_O + 256 * 2048, WS_END = WS_Y + NRED * 16384;
constexpr size_t WS_STAMP = 8u << 20;
constexpr unsigned SPIN_CAP = 1u << 17;

#define FIRST1 4
#define FIRST2 16
#define QSLEEP 64
#define XPOS 7
#define HSH 4
#define MID 8
#define HFIRST 1
#define GW 1
#define KWARM 1
#define CPF2 0
#define CPF 0
#define KAFIRST 1
#define VORD 1
#define OUTSC 0
constexpr int NT = 2;
constexpr unsigned OOR = 0x40000000u;

__device__ __forceinline__ float bflo(unsigned u) { return __builtin_bit_cast(float, u << 16); }
__device__ __forceinline__ float bfhi(unsigned u) { return __builtin_bit_cast(float, u & 0xffff0000u); }
__device__ __forceinline__ unsigned fu(float f) { return __float_as_uint(f); }
__device__ __forceinline__ float uf(unsigned u) { return __uint_as_float(u); }
__device__ __forceinline__ unsigned cvtpk(float lo, float hi) { bf16x2 v = {(__bf16)lo, (__bf16)hi}; return __builtin_bit_cast(unsigned, v); }
__device__ __forceinline__ float dot2(unsigned k, unsigned q, float acc) {
  return __builtin_amdgcn_fdot2_f32_bf16(__builtin_bit_cast(bf16x2, k), __builtin_bit_cast(bf16x2, q), acc, false);
}
template <int CTRL> __device__ __forceinline__ float dpp(float x) {
  return __builtin_bit_cast(float, __builtin_amdgcn_mov_dpp(__builtin_bit_cast(int, x), CTRL, 0xf, 0xf, true));
}
constexpr int XOR1 = 0xB1, XOR2 = 0x4E, XOR7 = 0x141, XOR8 = 0x128;
__device__ __forceinline__ f32x4 mfma(u32x4 a, u32x4 b, f32x4 c) {
  return __builtin_amdgcn_mfma_f32_16x16x32_bf16(__builtin_bit_cast(bf16x8, a), __builtin_bit_cast(bf16x8, b), c, 0, 0, 0);
}
__device__ __forceinline__ float fold32(float a, float b) {
  auto r = __builtin_amdgcn_permlane32_swap(fu(a), fu(b), false, false);
  return uf(r[0]) + uf(r[1]);
}
__device__ __forceinline__ float fold16(float a, float b) {
  auto r = __builtin_amdgcn_permlane16_swap(fu(a), fu(b), false, false);
  return uf(r[0]) + uf(r[1]);
}
__device__ __forceinline__ float fmax2(float a, float b) { return __builtin_amdgcn_fmed3f(a, b, INFINITY); }
__device__ __forceinline__ float max16_32(float x) {
  auto s = __builtin_amdgcn_permlane16_swap(fu(x), fu(x), false, false);
  x = fmax2(uf(s[0]), uf(s[1]));
  auto t = __builtin_amdgcn_permlane32_swap(fu(x), fu(x), false, false);
  return fmax2(uf(t[0]), uf(t[1]));
}
__device__ __forceinline__ rsrc_t mkrs(const void* p, unsigned bytes) { return __builtin_amdgcn_make_buffer_rsrc((void*)p, 0, bytes, 0x00020000); }
template <int AUX> __device__ __forceinline__ u32x4 ldb(rsrc_t r, unsigned voff, unsigned soff) { return __builtin_amdgcn_raw_buffer_load_b128(r, voff, soff, AUX); }
constexpr int SC1 = 16;
__device__ __forceinline__ u32x4 ldx(rsrc_t r, unsigned voff, unsigned soff) { asm volatile("" ::: "memory"); return __builtin_amdgcn_raw_buffer_load_b128(r, voff, soff, SC1); }
__device__ __forceinline__ void stx(rsrc_t r, unsigned voff, unsigned soff, u32x4 v) { __builtin_amdgcn_raw_buffer_store_b128(v, r, voff, soff, SC1); }
__device__ __forceinline__ unsigned xor3(unsigned a, unsigned b, unsigned c) { return __builtin_amdgcn_bitop3_b32(a, b, c, 0x96); }
__device__ __forceinline__ u32x4 seal(unsigned a, unsigned b, unsigned c, unsigned tag) { return u32x4{a, b, c, xor3(a, b, c ^ tag)}; }
__device__ __forceinline__ unsigned bad(u32x4 g, unsigned tag) { return xor3(xor3(g[0], g[1], g[2]), g[3], tag); }
__device__ __forceinline__ u32x4 seal4(float a, float b, float c, float d, unsigned tag) {
  return seal(__builtin_amdgcn_perm(fu(a), fu(d), 0x07060501u), __builtin_amdgcn_perm(fu(b), fu(d), 0x07060502u), __builtin_amdgcn_perm(fu(c), fu(d), 0x07060503u), tag);
}
__device__ __forceinline__ float fourth(u32x4 g) { return uf(__builtin_amdgcn_perm(g[2], __builtin_amdgcn_perm(g[1], g[0], 0x0c04000cu), 0x04020100u)); }
#define SB() __builtin_amdgcn_sched_barrier(0)
#define ST(i)
#define STDUMP()
#define STDECL()
#define STPAR
#define STARG

struct Lds {
  unsigned char wo[9][4096];
  float part[11][2][132];
  unsigned q[2][128];
  unsigned x[144];
  float fin[8][32];
  unsigned short o[32];
  unsigned qready, kissued, issued, done[2];
};

__device__ __forceinline__ void lds_wait(unsigned* p, unsigned n) {
  for (unsigned spins = 0; __hip_atomic_load(p, RLX_WG) < n && spins < (SPIN_CAP << 3); ++spins) __builtin_amdgcn_s_sleep(1);
}
__device__ __forceinline__ void lds_inc(unsigned* p, int lane) { if (lane == 0) __hip_atomic_fetch_add(p, 1u, RLX_WG); }
__device__ __forceinline__ void poll2(rsrc_t r, unsigned voff, unsigned soff, unsigned tag, u32x4& g0, u32x4& g1) {
  for (unsigned spins = 0; spins < SPIN_CAP; ++spins) {
    g0 = ldx(r, voff, soff); g1 = ldx(r, voff + 1024, soff);
    if (!__any((bad(g0, tag) | bad(g1, tag)) != 0u)) break;
  }
}

__global__ void __launch_bounds__(NTHR, 3) fused_decode(const void* kA, const void* vA, const void* wqA, const void* xA, const void* kB, const void* vB, const void* wqB, const void* xB,
                                                         const void* woA, const void* woB, void* out, void* wsp) {
  __shared__ __attribute__((aligned(16))) Lds L;
  STDECL();
  ST(0);
  const auto karg = __builtin_amdgcn_kernarg_segment_ptr();
  if (KWARM) asm volatile("s_load_dword s94, %0, 0x0\n\ts_load_dword s95, %0, 0x40" :: "s"(karg) : "s94", "s95");
  SB();
  u64x2 a2; u64x4 a4;
#define GETARGS(OFF, ...) asm volatile("s_load_dwordx4 %0, %[ka], %[off]\n\ts_load_dwordx8 %1, %[ka], 0x40\n\ts_waitcnt lgkmcnt(0)" : "=&s"(a2), "=&s"(a4) : [ka] "s"(karg), [off] "s"(OFF), __VA_ARGS__ : "memory")
  const int tid = threadIdx.x, lane = tid & 63, wave = __builtin_amdgcn_readfirstlane(tid >> 6);
  const int w = blockIdx.x, h = (w & 7) * 2 + (w >> 7), s = (w >> 3) & 15;
  const int hp = (h + HSH) & 15, pbr = s >> 3;
  unsigned tag;

  if (__builtin_expect(wave == 0, 1)) {
    const int rs = lane >> 3, c = lane & 7;
    const int qbr = (w >> 1) & 1, half = (w >> 2) & 1, j = w >> 3, blk = (j >> 1) * 4 + (j & 1) * 2 + ((j ^ w) & 1);
    unsigned woff = (unsigned)((half * 288 + rs * 36) * NQ + blk * 64 + c * 8) * 2;
    asm volatile("" : "+v"(woff));
    GETARGS(qbr * 32 + 16, "s"(half));
    const rsrc_t wr = mkrs((const void*)a2[0], DM * NQ * 2);
    const rsrc_t wsr = mkrs((const void*)a4[3], WS_END);
    u32x4 wv[36], xr;
    unsigned epoch, so = 0;
#pragma unroll
    for (int t = 0; t < 36; ++t) {
      wv[t] = ldb<NT>(wr, woff, so); SB();
      so += NQ * 2; asm volatile("" : "+s"(so)); SB();
      if (t == XPOS) {
        xr = ldb<0>(mkrs((const char*)a2[1] + half * 576, 576), lane * 16, 0);
        epoch = __hip_atomic_load((GAS unsigned*)(a4[3] + WS_EPOCH), RLX_AGENT); SB();
      }
    }
    __builtin_amdgcn_s_barrier();
    SB();
    ST(8);
    if (lane < 36) *(u32x4*)&L.x[lane * 4] = xr;
    float acc[8] = {0, 0, 0, 0, 0, 0, 0, 0};
#pragma unroll
    for (int t4 = 0; t4 < 9; ++t4) {
      const u32x2 xw = *(const u32x2*)&L.x[rs * 18 + t4 * 2];
#pragma unroll
      for (int u = 0; u < 4; ++u) {
        const int t = t4 * 4 + u;
        const unsigned xl = (u & 1) ? xw[u >> 1] >> 16 : xw[u >> 1] & 0xffffu, xh = (u & 1) ? xw[u >> 1] & 0xffff0000u : xw[u >> 1] << 16;
#pragma unroll
        for (int k = 0; k < 4; ++k) { acc[2 * k] = dot2(wv[t][k], xl, acc[2 * k]); acc[2 * k + 1] = dot2(wv[t][k], xh, acc[2 * k + 1]); }
      }
      SB();
    }
    float b[4];
#pragma unroll
    for (int k = 0; k < 4; ++k) { b[k] = fold32(acc[k], acc[k + 4]); b[k] = fold16(b[k], b[k]); b[k] += dpp<XOR8>(b[k]); }
    asm volatile("" : "+v"(epoch));
    tag = __builtin_amdgcn_readfirstlane(epoch) + 1u;
    tag += tag == 0u;
    stx(wsr, (lane & 24) ? OOR : (c * 2 + (lane >> 5)) * 16, WS_Q + (blk >> 2) * 4096 + (qbr * 2 + half) * 1024 + (blk & 3) * 256, seal4(b[0], b[1], b[2], b[3], tag));
    ST(1);
    __builtin_amdgcn_s_sleep(QSLEEP);
    u32x4 g[4];
    for (unsigned spins = 0; spins < SPIN_CAP; ++spins) {
#pragma unroll
      for (int i = 0; i < 4; ++i) g[i] = ldx(wsr, lane * 16 + i * 1024, WS_Q + h * 4096);
      if (!__any((bad(g[0], tag) | bad(g[1], tag) | bad(g[2], tag) | bad(g[3], tag)) != 0u)) break;
    }
#pragma unroll
    for (int bb = 0; bb < 2; ++bb) {
      const u32x4 p0 = g[2 * bb], p1 = g[2 * bb + 1];
      *(u32x2*)&L.q[bb][2 * lane] = u32x2{cvtpk(uf(p0[0]) + uf(p1[0]), uf(p0[1]) + uf(p1[1])), cvtpk(uf(p0[2]) + uf(p1[2]), fourth(p0) + fourth(p1))};
    }
    __builtin_amdgcn_fence(__ATOMIC_RELEASE, "workgroup");
    if (lane == 0) __hip_atomic_store(&L.qready, tag, RLX_WG);
    ST(2);
  } else {
    const int br = wave == 1 || wave == 3 ? 0 : 1;
    const int pair = wave < 5 ? 3 : (wave - 5) >> 1, odd = wave < 5 ? (wave - 1) >> 1 : (wave - 5) & 1;
    const int g = lane >> 4, i = lane & 15;
    unsigned loff = (h * 2048 + pair * 512 + s * 32 + odd * 2) * 1024 + lane * 16;
    const bool late = wave < (VORD ? 4 : 3), closer = wave < 3;
    unsigned so = ((s + h) & 7) * 4096, knead = KAFIRST && br ? 2u : 0u;
    if (tid == 64) { L.qready = 0u; L.kissued = 0u; L.issued = 0u; L.done[0] = 0u; L.done[1] = 0u; }
    unsigned cp0 = 0, cp1 = 0, cp2 = 0;
    asm volatile("" : "+v"(loff));
    asm volatile("" : "+s"(so), "+s"(knead));
    GETARGS(br * 32, "s"(so));
    if (CPF && wave == 10) {
      u64 pc; asm volatile("s_getpc_b64 %0" : "=s"(pc));
      pc = (pc & ~127ull) - CPF * 128 + (w >> 3) * 128;
      asm volatile("s_load_dword %0, %3, 0x0\n\ts_load_dword %1, %3, 0x1000\n\ts_load_dword %2, %4, 0x0" : "=&s"(cp0), "=&s"(cp1), "=&s"(cp2) : "s"(pc), "s"(pc + ((w >> 3) < CPF2 ? 8192 : 0)));
    }
    const rsrc_t kr = mkrs((const void*)a2[0], 32 << 20), vr = mkrs((const void*)a2[1], 32 << 20);
    const rsrc_t wsr = mkrs((const void*)a4[3], WS_END);
    asm volatile("" :: "s"(kr), "s"(vr));
    __builtin_amdgcn_s_barrier();
    SB();
    ST(8);
    u32x4 K[16], V[16];
#define LOAD16(X, R) _Pragma("unroll") for (int t = 0; t < 16; t += 2) { \
      X[t] = ldb<NT>(R, loff, so); SB(); unsigned nx = so + 4096u; asm volatile("" : "+s"(nx)); SB(); \
      X[t + 1] = ldb<NT>(R, loff + 1024, so); SB(); so = nx & 0x7000u; asm volatile("" : "+s"(so)); SB(); }
    if (knead) lds_wait(&L.kissued, knead);
    SB();
#pragma unroll
    for (int t = 0; t < 16; t += 2)
      asm volatile("buffer_load_dwordx4 %0, %3, %4, %2 offen nt\n\ts_nop 0\n\tbuffer_load_dwordx4 %1, %3, %4, %2 offen offset:1024 nt\n\t"
                   "s_addk_i32 %2, 0x1000\n\ts_and_b32 %2, %2, 0x7000" : "=&v"(K[t]), "=&v"(K[t + 1]), "+s"(so) : "v"(loff), "s"(kr) : "scc");
    SB();
    lds_inc(&L.kissued, lane);
    ST(9);
    if (!late) {
      lds_wait(&L.kissued, 10u);
      SB();
      LOAD16(V, vr)
      lds_inc(&L.issued, lane);
      ST(1);
      asm volatile("s_waitcnt vmcnt(16)" ::: "memory");
    } else {
      asm volatile("s_waitcnt vmcnt(0)" ::: "memory");
    }
#pragma unroll
    for (int t = 0; t < 16; ++t) asm volatile("" : "+v"(K[t]));
    lds_wait(&L.qready, 1u);
    tag = __builtin_amdgcn_readfirstlane(__hip_atomic_load(&L.qready, RLX_WG));
    __builtin_amdgcn_fence(__ATOMIC_ACQUIRE, "workgroup");
    const u32x4 q0 = *(const u32x4*)&L.q[br][4 * i], q1 = *(const u32x4*)&L.q[br][64 + 4 * i];
    constexpr float C2 = 0.088388347648318440f * 1.4426950408889634f;
    float sc[16], m = -INFINITY;
#pragma unroll
    for (int t = 0; t < 16; ++t) {
      float d0 = 0, d1 = 0;
#pragma unroll
      for (int k = 0; k < 4; ++k) { d0 = dot2(K[t][k], q0[k], d0); d1 = dot2(K[t][k], q1[k], d1); }
      d0 += dpp<XOR8>(d0); d1 += dpp<XOR8>(d1);
      float d = (i & 8) ? d1 : d0;
      d += dpp<XOR7>(d); d += dpp<XOR2>(d); d += dpp<XOR1>(d);
      sc[t] = d; m = fmaxf(m, d);
    }
    m = max16_32(m) * C2;
    unsigned msk[4];
#pragma unroll
    for (int k = 0; k < 4; ++k) msk[k] = (((i & 7) >> 1) == k) ? ((i & 1) ? 0xffff0000u : 0x0000ffffu) : 0u;
    float l = 0;
    u32x4 a[16];
#pragma unroll
    for (int t = 0; t < 16; ++t) {
      const float p = __builtin_amdgcn_exp2f(fmaf(sc[t], C2, -m));
      const unsigned pp = cvtpk(p, p);
      l += p;
      a[t] = u32x4{pp & msk[0], pp & msk[1], pp & msk[2], pp & msk[3]};
    }
    l = fold16(l, l); l = fold32(l, l);
    if ((lane & 55) == 0) *(float2*)&L.part[wave][lane >> 3][0] = make_float2(m, l);
#pragma unroll
    for (int t = 0; t < 16; ++t) asm volatile("" : "+v"(a[t]));
    SB();
    ST(2);
    if (late) {
      lds_wait(&L.issued, VORD ? (wave == 2 ? 7u : wave == 3 ? 8u : 9u) : 8u);
      SB();
      LOAD16(V, vr)
      lds_inc(&L.issued, lane);
      ST(1);
    }
    f32x4 acc = {0, 0, 0, 0};
#pragma unroll
    for (int t = 0; t < MID; ++t) acc = mfma(a[t], V[t], acc);
    asm volatile("" : "+v"(acc));
    SB();
    const int hd = g >> 1, oi = 4 + 8 * i + 4 * (g & 1);
    float f = 0; f32x4 base = {0, 0, 0, 0};
    unsigned lse = 0, poff = 0;
    if (closer) {
      lds_wait(&L.done[br], br ? 7u : 1u);
      __builtin_amdgcn_fence(__ATOMIC_ACQUIRE, "workgroup");
      const float2 me = *(const float2*)&L.part[wave][hd][0];
      float M, Ls;
      if (br) {
        float2 r[7]; f32x4 o[7];
#pragma unroll
        for (int u = 0; u < 7; ++u) { r[u] = *(const float2*)&L.part[4 + u][hd][0]; o[u] = *(const f32x4*)&L.part[4 + u][hd][oi]; }
        M = me.x;
#pragma unroll
        for (int u = 0; u < 7; ++u) M = fmax2(M, r[u].x);
        Ls = 0;
#pragma unroll
        for (int u = 0; u < 7; ++u) { const float e = __builtin_amdgcn_exp2f(r[u].x - M); Ls = fmaf(e, r[u].y, Ls); base += e * o[u]; }
      } else {
        const float2 r = *(const float2*)&L.part[3][hd][0]; const f32x4 o = *(const f32x4*)&L.part[3][hd][oi];
        M = fmax2(me.x, r.x);
        const float e = __builtin_amdgcn_exp2f(r.x - M); Ls = e * r.y; base = e * o;
      }
      f = __builtin_amdgcn_exp2f(me.x - M); Ls = fmaf(f, me.y, Ls);
      const float inv = __builtin_amdgcn_rcpf(Ls);
      f *= inv; base *= inv;
      lse = fu(M + __builtin_amdgcn_logf(Ls));
      poff = WS_O + h * 32768 + br * 16384 + s * 128 + (4 * hd + (i >> 2)) * 2048 + (4 * (g & 1) + (i & 3)) * 16;
      ST(4);
    }
    asm volatile("" : "+v"(f), "+v"(base), "+v"(lse), "+v"(poff));
    SB();
#pragma unroll
    for (int t = MID; t < 16; ++t) acc = mfma(a[t], V[t], acc);
    if (closer) {
      acc = acc * f + base;
      stx(wsr, poff, 0, seal(cvtpk(acc[0], acc[1]), cvtpk(acc[2], acc[3]), lse, tag));
    } else {
      *(f32x4*)&L.part[wave][hd][oi] = acc;
      __builtin_amdgcn_fence(__ATOMIC_RELEASE, "workgroup");
      lds_inc(&L.done[br], lane);
    }
    asm volatile("" :: "s"(cp0), "s"(cp1), "s"(cp2));
    ST(3);
  }
  const rsrc_t wsr = mkrs((const void*)a4[3], WS_END);
  GAS unsigned char* ws = (GAS unsigned char*)a4[3];
  const int gw = GW;
  const int pw = wave == 0 ? 8 : wave == (VORD ? 2 : 3) ? 0 : wave < 4 ? -1 : wave - 3;
  u32x4 wf[4];
  unsigned yoff = 0, oaddr = (lane >> 4) * 16;
  if (pw >= 0) {
    lds_wait(&L.issued, 10u);
    const rsrc_t wr = mkrs((const uint16_t*)(pbr ? a4[1] : a4[0]) + (hp * 256 + (s & 7) * 32) * DM + pw * 64, 32 * DM * 2);
    const unsigned woff = ((lane >> 3) * DM + (lane & 7) * 8) * 2;
    LAS unsigned char* dst = (LAS unsigned char*)&L.wo[pw][0];
#pragma unroll
    for (int t = 0; t < 4; ++t) __builtin_amdgcn_raw_ptr_buffer_load_lds(wr, dst + t * 1024, 16, woff, t * 8 * DM * 2, 0, NT);
    yoff = lane < 16 ? WS_Y + (pbr * 18 + pw * 2 + (lane >> 3)) * 16384 + (hp * 8 + (s & 7)) * 128 + (lane & 7) * 16 : OOR;
    asm volatile("s_waitcnt vmcnt(0)" ::: "memory");
    LAS unsigned char* src = dst + (lane >> 4) * 1024 + ((lane >> 2) & 3) * 128 + (lane & 3) * 8;
#pragma unroll
    for (int b = 0; b < 4; ++b) {
      const u32x2 lo = __builtin_bit_cast(u32x2, __builtin_amdgcn_ds_read_tr16_b64_v4i16((LAS s16x4*)(src + b * 32)));
      const u32x2 hi = __builtin_bit_cast(u32x2, __builtin_amdgcn_ds_read_tr16_b64_v4i16((LAS s16x4*)(src + b * 32 + 512)));
      wf[b] = u32x4{lo[0], lo[1], hi[0], hi[1]};
    }
    asm volatile("" : "+v"(wf[0]), "+v"(wf[1]), "+v"(wf[2]), "+v"(wf[3]));
    ST(9);
  } else if (wave == gw) {
    unsigned oidx = 8 * (lane & 3) + (lane & 4) + (lane >> 4);
    asm volatile("" : "+v"(oidx));
    __builtin_amdgcn_s_sleep(FIRST1);
    u32x4 g0, g1;
    poll2(wsr, lane * 16, WS_O + (hp * 16 + s) * 2048, tag, g0, g1);
    ST(10);
    float mx = fmax2(uf(g0[2]), uf(g1[2]));
    mx = max16_32(fmax2(mx, dpp<XOR8>(mx)));
    const float t0 = __builtin_amdgcn_exp2f(uf(g0[2]) - mx), t1 = __builtin_amdgcn_exp2f(uf(g1[2]) - mx);
    float e = t0 + t1;
    e += dpp<XOR8>(e); e = fold16(e, e); e = fold32(e, e);
    const float a0 = fmaf(t0, bflo(g0[0]), t1 * bflo(g1[0])), a1 = fmaf(t0, bfhi(g0[0]), t1 * bfhi(g1[0]));
    const float a2 = fmaf(t0, bflo(g0[1]), t1 * bflo(g1[1])), a3 = fmaf(t0, bfhi(g0[1]), t1 * bfhi(g1[1]));
    float a = fold16(fold32(a0, a2), fold32(a1, a3));
    a += dpp<XOR8>(a);
    L.o[oidx] = (unsigned short)cvtpk(a * __builtin_amdgcn_rcpf(e), 0.f);
    ST(5);
  }
  asm volatile("" : "+v"(yoff), "+v"(oaddr));
  asm volatile("s_waitcnt lgkmcnt(0)" ::: "memory");
  __builtin_amdgcn_s_barrier();
  SB();
  ST(11);
  if (pw >= 0) {
    const u32x4 ov = *(const u32x4*)((const char*)L.o + oaddr);
    const f32x4 z = {0, 0, 0, 0};
    const float y3 = mfma(ov, wf[3], z)[0], y0 = mfma(ov, wf[0], z)[0], y1 = mfma(ov, wf[1], z)[0], y2 = mfma(ov, wf[2], z)[0];
    stx(wsr, yoff, 0, seal4(y0, y1, y2, y3, tag));
  }
  ST(6);
  if (w >= NRED) { STDUMP(); return; }
  unsigned fidx = (lane >> 5) * 128 + (lane & 31), ooff = (lane & 33) ? OOR : ((w >> 1) * 32 + (w & 1) * 4 + (lane >> 3) * 8 + ((lane & 7) >> 1)) * 4;
  const rsrc_t outr = mkrs((const void*)a4[2], 2 * DM * 2);
  asm volatile("" : "+v"(fidx), "+v"(ooff));
  if (wave < 8) {
    unsigned widx = wave * 32 + (lane >> 4) * 8 + (lane & 7);
    asm volatile("" : "+v"(widx));
    __builtin_amdgcn_s_sleep(FIRST2);
    u32x4 g0, g1;
    poll2(wsr, lane * 16, WS_Y + w * 16384 + wave * 2048, tag, g0, g1);
    float y = fold16(fold32(uf(g0[0]) + uf(g1[0]), uf(g0[2]) + uf(g1[2])), fold32(uf(g0[1]) + uf(g1[1]), fourth(g0) + fourth(g1)));
    y += dpp<XOR8>(y);
    (&L.fin[0][0])[widx] = y;
  }
  asm volatile("s_waitcnt lgkmcnt(0)" ::: "memory");
  __builtin_amdgcn_s_barrier();
  SB();
  if (wave == 0) {
    const float* f = &L.fin[0][0] + fidx;
    float y = (f[0] + f[32]) + (f[64] + f[96]);
    y = fold32(y, y);
    __builtin_amdgcn_raw_buffer_store_b32(cvtpk(y, dpp<XOR1>(y)), outr, ooff, 0, OUTSC);
    ST(7);
  } else if (w == 0 && tid == 64) {
    __hip_atomic_store((GAS unsigned*)(ws + WS_EPOCH), tag, RLX_AGENT);
  }
  STDUMP();
}

extern "C" void kernel_launch(void* const* d_in, const int* in_sizes, int n_in, void* d_out, int out_size, void* d_ws, size_t ws_size, hipStream_t stream) {
  fused_decode<<<256, NTHR, 0, stream>>>(d_in[1], d_in[2], d_in[3], d_in[0], d_in[6], d_in[7], d_in[8], d_in[5], d_in[4], d_in[9], d_out, d_ws);
}
```

```cpp
#define PF1 1
#define PF2 1
#define STAMP
#define PM1 2
#define PM2 1
#define PW1 0
#define PK1 0
#define PW2 0
#define PK2 8
#define PSC 8
#include <hip/hip_runtime.h>
#include <hip/hip_bf16.h>
#include <stdint.h>

#define GAS __attribute__((address_space(1)))
#define LAS __attribute__((address_space(3)))
#define RLX_AGENT __ATOMIC_RELAXED, __HIP_MEMORY_SCOPE_AGENT
#define RLX_WG __ATOMIC_RELAXED, __HIP_MEMORY_SCOPE_WORKGROUP
typedef unsigned u32x4 __attribute__((ext_vector_type(4)));
typedef unsigned u32x2 __attribute__((ext_vector_type(2)));
typedef float f32x4 __attribute__((ext_vector_type(4)));
typedef short s16x4 __attribute__((ext_vector_type(4)));
typedef __bf16 bf16x2 __attribute__((ext_vector_type(2)));
typedef __bf16 bf16x8 __attribute__((ext_vector_type(8)));
typedef unsigned long long u64;
typedef u64 u64x2 __attribute__((ext_vector_type(2)));
typedef u64 u64x4 __attribute__((ext_vector_type(4)));
typedef __amdgpu_buffer_rsrc_t rsrc_t;

constexpr int DM = 576, NQ = 4096;
constexpr int NWAVE = 11, NTHR = NWAVE * 64, NRED = 36;
constexpr unsigned WS_EPOCH = 0, WS_Q = 4096, WS_O = WS_Q + 16 * 4096, WS_Y = WS_O + 256 * 2048, WS_END = WS_Y + NRED * 16384;
constexpr size_t WS_STAMP = 8u << 20;
constexpr unsigned SPIN_CAP = 1u << 17;

#define FIRST1 4
#define FIRST2 16
#define QSLEEP 64
#define XPOS 7
#define HSH 4
#define MID 8
#define HFIRST 1
#define GW 1
#define KWARM 1
#define CPF2 0
#define CPF 0
#define KAFIRST 1
#define VORD 1
#define OUTSC 0
constexpr int NT = 2;
constexpr unsigned OOR = 0x40000000u;

__device__ __forceinline__ float bflo(unsigned u) { return __builtin_bit_cast(float, u << 16); }
__device__ __forceinline__ float bfhi(unsigned u) { return __builtin_bit_cast(float, u & 0xffff0000u); }
__device__ __forceinline__ unsigned fu(float f) { return __float_as_uint(f); }
__device__ __forceinline__ float uf(unsigned u) { return __uint_as_float(u); }
__device__ __forceinline__ unsigned cvtpk(float lo, float hi) { bf16x2 v = {(__bf16)lo, (__bf16)hi}; return __builtin_bit_cast(unsigned, v); }
__device__ __forceinline__ float dot2(unsigned k, unsigned q, float acc) {
  return __builtin_amdgcn_fdot2_f32_bf16(__builtin_bit_cast(bf16x2, k), __builtin_bit_cast(bf16x2, q), acc, false);
}
template <int CTRL> __device__ __forceinline__ float dpp(float x) {
  return __builtin_bit_cast(float, __builtin_amdgcn_mov_dpp(__builtin_bit_cast(int, x), CTRL, 0xf, 0xf, true));
}
constexpr int XOR1 = 0xB1, XOR2 = 0x4E, XOR7 = 0x141, XOR8 = 0x128;
__device__ __forceinline__ f32x4 mfma(u32x4 a, u32x4 b, f32x4 c) {
  return __builtin_amdgcn_mfma_f32_16x16x32_bf16(__builtin_bit_cast(bf16x8, a), __builtin_bit_cast(bf16x8, b), c, 0, 0, 0);
}
__device__ __forceinline__ float fold32(float a, float b) {
  auto r = __builtin_amdgcn_permlane32_swap(fu(a), fu(b), false, false);
  return uf(r[0]) + uf(r[1]);
}
__device__ __forceinline__ float fold16(float a, float b) {
  auto r = __builtin_amdgcn_permlane16_swap(fu(a), fu(b), false, false);
  return uf(r[0]) + uf(r[1]);
}
__device__ __forceinline__ float fmax2(float a, float b) { return __builtin_amdgcn_fmed3f(a, b, INFINITY); }
__device__ __forceinline__ float max16_32(float x) {
  auto s = __builtin_amdgcn_permlane16_swap(fu(x), fu(x), false, false);
  x = fmax2(uf(s[0]), uf(s[1]));
  auto t = __builtin_amdgcn_permlane32_swap(fu(x), fu(x), false, false);
  return fmax2(uf(t[0]), uf(t[1]));
}
__device__ __forceinline__ rsrc_t mkrs(const void* p, unsigned bytes) { return __builtin_amdgcn_make_buffer_rsrc((void*)p, 0, bytes, 0x00020000); }
template <int AUX> __device__ __forceinline__ u32x4 ldb(rsrc_t r, unsigned voff, unsigned soff) { return __builtin_amdgcn_raw_buffer_load_b128(r, voff, soff, AUX); }
constexpr int SC1 = 16;
__device__ __forceinline__ u32x4 ldx(rsrc_t r, unsigned voff, unsigned soff) { asm volatile("" ::: "memory"); return __builtin_amdgcn_raw_buffer_load_b128(r, voff, soff, SC1); }
__device__ __forceinline__ void stx(rsrc_t r, unsigned voff, unsigned soff, u32x4 v) { __builtin_amdgcn_raw_buffer_store_b128(v, r, voff, soff, SC1); }
__device__ __forceinline__ unsigned xor3(unsigned a, unsigned b, unsigned c) { return __builtin_amdgcn_bitop3_b32(a, b, c, 0x96); }
__device__ __forceinline__ u32x4 seal(unsigned a, unsigned b, unsigned c, unsigned tag) { return u32x4{a, b, c, xor3(a, b, c ^ tag)}; }
__device__ __forceinline__ unsigned bad(u32x4 g, unsigned tag) { return xor3(xor3(g[0], g[1], g[2]), g[3], tag); }
__device__ __forceinline__ u32x4 seal4(float a, float b, float c, float d, unsigned tag) {
  return seal(__builtin_amdgcn_perm(fu(a), fu(d), 0x07060501u), __builtin_amdgcn_perm(fu(b), fu(d), 0x07060502u), __builtin_amdgcn_perm(fu(c), fu(d), 0x07060503u), tag);
}
__device__ __forceinline__ float fourth(u32x4 g) { return uf(__builtin_amdgcn_perm(g[2], __builtin_amdgcn_perm(g[1], g[0], 0x0c04000cu), 0x04020100u)); }
#define SB() __builtin_amdgcn_sched_barrier(0)
#define ST(i) do { SB(); u64 t_; asm volatile("s_memrealtime %0\n\ts_waitcnt lgkmcnt(0)" : "=s"(t_) :: "memory"); stp[i] = (unsigned)t_; SB(); } while (0)
#define PAGG(M, mn, mx, sm, c) (M == 0 ? mx : M == 1 ? sm / max(c, 1) : mn)
#define STDUMP() do { GAS u64* pg = (GAS u64*)(ws + WS_STAMP); \
  if (lane == 0) { if (wave == 0) __hip_atomic_store(pg + blockIdx.x, ((u64)tag << 32) | stp[0], RLX_AGENT); \
    if (wave == PW1) __hip_atomic_store(pg + 256 + blockIdx.x, ((u64)tag << 32) | ((PF1) ? stp[PK1] : 0u), RLX_AGENT); \
    if (wave == PW2) __hip_atomic_store(pg + 512 + blockIdx.x, ((u64)tag << 32) | ((PF2) ? stp[PK2] : 0u), RLX_AGENT); } \
  if (blockIdx.x == 0 && wave == 0) { u64 a[12]; \
    for (unsigned sp = 0; sp < SPIN_CAP; ++sp) { bool ok = true; for (int i_ = 0; i_ < 12; ++i_) { a[i_] = __hip_atomic_load(pg + lane + 64 * i_, RLX_AGENT); ok &= (unsigned)(a[i_] >> 32) == tag; } if (__all(ok)) break; } \
    int mn[3], mx[3], sm[3], cn[3]; \
    for (int q_ = 0; q_ < 3; ++q_) { mn[q_] = 0x7fffffff; mx[q_] = -0x7fffffff; sm[q_] = 0; cn[q_] = 0; \
      for (int i_ = 0; i_ < 4; ++i_) { const unsigned v_ = (unsigned)a[q_ * 4 + i_]; if (v_) { const int d_ = (int)(v_ - stp[0]); mn[q_] = min(mn[q_], d_); mx[q_] = max(mx[q_], d_); sm[q_] += d_; cn[q_]++; } } \
      for (int o_ = 1; o_ < 64; o_ <<= 1) { mn[q_] = min(mn[q_], __shfl_xor(mn[q_], o_)); mx[q_] = max(mx[q_], __shfl_xor(mx[q_], o_)); sm[q_] += __shfl_xor(sm[q_], o_); cn[q_] += __shfl_xor(cn[q_], o_); } } \
    const int X = PAGG(PM2, mn[2], mx[2], sm[2], cn[2]) - PAGG(PM1, mn[1], mx[1], sm[1], cn[1]); \
    const unsigned until = stp[0] + mn[0] + 6000 + PSC * X; \
    for (unsigned sp = 0; sp < 1000000; ++sp) { if ((int)((unsigned)__builtin_amdgcn_s_memrealtime() - until) >= 0) break; } } } while (0)
#define STDECL() unsigned stp[12] = {0, 0, 0, 0, 0, 0, 0, 0, 0, 0, 0, 0}
#define STPAR , unsigned (&stp)[12]
#define STARG , stp

struct Lds {
  unsigned char wo[9][4096];
  float part[11][2][132];
  unsigned q[2][128];
  unsigned x[144];
  float fin[8][32];
  unsigned short o[32];
  unsigned qready, kissued, issued, done[2];
};

__device__ __forceinline__ void lds_wait(unsigned* p, unsigned n) {
  for (unsigned spins = 0; __hip_atomic_load(p, RLX_WG) < n && spins < (SPIN_CAP << 3); ++spins) __builtin_amdgcn_s_sleep(1);
}
__device__ __forceinline__ void lds_inc(unsigned* p, int lane) { if (lane == 0) __hip_atomic_fetch_add(p, 1u, RLX_WG); }
__device__ __forceinline__ void poll2(rsrc_t r, unsigned voff, unsigned soff, unsigned tag, u32x4& g0, u32x4& g1) {
  for (unsigned spins = 0; spins < SPIN_CAP; ++spins) {
    g0 = ldx(r, voff, soff); g1 = ldx(r, voff + 1024, soff);
    if (!__any((bad(g0, tag) | bad(g1, tag)) != 0u)) break;
  }
}

__global__ void __launch_bounds__(NTHR, 3) fused_decode(const void* kA, const void* vA, const void* wqA, const void* xA, const void* kB, const void* vB, const void* wqB, const void* xB,
                                                         const void* woA, const void* woB, void* out, void* wsp) {
  __shared__ __attribute__((aligned(16))) Lds L;
  STDECL();
  ST(0);
  const auto karg = __builtin_amdgcn_kernarg_segment_ptr();
  if (KWARM) asm volatile("s_load_dword s94, %0, 0x0\n\ts_load_dword s95, %0, 0x40" :: "s"(karg) : "s94", "s95");
  SB();
  u64x2 a2; u64x4 a4;
#define GETARGS(OFF, ...) asm volatile("s_load_dwordx4 %0, %[ka], %[off]\n\ts_load_dwordx8 %1, %[ka], 0x40\n\ts_waitcnt lgkmcnt(0)" : "=&s"(a2), "=&s"(a4) : [ka] "s"(karg), [off] "s"(OFF), __VA_ARGS__ : "memory")
  const int tid = threadIdx.x, lane = tid & 63, wave = __builtin_amdgcn_readfirstlane(tid >> 6);
  const int w = blockIdx.x, h = (w & 7) * 2 + (w >> 7), s = (w >> 3) & 15;
  const int hp = (h + HSH) & 15, pbr = s >> 3;
  unsigned tag;

  if (__builtin_expect(wave == 0, 1)) {
    const int rs = lane >> 3, c = lane & 7;
    const int qbr = (w >> 1) & 1, half = (w >> 2) & 1, j = w >> 3, blk = (j >> 1) * 4 + (j & 1) * 2 + ((j ^ w) & 1);
    unsigned woff = (unsigned)((half * 288 + rs * 36) * NQ + blk * 64 + c * 8) * 2;
    asm volatile("" : "+v"(woff));
    GETARGS(qbr * 32 + 16, "s"(half));
    const rsrc_t wr = mkrs((const void*)a2[0], DM * NQ * 2);
    const rsrc_t wsr = mkrs((const void*)a4[3], WS_END);
    u32x4 wv[36], xr;
    unsigned epoch, so = 0;
#pragma unroll
    for (int t = 0; t < 36; ++t) {
      wv[t] = ldb<NT>(wr, woff, so); SB();
      so += NQ * 2; asm volatile("" : "+s"(so)); SB();
      if (t == XPOS) {
        xr = ldb<0>(mkrs((const char*)a2[1] + half * 576, 576), lane * 16, 0);
        epoch = __hip_atomic_load((GAS unsigned*)(a4[3] + WS_EPOCH), RLX_AGENT); SB();
      }
    }
    __builtin_amdgcn_s_barrier();
    SB();
    ST(8);
    if (lane < 36) *(u32x4*)&L.x[lane * 4] = xr;
    float acc[8] = {0, 0, 0, 0, 0, 0, 0, 0};
#pragma unroll
    for (int t4 = 0; t4 < 9; ++t4) {
      const u32x2 xw = *(const u32x2*)&L.x[rs * 18 + t4 * 2];
#pragma unroll
      for (int u = 0; u < 4; ++u) {
        const int t = t4 * 4 + u;
        const unsigned xl = (u & 1) ? xw[u >> 1] >> 16 : xw[u >> 1] & 0xffffu, xh = (u & 1) ? xw[u >> 1] & 0xffff0000u : xw[u >> 1] << 16;
#pragma unroll
        for (int k = 0; k < 4; ++k) { acc[2 * k] = dot2(wv[t][k], xl, acc[2 * k]); acc[2 * k + 1] = dot2(wv[t][k], xh, acc[2 * k + 1]); }
      }
      SB();
    }
    float b[4];
#pragma unroll
    for (int k = 0; k < 4; ++k) { b[k] = fold32(acc[k], acc[k + 4]); b[k] = fold16(b[k], b[k]); b[k] += dpp<XOR8>(b[k]); }
    asm volatile("" : "+v"(epoch));
    tag = __builtin_amdgcn_readfirstlane(epoch) + 1u;
    tag += tag == 0u;
    stx(wsr, (lane & 24) ? OOR : (c * 2 + (lane >> 5)) * 16, WS_Q + (blk >> 2) * 4096 + (qbr * 2 + half) * 1024 + (blk & 3) * 256, seal4(b[0], b[1], b[2], b[3], tag));
    ST(1);
    __builtin_amdgcn_s_sleep(QSLEEP);
    u32x4 g[4];
    for (unsigned spins = 0; spins < SPIN_CAP; ++spins) {
#pragma unroll
      for (int i = 0; i < 4; ++i) g[i] = ldx(wsr, lane * 16 + i * 1024, WS_Q + h * 4096);
      if (!__any((bad(g[0], tag) | bad(g[1], tag) | bad(g[2], tag) | bad(g[3], tag)) != 0u)) break;
    }
#pragma unroll
    for (int bb = 0; bb < 2; ++bb) {
      const u32x4 p0 = g[2 * bb], p1 = g[2 * bb + 1];
      *(u32x2*)&L.q[bb][2 * lane] = u32x2{cvtpk(uf(p0[0]) + uf(p1[0]), uf(p0[1]) + uf(p1[1])), cvtpk(uf(p0[2]) + uf(p1[2]), fourth(p0) + fourth(p1))};
    }
    __builtin_amdgcn_fence(__ATOMIC_RELEASE, "workgroup");
    if (lane == 0) __hip_atomic_store(&L.qready, tag, RLX_WG);
    ST(2);
  } else {
    const int br = wave == 1 || wave == 3 ? 0 : 1;
    const int pair = wave < 5 ? 3 : (wave - 5) >> 1, odd = wave < 5 ? (wave - 1) >> 1 : (wave - 5) & 1;
    const int g = lane >> 4, i = lane & 15;
    unsigned loff = (h * 2048 + pair * 512 + s * 32 + odd * 2) * 1024 + lane * 16;
    const bool late = wave < (VORD ? 4 : 3), closer = wave < 3;
    unsigned so = ((s + h) & 7) * 4096, knead = KAFIRST && br ? 2u : 0u;
    if (tid == 64) { L.qready = 0u; L.kissued = 0u; L.issued = 0u; L.done[0] = 0u; L.done[1] = 0u; }
    unsigned cp0 = 0, cp1 = 0, cp2 = 0;
    asm volatile("" : "+v"(loff));
    asm volatile("" : "+s"(so), "+s"(knead));
    GETARGS(br * 32, "s"(so));
    if (CPF && wave == 10) {
      u64 pc; asm volatile("s_getpc_b64 %0" : "=s"(pc));
      pc = (pc & ~127ull) - CPF * 128 + (w >> 3) * 128;
      asm volatile("s_load_dword %0, %3, 0x0\n\ts_load_dword %1, %3, 0x1000\n\ts_load_dword %2, %4, 0x0" : "=&s"(cp0), "=&s"(cp1), "=&s"(cp2) : "s"(pc), "s"(pc + ((w >> 3) < CPF2 ? 8192 : 0)));
    }
    const rsrc_t kr = mkrs((const void*)a2[0], 32 << 20), vr = mkrs((const void*)a2[1], 32 << 20);
    const rsrc_t wsr = mkrs((const void*)a4[3], WS_END);
    asm volatile("" :: "s"(kr), "s"(vr));
    __builtin_amdgcn_s_barrier();
    SB();
    ST(8);
    u32x4 K[16], V[16];
#define LOAD16(X, R) _Pragma("unroll") for (int t = 0; t < 16; t += 2) { \
      X[t] = ldb<NT>(R, loff, so); SB(); unsigned nx = so + 4096u; asm volatile("" : "+s"(nx)); SB(); \
      X[t + 1] = ldb<NT>(R, loff + 1024, so); SB(); so = nx & 0x7000u; asm volatile("" : "+s"(so)); SB(); }
    if (knead) lds_wait(&L.kissued, knead);
    SB();
#pragma unroll
    for (int t = 0; t < 16; t += 2)
      asm volatile("buffer_load_dwordx4 %0, %3, %4, %2 offen nt\n\ts_nop 0\n\tbuffer_load_dwordx4 %1, %3, %4, %2 offen offset:1024 nt\n\t"
                   "s_addk_i32 %2, 0x1000\n\ts_and_b32 %2, %2, 0x7000" : "=&v"(K[t]), "=&v"(K[t + 1]), "+s"(so) : "v"(loff), "s"(kr) : "scc");
    SB();
    lds_inc(&L.kissued, lane);
    ST(9);
    if (!late) {
      lds_wait(&L.kissued, 10u);
      SB();
      LOAD16(V, vr)
      lds_inc(&L.issued, lane);
      ST(1);
      asm volatile("s_waitcnt vmcnt(16)" ::: "memory");
    } else {
      asm volatile("s_waitcnt vmcnt(0)" ::: "memory");
    }
#pragma unroll
    for (int t = 0; t < 16; ++t) asm volatile("" : "+v"(K[t]));
    lds_wait(&L.qready, 1u);
    tag = __builtin_amdgcn_readfirstlane(__hip_atomic_load(&L.qready, RLX_WG));
    __builtin_amdgcn_fence(__ATOMIC_ACQUIRE, "workgroup");
    const u32x4 q0 = *(const u32x4*)&L.q[br][4 * i], q1 = *(const u32x4*)&L.q[br][64 + 4 * i];
    constexpr float C2 = 0.088388347648318440f * 1.4426950408889634f;
    float sc[16], m = -INFINITY;
#pragma unroll
    for (int t = 0; t < 16; ++t) {
      float d0 = 0, d1 = 0;
#pragma unroll
      for (int k = 0; k < 4; ++k) { d0 = dot2(K[t][k], q0[k], d0); d1 = dot2(K[t][k], q1[k], d1); }
      d0 += dpp<XOR8>(d0); d1 += dpp<XOR8>(d1);
      float d = (i & 8) ? d1 : d0;
      d += dpp<XOR7>(d); d += dpp<XOR2>(d); d += dpp<XOR1>(d);
      sc[t] = d; m = fmaxf(m, d);
    }
    m = max16_32(m) * C2;
    unsigned msk[4];
#pragma unroll
    for (int k = 0; k < 4; ++k) msk[k] = (((i & 7) >> 1) == k) ? ((i & 1) ? 0xffff0000u : 0x0000ffffu) : 0u;
    float l = 0;
    u32x4 a[16];
#pragma unroll
    for (int t = 0; t < 16; ++t) {
      const float p = __builtin_amdgcn_exp2f(fmaf(sc[t], C2, -m));
      const unsigned pp = cvtpk(p, p);
      l += p;
      a[t] = u32x4{pp & msk[0], pp & msk[1], pp & msk[2], pp & msk[3]};
    }
    l = fold16(l, l); l = fold32(l, l);
    if ((lane & 55) == 0) *(float2*)&L.part[wave][lane >> 3][0] = make_float2(m, l);
#pragma unroll
    for (int t = 0; t < 16; ++t) asm volatile("" : "+v"(a[t]));
    SB();
    ST(2);
    if (late) {
      lds_wait(&L.issued, VORD ? (wave == 2 ? 7u : wave == 3 ? 8u : 9u) : 8u);
      SB();
      LOAD16(V, vr)
      lds_inc(&L.issued, lane);
      ST(1);
    }
    f32x4 acc = {0, 0, 0, 0};
#pragma unroll
    for (int t = 0; t < MID; ++t) acc = mfma(a[t], V[t], acc);
    asm volatile("" : "+v"(acc));
    SB();
    const int hd = g >> 1, oi = 4 + 8 * i + 4 * (g & 1);
    float f = 0; f32x4 base = {0, 0, 0, 0};
    unsigned lse = 0, poff = 0;
    if (closer) {
      lds_wait(&L.done[br], br ? 7u : 1u);
      __builtin_amdgcn_fence(__ATOMIC_ACQUIRE, "workgroup");
      const float2 me = *(const float2*)&L.part[wave][hd][0];
      float M, Ls;
      if (br) {
        float2 r[7]; f32x4 o[7];
#pragma unroll
        for (int u = 0; u < 7; ++u) { r[u] = *(const float2*)&L.part[4 + u][hd][0]; o[u] = *(const f32x4*)&L.part[4 + u][hd][oi]; }
        M = me.x;
#pragma unroll
        for (int u = 0; u < 7; ++u) M = fmax2(M, r[u].x);
        Ls = 0;
#pragma unroll
        for (int u = 0; u < 7; ++u) { const float e = __builtin_amdgcn_exp2f(r[u].x - M); Ls = fmaf(e, r[u].y, Ls); base += e * o[u]; }
      } else {
        const float2 r = *(const float2*)&L.part[3][hd][0]; const f32x4 o = *(const f32x4*)&L.part[3][hd][oi];
        M = fmax2(me.x, r.x);
        const float e = __builtin_amdgcn_exp2f(r.x - M); Ls = e * r.y; base = e * o;
      }
      f = __builtin_amdgcn_exp2f(me.x - M); Ls = fmaf(f, me.y, Ls);
      const float inv = __builtin_amdgcn_rcpf(Ls);
      f *= inv; base *= inv;
      lse = fu(M + __builtin_amdgcn_logf(Ls));
      poff = WS_O + h * 32768 + br * 16384 + s * 128 + (4 * hd + (i >> 2)) * 2048 + (4 * (g & 1) + (i & 3)) * 16;
      ST(4);
    }
    asm volatile("" : "+v"(f), "+v"(base), "+v"(lse), "+v"(poff));
    SB();
#pragma unroll
    for (int t = MID; t < 16; ++t) acc = mfma(a[t], V[t], acc);
    if (closer) {
      acc = acc * f + base;
      stx(wsr, poff, 0, seal(cvtpk(acc[0], acc[1]), cvtpk(acc[2], acc[3]), lse, tag));
    } else {
      *(f32x4*)&L.part[wave][hd][oi] = acc;
      __builtin_amdgcn_fence(__ATOMIC_RELEASE, "workgroup");
      lds_inc(&L.done[br], lane);
    }
    asm volatile("" :: "s"(cp0), "s"(cp1), "s"(cp2));
    ST(3);
  }
  const rsrc_t wsr = mkrs((const void*)a4[3], WS_END);
  GAS unsigned char* ws = (GAS unsigned char*)a4[3];
  const int gw = GW;
  const int pw = wave == 0 ? 8 : wave == (VORD ? 2 : 3) ? 0 : wave < 4 ? -1 : wave - 3;
  u32x4 wf[4];
  unsigned yoff = 0, oaddr = (lane >> 4) * 16;
  if (pw >= 0) {
    lds_wait(&L.issued, 10u);
    const rsrc_t wr = mkrs((const uint16_t*)(pbr ? a4[1] : a4[0]) + (hp * 256 + (s & 7) * 32) * DM + pw * 64, 32 * DM * 2);
    const unsigned woff = ((lane >> 3) * DM + (lane & 7) * 8) * 2;
    LAS unsigned char* dst = (LAS unsigned char*)&L.wo[pw][0];
#pragma unroll
    for (int t = 0; t < 4; ++t) __builtin_amdgcn_raw_ptr_buffer_load_lds(wr, dst + t * 1024, 16, woff, t * 8 * DM * 2, 0, NT);
    yoff = lane < 16 ? WS_Y + (pbr * 18 + pw * 2 + (lane >> 3)) * 16384 + (hp * 8 + (s & 7)) * 128 + (lane & 7) * 16 : OOR;
    asm volatile("s_waitcnt vmcnt(0)" ::: "memory");
    LAS unsigned char* src = dst + (lane >> 4) * 1024 + ((lane >> 2) & 3) * 128 + (lane & 3) * 8;
#pragma unroll
    for (int b = 0; b < 4; ++b) {
      const u32x2 lo = __builtin_bit_cast(u32x2, __builtin_amdgcn_ds_read_tr16_b64_v4i16((LAS s16x4*)(src + b * 32)));
      const u32x2 hi = __builtin_bit_cast(u32x2, __builtin_amdgcn_ds_read_tr16_b64_v4i16((LAS s16x4*)(src + b * 32 + 512)));
      wf[b] = u32x4{lo[0], lo[1], hi[0], hi[1]};
    }
    asm volatile("" : "+v"(wf[0]), "+v"(wf[1]), "+v"(wf[2]), "+v"(wf[3]));
    ST(9);
  } else if (wave == gw) {
    unsigned oidx = 8 * (lane & 3) + (lane & 4) + (lane >> 4);
    asm volatile("" : "+v"(oidx));
    __builtin_amdgcn_s_sleep(FIRST1);
    u32x4 g0, g1;
    poll2(wsr, lane * 16, WS_O + (hp * 16 + s) * 2048, tag, g0, g1);
    ST(10);
    float mx = fmax2(uf(g0[2]), uf(g1[2]));
    mx = max16_32(fmax2(mx, dpp<XOR8>(mx)));
    const float t0 = __builtin_amdgcn_exp2f(uf(g0[2]) - mx), t1 = __builtin_amdgcn_exp2f(uf(g1[2]) - mx);
    float e = t0 + t1;
    e += dpp<XOR8>(e); e = fold16(e, e); e = fold32(e, e);
    const float a0 = fmaf(t0, bflo(g0[0]), t1 * bflo(g1[0])), a1 = fmaf(t0, bfhi(g0[0]), t1 * bfhi(g1[0]));
    const float a2 = fmaf(t0, bflo(g0[1]), t1 * bflo(g1[1])), a3 = fmaf(t0, bfhi(g0[1]), t1 * bfhi(g1[1]));
    float a = fold16(fold32(a0, a2), fold32(a1, a3));
    a += dpp<XOR8>(a);
    L.o[oidx] = (unsigned short)cvtpk(a * __builtin_amdgcn_rcpf(e), 0.f);
    ST(5);
  }
  asm volatile("" : "+v"(yoff), "+v"(oaddr));
  asm volatile("s_waitcnt lgkmcnt(0)" ::: "memory");
  __builtin_amdgcn_s_barrier();
  SB();
  ST(11);
  if (pw >= 0) {
    const u32x4 ov = *(const u32x4*)((const char*)L.o + oaddr);
    const f32x4 z = {0, 0, 0, 0};
    const float y3 = mfma(ov, wf[3], z)[0], y0 = mfma(ov, wf[0], z)[0], y1 = mfma(ov, wf[1], z)[0], y2 = mfma(ov, wf[2], z)[0];
    stx(wsr, yoff, 0, seal4(y0, y1, y2, y3, tag));
  }
  ST(6);
  if (w >= NRED) { STDUMP(); return; }
  unsigned fidx = (lane >> 5) * 128 + (lane & 31), ooff = (lane & 33) ? OOR : ((w >> 1) * 32 + (w & 1) * 4 + (lane >> 3) * 8 + ((lane & 7) >> 1)) * 4;
  const rsrc_t outr = mkrs((const void*)a4[2], 2 * DM * 2);
  asm volatile("" : "+v"(fidx), "+v"(ooff));
  if (wave < 8) {
    unsigned widx = wave * 32 + (lane >> 4) * 8 + (lane & 7);
    asm volatile("" : "+v"(widx));
    __builtin_amdgcn_s_sleep(FIRST2);
    u32x4 g0, g1;
    poll2(wsr, lane * 16, WS_Y + w * 16384 + wave * 2048, tag, g0, g1);
    float y = fold16(fold32(uf(g0[0]) + uf(g1[0]), uf(g0[2]) + uf(g1[2])), fold32(uf(g0[1]) + uf(g1[1]), fourth(g0) + fourth(g1)));
    y += dpp<XOR8>(y);
    (&L.fin[0][0])[widx] = y;
  }
  asm volatile("s_waitcnt lgkmcnt(0)" ::: "memory");
  __builtin_amdgcn_s_barrier();
  SB();
  if (wave == 0) {
    const float* f = &L.fin[0][0] + fidx;
    float y = (f[0] + f[32]) + (f[64] + f[96]);
    y = fold32(y, y);
    __builtin_amdgcn_raw_buffer_store_b32(cvtpk(y, dpp<XOR1>(y)), outr, ooff, 0, OUTSC);
    ST(7);
  } else if (w == 0 && tid == 64) {
    __hip_atomic_store((GAS unsigned*)(ws + WS_EPOCH), tag, RLX_AGENT);
  }
  STDUMP();
}

extern "C" void kernel_launch(void* const* d_in, const int* in_sizes, int n_in, void* d_out, int out_size, void* d_ws, size_t ws_size, hipStream_t stream) {
  fused_decode<<<256, NTHR, 0, stream>>>(d_in[1], d_in[2], d_in[3], d_in[0], d_in[6], d_in[7], d_in[8], d_in[5], d_in[4], d_in[9], d_out, d_ws);
}
```

```cpp
#define PF1 !(w&1)
#define PF2 (w&1)
#define STAMP
#define PM1 1
#define PM2 1
#define PW1 0
#define PK1 0
#define PW2 0
#define PK2 0
#define PSC 16
#include <hip/hip_runtime.h>
#include <hip/hip_bf16.h>
#include <stdint.h>

#define GAS __attribute__((address_space(1)))
#define LAS __attribute__((address_space(3)))
#define RLX_AGENT __ATOMIC_RELAXED, __HIP_MEMORY_SCOPE_AGENT
#define RLX_WG __ATOMIC_RELAXED, __HIP_MEMORY_SCOPE_WORKGROUP
typedef unsigned u32x4 __attribute__((ext_vector_type(4)));
typedef unsigned u32x2 __attribute__((ext_vector_type(2)));
typedef float f32x4 __attribute__((ext_vector_type(4)));
typedef short s16x4 __attribute__((ext_vector_type(4)));
typedef __bf16 bf16x2 __attribute__((ext_vector_type(2)));
typedef __bf16 bf16x8 __attribute__((ext_vector_type(8)));
typedef unsigned long long u64;
typedef u64 u64x2 __attribute__((ext_vector_type(2)));
typedef u64 u64x4 __attribute__((ext_vector_type(4)));
typedef __amdgpu_buffer_rsrc_t rsrc_t;

constexpr int DM = 576, NQ = 4096;
constexpr int NWAVE = 11, NTHR = NWAVE * 64, NRED = 36;
constexpr unsigned WS_EPOCH = 0, WS_Q = 4096, WS_O = WS_Q + 16 * 4096, WS_Y = WS_O + 256 * 2048, WS_END = WS_Y + NRED * 16384;
constexpr size_t WS_STAMP = 8u << 20;
constexpr unsigned SPIN_CAP = 1u << 17;

#define FIRST1 4
#define FIRST2 16
#define QSLEEP 64
#define XPOS 7
#define HSH 4
#define MID 8
#define HFIRST 1
#define GW 1
#define KWARM 1
#define CPF2 0
#define CPF 0
#define KAFIRST 1
#define VORD 1
#define OUTSC 0
constexpr int NT = 2;
constexpr unsigned OOR = 0x40000000u;

__device__ __forceinline__ float bflo(unsigned u) { return __builtin_bit_cast(float, u << 16); }
__device__ __forceinline__ float bfhi(unsigned u) { return __builtin_bit_cast(float, u & 0xffff0000u); }
__device__ __forceinline__ unsigned fu(float f) { return __float_as_uint(f); }
__device__ __forceinline__ float uf(unsigned u) { return __uint_as_float(u); }
__device__ __forceinline__ unsigned cvtpk(float lo, float hi) { bf16x2 v = {(__bf16)lo, (__bf16)hi}; return __builtin_bit_cast(unsigned, v); }
__device__ __forceinline__ float dot2(unsigned k, unsigned q, float acc) {
  return __builtin_amdgcn_fdot2_f32_bf16(__builtin_bit_cast(bf16x2, k), __builtin_bit_cast(bf16x2, q), acc, false);
}
template <int CTRL> __device__ __forceinline__ float dpp(float x) {
  return __builtin_bit_cast(float, __builtin_amdgcn_mov_dpp(__builtin_bit_cast(int, x), CTRL, 0xf, 0xf, true));
}
constexpr int XOR1 = 0xB1, XOR2 = 0x4E, XOR7 = 0x141, XOR8 = 0x128;
__device__ __forceinline__ f32x4 mfma(u32x4 a, u32x4 b, f32x4 c) {
  return __builtin_amdgcn_mfma_f32_16x16x32_bf16(__builtin_bit_cast(bf16x8, a), __builtin_bit_cast(bf16x8, b), c, 0, 0, 0);
}
__device__ __forceinline__ float fold32(float a, float b) {
  auto r = __builtin_amdgcn_permlane32_swap(fu(a), fu(b), false, false);
  return uf(r[0]) + uf(r[1]);
}
__device__ __forceinline__ float fold16(float a, float b) {
  auto r = __builtin_amdgcn_permlane16_swap(fu(a), fu(b), false, false);
  return uf(r[0]) + uf(r[1]);
}
__device__ __forceinline__ float fmax2(float a, float b) { return __builtin_amdgcn_fmed3f(a, b, INFINITY); }
__device__ __forceinline__ float max16_32(float x) {
  auto s = __builtin_amdgcn_permlane16_swap(fu(x), fu(x), false, false);
  x = fmax2(uf(s[0]), uf(s[1]));
  auto t = __builtin_amdgcn_permlane32_swap(fu(x), fu(x), false, false);
  return fmax2(uf(t[0]), uf(t[1]));
}
__device__ __forceinline__ rsrc_t mkrs(const void* p, unsigned bytes) { return __builtin_amdgcn_make_buffer_rsrc((void*)p, 0, bytes, 0x00020000); }
template <int AUX> __device__ __forceinline__ u32x4 ldb(rsrc_t r, unsigned voff, unsigned soff) { return __builtin_amdgcn_raw_buffer_load_b128(r, voff, soff, AUX); }
constexpr int SC1 = 16;
__device__ __forceinline__ u32x4 ldx(rsrc_t r, unsigned voff, unsigned soff) { asm volatile("" ::: "memory"); return __builtin_amdgcn_raw_buffer_load_b128(r, voff, soff, SC1); }
__device__ __forceinline__ void stx(rsrc_t r, unsigned voff, unsigned soff, u32x4 v) { __builtin_amdgcn_raw_buffer_store_b128(v, r, voff, soff, SC1); }
__device__ __forceinline__ unsigned xor3(unsigned a, unsigned b, unsigned c) { return __builtin_amdgcn_bitop3_b32(a, b, c, 0x96); }
__device__ __forceinline__ u32x4 seal(unsigned a, unsigned b, unsigned c, unsigned tag) { return u32x4{a, b, c, xor3(a, b, c ^ tag)}; }
__device__ __forceinline__ unsigned bad(u32x4 g, unsigned tag) { return xor3(xor3(g[0], g[1], g[2]), g[3], tag); }
__device__ __forceinline__ u32x4 seal4(float a, float b, float c, float d, unsigned tag) {
  return seal(__builtin_amdgcn_perm(fu(a), fu(d), 0x07060501u), __builtin_amdgcn_perm(fu(b), fu(d), 0x07060502u), __builtin_amdgcn_perm(fu(c), fu(d), 0x07060503u), tag);
}
__device__ __forceinline__ float fourth(u32x4 g) { return uf(__builtin_amdgcn_perm(g[2], __builtin_amdgcn_perm(g[1], g[0], 0x0c04000cu), 0x04020100u)); }
#define SB() __builtin_amdgcn_sched_barrier(0)
#define ST(i) do { SB(); u64 t_; asm volatile("s_memrealtime %0\n\ts_waitcnt lgkmcnt(0)" : "=s"(t_) :: "memory"); stp[i] = (unsigned)t_; SB(); } while (0)
#define PAGG(M, mn, mx, sm, c) (M == 0 ? mx : M == 1 ? sm / max(c, 1) : mn)
#define STDUMP() do { GAS u64* pg = (GAS u64*)(ws + WS_STAMP); \
  if (lane == 0) { if (wave == 0) __hip_atomic_store(pg + blockIdx.x, ((u64)tag << 32) | stp[0], RLX_AGENT); \
    if (wave == PW1) __hip_atomic_store(pg + 256 + blockIdx.x, ((u64)tag << 32) | ((PF1) ? stp[PK1] : 0u), RLX_AGENT); \
    if (wave == PW2) __hip_atomic_store(pg + 512 + blockIdx.x, ((u64)tag << 32) | ((PF2) ? stp[PK2] : 0u), RLX_AGENT); } \
  if (blockIdx.x == 0 && wave == 0) { u64 a[12]; \
    for (unsigned sp = 0; sp < SPIN_CAP; ++sp) { bool ok = true; for (int i_ = 0; i_ < 12; ++i_) { a[i_] = __hip_atomic_load(pg + lane + 64 * i_, RLX_AGENT); ok &= (unsigned)(a[i_] >> 32) == tag; } if (__all(ok)) break; } \
    int mn[3], mx[3], sm[3], cn[3]; \
    for (int q_ = 0; q_ < 3; ++q_) { mn[q_] = 0x7fffffff; mx[q_] = -0x7fffffff; sm[q_] = 0; cn[q_] = 0; \
      for (int i_ = 0; i_ < 4; ++i_) { const unsigned v_ = (unsigned)a[q_ * 4 + i_]; if (v_) { const int d_ = (int)(v_ - stp[0]); mn[q_] = min(mn[q_], d_); mx[q_] = max(mx[q_], d_); sm[q_] += d_; cn[q_]++; } } \
      for (int o_ = 1; o_ < 64; o_ <<= 1) { mn[q_] = min(mn[q_], __shfl_xor(mn[q_], o_)); mx[q_] = max(mx[q_], __shfl_xor(mx[q_], o_)); sm[q_] += __shfl_xor(sm[q_], o_); cn[q_] += __shfl_xor(cn[q_], o_); } } \
    const int X = PAGG(PM2, mn[2], mx[2], sm[2], cn[2]) - PAGG(PM1, mn[1], mx[1], sm[1], cn[1]); \
    const unsigned until = stp[0] + mn[0] + 6000 + PSC * X; \
    for (unsigned sp = 0; sp < 1000000; ++sp) { if ((int)((unsigned)__builtin_amdgcn_s_memrealtime() - until) >= 0) break; } } } while (0)
#define STDECL() unsigned stp[12] = {0, 0, 0, 0, 0, 0, 0, 0, 0, 0, 0, 0}
#define STPAR , unsigned (&stp)[12]
#define STARG , stp

struct Lds {
  unsigned char wo[9][4096];
  float part[11][2][132];
  unsigned q[2][128];
  unsigned x[144];
  float fin[8][32];
  unsigned short o[32];
  unsigned qready, kissued, issued, done[2];
};

__device__ __forceinline__ void lds_wait(unsigned* p, unsigned n) {
  for (unsigned spins = 0; __hip_atomic_load(p, RLX_WG) < n && spins < (SPIN_CAP << 3); ++spins) __builtin_amdgcn_s_sleep(1);
}
__device__ __forceinline__ void lds_inc(unsigned* p, int lane) { if (lane == 0) __hip_atomic_fetch_add(p, 1u, RLX_WG); }
__device__ __forceinline__ void poll2(rsrc_t r, unsigned voff, unsigned soff, unsigned tag, u32x4& g0, u32x4& g1) {
  for (unsigned spins = 0; spins < SPIN_CAP; ++spins) {
    g0 = ldx(r, voff, soff); g1 = ldx(r, voff + 1024, soff);
    if (!__any((bad(g0, tag) | bad(g1, tag)) != 0u)) break;
  }
}

__global__ void __launch_bounds__(NTHR, 3) fused_decode(const void* kA, const void* vA, const void* wqA, const void* xA, const void* kB, const void* vB, const void* wqB, const void* xB,
                                                         const void* woA, const void* woB, void* out, void* wsp) {
  __shared__ __attribute__((aligned(16))) Lds L;
  STDECL();
  ST(0);
  const auto karg = __builtin_amdgcn_kernarg_segment_ptr();
  if (KWARM) asm volatile("s_load_dword s94, %0, 0x0\n\ts_load_dword s95, %0, 0x40" :: "s"(karg) : "s94", "s95");
  SB();
  u64x2 a2; u64x4 a4;
#define GETARGS(OFF, ...) asm volatile("s_load_dwordx4 %0, %[ka], %[off]\n\ts_load_dwordx8 %1, %[ka], 0x40\n\ts_waitcnt lgkmcnt(0)" : "=&s"(a2), "=&s"(a4) : [ka] "s"(karg), [off] "s"(OFF), __VA_ARGS__ : "memory")
  const int tid = threadIdx.x, lane = tid & 63, wave = __builtin_amdgcn_readfirstlane(tid >> 6);
  const int w = blockIdx.x, h = (w & 7) * 2 + (w >> 7), s = (w >> 3) & 15;
  const int hp = (h + HSH) & 15, pbr = s >> 3;
  unsigned tag;

  if (__builtin_expect(wave == 0, 1)) {
    const int rs = lane >> 3, c = lane & 7;
    const int qbr = (w >> 1) & 1, half = (w >> 2) & 1, j = w >> 3, blk = (j >> 1) * 4 + (j & 1) * 2 + ((j ^ w) & 1);
    unsigned woff = (unsigned)((half * 288 + rs * 36) * NQ + blk * 64 + c * 8) * 2;
    asm volatile("" : "+v"(woff));
    GETARGS(qbr * 32 + 16, "s"(half));
    const rsrc_t wr = mkrs((const void*)a2[0], DM * NQ * 2);
    const rsrc_t wsr = mkrs((const void*)a4[3], WS_END);
    u32x4 wv[36], xr;
    unsigned epoch, so = 0;
#pragma unroll
    for (int t = 0; t < 36; ++t) {
      wv[t] = ldb<NT>(wr, woff, so); SB();
      so += NQ * 2; asm volatile("" : "+s"(so)); SB();
      if (t == XPOS) {
        xr = ldb<0>(mkrs((const char*)a2[1] + half * 576, 576), lane * 16, 0);
        epoch = __hip_atomic_load((GAS unsigned*)(a4[3] + WS_EPOCH), RLX_AGENT); SB();
      }
    }
    __builtin_amdgcn_s_barrier();
    SB();
    ST(8);
    if (lane < 36) *(u32x4*)&L.x[lane * 4] = xr;
    float acc[8] = {0, 0, 0, 0, 0, 0, 0, 0};
#pragma unroll
    for (int t4 = 0; t4 < 9; ++t4) {
      const u32x2 xw = *(const u32x2*)&L.x[rs * 18 + t4 * 2];
#pragma unroll
      for (int u = 0; u < 4; ++u) {
        const int t = t4 * 4 + u;
        const unsigned xl = (u & 1) ? xw[u >> 1] >> 16 : xw[u >> 1] & 0xffffu, xh = (u & 1) ? xw[u >> 1] & 0xffff0000u : xw[u >> 1] << 16;
#pragma unroll
        for (int k = 0; k < 4; ++k) { acc[2 * k] = dot2(wv[t][k], xl, acc[2 * k]); acc[2 * k + 1] = dot2(wv[t][k], xh, acc[2 * k + 1]); }
      }
      SB();
    }
    float b[4];
#pragma unroll
    for (int k = 0; k < 4; ++k) { b[k] = fold32(acc[k], acc[k + 4]); b[k] = fold16(b[k], b[k]); b[k] += dpp<XOR8>(b[k]); }
    asm volatile("" : "+v"(epoch));
    tag = __builtin_amdgcn_readfirstlane(epoch) + 1u;
    tag += tag == 0u;
    stx(wsr, (lane & 24) ? OOR : (c * 2 + (lane >> 5)) * 16, WS_Q + (blk >> 2) * 4096 + (qbr * 2 + half) * 1024 + (blk & 3) * 256, seal4(b[0], b[1], b[2], b[3], tag));
    ST(1);
    __builtin_amdgcn_s_sleep(QSLEEP);
    u32x4 g[4];
    for (unsigned spins = 0; spins < SPIN_CAP; ++spins) {
#pragma unroll
      for (int i = 0; i < 4; ++i) g[i] = ldx(wsr, lane * 16 + i * 1024, WS_Q + h * 4096);
      if (!__any((bad(g[0], tag) | bad(g[1], tag) | bad(g[2], tag) | bad(g[3], tag)) != 0u)) break;
    }
#pragma unroll
    for (int bb = 0; bb < 2; ++bb) {
      const u32x4 p0 = g[2 * bb], p1 = g[2 * bb + 1];
      *(u32x2*)&L.q[bb][2 * lane] = u32x2{cvtpk(uf(p0[0]) + uf(p1[0]), uf(p0[1]) + uf(p1[1])), cvtpk(uf(p0[2]) + uf(p1[2]), fourth(p0) + fourth(p1))};
    }
    __builtin_amdgcn_fence(__ATOMIC_RELEASE, "workgroup");
    if (lane == 0) __hip_atomic_store(&L.qready, tag, RLX_WG);
    ST(2);
  } else {
    const int br = wave == 1 || wave == 3 ? 0 : 1;
    const int pair = wave < 5 ? 3 : (wave - 5) >> 1, odd = wave < 5 ? (wave - 1) >> 1 : (wave - 5) & 1;
    const int g = lane >> 4, i = lane & 15;
    unsigned loff = (h * 2048 + pair * 512 + s * 32 + odd * 2) * 1024 + lane * 16;
    const bool late = wave < (VORD ? 4 : 3), closer = wave < 3;
    unsigned so = ((s + h) & 7) * 4096, knead = KAFIRST && br ? 2u : 0u;
    if (tid == 64) { L.qready = 0u; L.kissued = 0u; L.issued = 0u; L.done[0] = 0u; L.done[1] = 0u; }
    unsigned cp0 = 0, cp1 = 0, cp2 = 0;
    asm volatile("" : "+v"(loff));
    asm volatile("" : "+s"(so), "+s"(knead));
    GETARGS(br * 32, "s"(so));
    if (CPF && wave == 10) {
      u64 pc; asm volatile("s_getpc_b64 %0" : "=s"(pc));
      pc = (pc & ~127ull) - CPF * 128 + (w >> 3) * 128;
      asm volatile("s_load_dword %0, %3, 0x0\n\ts_load_dword %1, %3, 0x1000\n\ts_load_dword %2, %4, 0x0" : "=&s"(cp0), "=&s"(cp1), "=&s"(cp2) : "s"(pc), "s"(pc + ((w >> 3) < CPF2 ? 8192 : 0)));
    }
    const rsrc_t kr = mkrs((const void*)a2[0], 32 << 20), vr = mkrs((const void*)a2[1], 32 << 20);
    const rsrc_t wsr = mkrs((const void*)a4[3], WS_END);
    asm volatile("" :: "s"(kr), "s"(vr));
    __builtin_amdgcn_s_barrier();
    SB();
    ST(8);
    u32x4 K[16], V[16];
#define LOAD16(X, R) _Pragma("unroll") for (int t = 0; t < 16; t += 2) { \
      X[t] = ldb<NT>(R, loff, so); SB(); unsigned nx = so + 4096u; asm volatile("" : "+s"(nx)); SB(); \
      X[t + 1] = ldb<NT>(R, loff + 1024, so); SB(); so = nx & 0x7000u; asm volatile("" : "+s"(so)); SB(); }
    if (knead) lds_wait(&L.kissued, knead);
    SB();
#pragma unroll
    for (int t = 0; t < 16; t += 2)
      asm volatile("buffer_load_dwordx4 %0, %3, %4, %2 offen nt\n\ts_nop 0\n\tbuffer_load_dwordx4 %1, %3, %4, %2 offen offset:1024 nt\n\t"
                   "s_addk_i32 %2, 0x1000\n\ts_and_b32 %2, %2, 0x7000" : "=&v"(K[t]), "=&v"(K[t + 1]), "+s"(so) : "v"(loff), "s"(kr) : "scc");
    SB();
    lds_inc(&L.kissued, lane);
    ST(9);
    if (!late) {
      lds_wait(&L.kissued, 10u);
      SB();
      LOAD16(V, vr)
      lds_inc(&L.issued, lane);
      ST(1);
      asm volatile("s_waitcnt vmcnt(16)" ::: "memory");
    } else {
      asm volatile("s_waitcnt vmcnt(0)" ::: "memory");
    }
#pragma unroll
    for (int t = 0; t < 16; ++t) asm volatile("" : "+v"(K[t]));
    lds_wait(&L.qready, 1u);
    tag = __builtin_amdgcn_readfirstlane(__hip_atomic_load(&L.qready, RLX_WG));
    __builtin_amdgcn_fence(__ATOMIC_ACQUIRE, "workgroup");
    const u32x4 q0 = *(const u32x4*)&L.q[br][4 * i], q1 = *(const u32x4*)&L.q[br][64 + 4 * i];
    constexpr float C2 = 0.088388347648318440f * 1.4426950408889634f;
    float sc[16], m = -INFINITY;
#pragma unroll
    for (int t = 0; t < 16; ++t) {
      float d0 = 0, d1 = 0;
#pragma unroll
      for (int k = 0; k < 4; ++k) { d0 = dot2(K[t][k], q0[k], d0); d1 = dot2(K[t][k], q1[k], d1); }
      d0 += dpp<XOR8>(d0); d1 += dpp<XOR8>(d1);
      float d = (i & 8) ? d1 : d0;
      d += dpp<XOR7>(d); d += dpp<XOR2>(d); d += dpp<XOR1>(d);
      sc[t] = d; m = fmaxf(m, d);
    }
    m = max16_32(m) * C2;
    unsigned msk[4];
#pragma unroll
    for (int k = 0; k < 4; ++k) msk[k] = (((i & 7) >> 1) == k) ? ((i & 1) ? 0xffff0000u : 0x0000ffffu) : 0u;
    float l = 0;
    u32x4 a[16];
#pragma unroll
    for (int t = 0; t < 16; ++t) {
      const float p = __builtin_amdgcn_exp2f(fmaf(sc[t], C2, -m));
      const unsigned pp = cvtpk(p, p);
      l += p;
      a[t] = u32x4{pp & msk[0], pp & msk[1], pp & msk[2], pp & msk[3]};
    }
    l = fold16(l, l); l = fold32(l, l);
    if ((lane & 55) == 0) *(float2*)&L.part[wave][lane >> 3][0] = make_float2(m, l);
#pragma unroll
    for (int t = 0; t < 16; ++t) asm volatile("" : "+v"(a[t]));
    SB();
    ST(2);
    if (late) {
      lds_wait(&L.issued, VORD ? (wave == 2 ? 7u : wave == 3 ? 8u : 9u) : 8u);
      SB();
      LOAD16(V, vr)
      lds_inc(&L.issued, lane);
      ST(1);
    }
    f32x4 acc = {0, 0, 0, 0};
#pragma unroll
    for (int t = 0; t < MID; ++t) acc = mfma(a[t], V[t], acc);
    asm volatile("" : "+v"(acc));
    SB();
    const int hd = g >> 1, oi = 4 + 8 * i + 4 * (g & 1);
    float f = 0; f32x4 base = {0, 0, 0, 0};
    unsigned lse = 0, poff = 0;
    if (closer) {
      lds_wait(&L.done[br], br ? 7u : 1u);
      __builtin_amdgcn_fence(__ATOMIC_ACQUIRE, "workgroup");
      const float2 me = *(const float2*)&L.part[wave][hd][0];
      float M, Ls;
      if (br) {
        float2 r[7]; f32x4 o[7];
#pragma unroll
        for (int u = 0; u < 7; ++u) { r[u] = *(const float2*)&L.part[4 + u][hd][0]; o[u] = *(const f32x4*)&L.part[4 + u][hd][oi]; }
        M = me.x;
#pragma unroll
        for (int u = 0; u < 7; ++u) M = fmax2(M, r[u].x);
        Ls = 0;
#pragma unroll
        for (int u = 0; u < 7; ++u) { const float e = __builtin_amdgcn_exp2f(r[u].x - M); Ls = fmaf(e, r[u].y, Ls); base += e * o[u]; }
      } else {
        const float2 r = *(const float2*)&L.part[3][hd][0]; const f32x4 o = *(const f32x4*)&L.part[3][hd][oi];
        M = fmax2(me.x, r.x);
        const float e = __builtin_amdgcn_exp2f(r.x - M); Ls = e * r.y; base = e * o;
      }
      f = __builtin_amdgcn_exp2f(me.x - M); Ls = fmaf(f, me.y, Ls);
      const float inv = __builtin_amdgcn_rcpf(Ls);
      f *= inv; base *= inv;
      lse = fu(M + __builtin_amdgcn_logf(Ls));
      poff = WS_O + h * 32768 + br * 16384 + s * 128 + (4 * hd + (i >> 2)) * 2048 + (4 * (g & 1) + (i & 3)) * 16;
      ST(4);
    }
    asm volatile("" : "+v"(f), "+v"(base), "+v"(lse), "+v"(poff));
    SB();
#pragma unroll
    for (int t = MID; t < 16; ++t) acc = mfma(a[t], V[t], acc);
    if (closer) {
      acc = acc * f + base;
      stx(wsr, poff, 0, seal(cvtpk(acc[0], acc[1]), cvtpk(acc[2], acc[3]), lse, tag));
    } else {
      *(f32x4*)&L.part[wave][hd][oi] = acc;
      __builtin_amdgcn_fence(__ATOMIC_RELEASE, "workgroup");
      lds_inc(&L.done[br], lane);
    }
    asm volatile("" :: "s"(cp0), "s"(cp1), "s"(cp2));
    ST(3);
  }
  const rsrc_t wsr = mkrs((const void*)a4[3], WS_END);
  GAS unsigned char* ws = (GAS unsigned char*)a4[3];
  const int gw = GW;
  const int pw = wave == 0 ? 8 : wave == (VORD ? 2 : 3) ? 0 : wave < 4 ? -1 : wave - 3;
  u32x4 wf[4];
  unsigned yoff = 0, oaddr = (lane >> 4) * 16;
  if (pw >= 0) {
    lds_wait(&L.issued, 10u);
    const rsrc_t wr = mkrs((const uint16_t*)(pbr ? a4[1] : a4[0]) + (hp * 256 + (s & 7) * 32) * DM + pw * 64, 32 * DM * 2);
    const unsigned woff = ((lane >> 3) * DM + (lane & 7) * 8) * 2;
    LAS unsigned char* dst = (LAS unsigned char*)&L.wo[pw][0];
#pragma unroll
    for (int t = 0; t < 4; ++t) __builtin_amdgcn_raw_ptr_buffer_load_lds(wr, dst + t * 1024, 16, woff, t * 8 * DM * 2, 0, NT);
    yoff = lane < 16 ? WS_Y + (pbr * 18 + pw * 2 + (lane >> 3)) * 16384 + (hp * 8 + (s & 7)) * 128 + (lane & 7) * 16 : OOR;
    asm volatile("s_waitcnt vmcnt(0)" ::: "memory");
    LAS unsigned char* src = dst + (lane >> 4) * 1024 + ((lane >> 2) & 3) * 128 + (lane & 3) * 8;
#pragma unroll
    for (int b = 0; b < 4; ++b) {
      const u32x2 lo = __builtin_bit_cast(u32x2, __builtin_amdgcn_ds_read_tr16_b64_v4i16((LAS s16x4*)(src + b * 32)));
      const u32x2 hi = __builtin_bit_cast(u32x2, __builtin_amdgcn_ds_read_tr16_b64_v4i16((LAS s16x4*)(src + b * 32 + 512)));
      wf[b] = u32x4{lo[0], lo[1], hi[0], hi[1]};
    }
    asm volatile("" : "+v"(wf[0]), "+v"(wf[1]), "+v"(wf[2]), "+v"(wf[3]));
    ST(9);
  } else if (wave == gw) {
    unsigned oidx = 8 * (lane & 3) + (lane & 4) + (lane >> 4);
    asm volatile("" : "+v"(oidx));
    __builtin_amdgcn_s_sleep(FIRST1);
    u32x4 g0, g1;
    poll2(wsr, lane * 16, WS_O + (hp * 16 + s) * 2048, tag, g0, g1);
    ST(10);
    float mx = fmax2(uf(g0[2]), uf(g1[2]));
    mx = max16_32(fmax2(mx, dpp<XOR8>(mx)));
    const float t0 = __builtin_amdgcn_exp2f(uf(g0[2]) - mx), t1 = __builtin_amdgcn_exp2f(uf(g1[2]) - mx);
    float e = t0 + t1;
    e += dpp<XOR8>(e); e = fold16(e, e); e = fold32(e, e);
    const float a0 = fmaf(t0, bflo(g0[0]), t1 * bflo(g1[0])), a1 = fmaf(t0, bfhi(g0[0]), t1 * bfhi(g1[0]));
    const float a2 = fmaf(t0, bflo(g0[1]), t1 * bflo(g1[1])), a3 = fmaf(t0, bfhi(g0[1]), t1 * bfhi(g1[1]));
    float a = fold16(fold32(a0, a2), fold32(a1, a3));
    a += dpp<XOR8>(a);
    L.o[oidx] = (unsigned short)cvtpk(a * __builtin_amdgcn_rcpf(e), 0.f);
    ST(5);
  }
  asm volatile("" : "+v"(yoff), "+v"(oaddr));
  asm volatile("s_waitcnt lgkmcnt(0)" ::: "memory");
  __builtin_amdgcn_s_barrier();
  SB();
  ST(11);
  if (pw >= 0) {
    const u32x4 ov = *(const u32x4*)((const char*)L.o + oaddr);
    const f32x4 z = {0, 0, 0, 0};
    const float y3 = mfma(ov, wf[3], z)[0], y0 = mfma(ov, wf[0], z)[0], y1 = mfma(ov, wf[1], z)[0], y2 = mfma(ov, wf[2], z)[0];
    stx(wsr, yoff, 0, seal4(y0, y1, y2, y3, tag));
  }
  ST(6);
  if (w >= NRED) { STDUMP(); return; }
  unsigned fidx = (lane >> 5) * 128 + (lane & 31), ooff = (lane & 33) ? OOR : ((w >> 1) * 32 + (w & 1) * 4 + (lane >> 3) * 8 + ((lane & 7) >> 1)) * 4;
  const rsrc_t outr = mkrs((const void*)a4[2], 2 * DM * 2);
  asm volatile("" : "+v"(fidx), "+v"(ooff));
  if (wave < 8) {
    unsigned widx = wave * 32 + (lane >> 4) * 8 + (lane & 7);
    asm volatile("" : "+v"(widx));
    __builtin_amdgcn_s_sleep(FIRST2);
    u32x4 g0, g1;
    poll2(wsr, lane * 16, WS_Y + w * 16384 + wave * 2048, tag, g0, g1);
    float y = fold16(fold32(uf(g0[0]) + uf(g1[0]), uf(g0[2]) + uf(g1[2])), fold32(uf(g0[1]) + uf(g1[1]), fourth(g0) + fourth(g1)));
    y += dpp<XOR8>(y);
    (&L.fin[0][0])[widx] = y;
  }
  asm volatile("s_waitcnt lgkmcnt(0)" ::: "memory");
  __builtin_amdgcn_s_barrier();
  SB();
  if (wave == 0) {
    const float* f = &L.fin[0][0] + fidx;
    float y = (f[0] + f[32]) + (f[64] + f[96]);
    y = fold32(y, y);
    __builtin_amdgcn_raw_buffer_store_b32(cvtpk(y, dpp<XOR1>(y)), outr, ooff, 0, OUTSC);
    ST(7);
  } else if (w == 0 && tid == 64) {
    __hip_atomic_store((GAS unsigned*)(ws + WS_EPOCH), tag, RLX_AGENT);
  }
  STDUMP();
}

extern "C" void kernel_launch(void* const* d_in, const int* in_sizes, int n_in, void* d_out, int out_size, void* d_ws, size_t ws_size, hipStream_t stream) {
  fused_decode<<<256, NTHR, 0, stream>>>(d_in[1], d_in[2], d_in[3], d_in[0], d_in[6], d_in[7], d_in[8], d_in[5], d_in[4], d_in[9], d_out, d_ws);
}
```

```cpp
#include <hip/hip_runtime.h>
#include <hip/hip_bf16.h>
#include <stdint.h>

#define GAS __attribute__((address_space(1)))
#define LAS __attribute__((address_space(3)))
#define RLX_AGENT __ATOMIC_RELAXED, __HIP_MEMORY_SCOPE_AGENT
#define RLX_WG __ATOMIC_RELAXED, __HIP_MEMORY_SCOPE_WORKGROUP
typedef unsigned u32x4 __attribute__((ext_vector_type(4)));
typedef unsigned u32x2 __attribute__((ext_vector_type(2)));
typedef float f32x4 __attribute__((ext_vector_type(4)));
typedef short s16x4 __attribute__((ext_vector_type(4)));
typedef __bf16 bf16x2 __attribute__((ext_vector_type(2)));
typedef __bf16 bf16x8 __attribute__((ext_vector_type(8)));
typedef unsigned long long u64;
typedef u64 u64x2 __attribute__((ext_vector_type(2)));
typedef u64 u64x4 __attribute__((ext_vector_type(4)));
typedef __amdgpu_buffer_rsrc_t rsrc_t;

constexpr int DM = 576, NQ = 4096;
constexpr int NWAVE = 11, NTHR = NWAVE * 64, NRED = 36;
constexpr unsigned WS_EPOCH = 0, WS_Q = 4096, WS_O = WS_Q + 16 * 4096, WS_Y = WS_O + 256 * 2048, WS_END = WS_Y + NRED * 16384;
constexpr size_t WS_STAMP = 8u << 20;
constexpr unsigned SPIN_CAP = 1u << 17;

#define FIRST1 4
#define FIRST2 16
#define QSLEEP 64
#define XPOS 7
#define HSH 4
#define MID 10
#define HFIRST 1
#define ROFF 0
#define BUMP 35
#define HR 3
#define GAPS 2
#define PGAL 1
#define PW0 1
#define GW 3
#define KWARM 1
#define KAFIRST 1
#define WOGATE 0
#define PIPE 0
#define OUTSC 0
constexpr int NT = 2;
constexpr unsigned OOR = 0x40000000u;

__device__ __forceinline__ float bflo(unsigned u) { return __builtin_bit_cast(float, u << 16); }
__device__ __forceinline__ float bfhi(unsigned u) { return __builtin_bit_cast(float, u & 0xffff0000u); }
__device__ __forceinline__ unsigned fu(float f) { return __float_as_uint(f); }
__device__ __forceinline__ float uf(unsigned u) { return __uint_as_float(u); }
__device__ __forceinline__ unsigned cvtpk(float lo, float hi) { bf16x2 v = {(__bf16)lo, (__bf16)hi}; return __builtin_bit_cast(unsigned, v); }
__device__ __forceinline__ float dot2(unsigned k, unsigned q, float acc) {
  return __builtin_amdgcn_fdot2_f32_bf16(__builtin_bit_cast(bf16x2, k), __builtin_bit_cast(bf16x2, q), acc, false);
}
template <int CTRL> __device__ __forceinline__ float dpp(float x) {
  return __builtin_bit_cast(float, __builtin_amdgcn_mov_dpp(__builtin_bit_cast(int, x), CTRL, 0xf, 0xf, true));
}
constexpr int XOR1 = 0xB1, XOR2 = 0x4E, XOR7 = 0x141, XOR8 = 0x128;
__device__ __forceinline__ f32x4 mfma(u32x4 a, u32x4 b, f32x4 c) {
  return __builtin_amdgcn_mfma_f32_16x16x32_bf16(__builtin_bit_cast(bf16x8, a), __builtin_bit_cast(bf16x8, b), c, 0, 0, 0);
}
__device__ __forceinline__ float fold32(float a, float b) {
  auto r = __builtin_amdgcn_permlane32_swap(fu(a), fu(b), false, false);
  return uf(r[0]) + uf(r[1]);
}
__device__ __forceinline__ float fold16(float a, float b) {
  auto r = __builtin_amdgcn_permlane16_swap(fu(a), fu(b), false, false);
  return uf(r[0]) + uf(r[1]);
}
__device__ __forceinline__ float fmax2(float a, float b) { return __builtin_amdgcn_fmed3f(a, b, INFINITY); }
__device__ __forceinline__ float max16_32(float x) {
  auto s = __builtin_amdgcn_permlane16_swap(fu(x), fu(x), false, false);
  x = fmax2(uf(s[0]), uf(s[1]));
  auto t = __builtin_amdgcn_permlane32_swap(fu(x), fu(x), false, false);
  return fmax2(uf(t[0]), uf(t[1]));
}
__device__ __forceinline__ rsrc_t mkrs(const void* p, unsigned bytes) { return __builtin_amdgcn_make_buffer_rsrc((void*)p, 0, bytes, 0x00020000); }
template <int AUX> __device__ __forceinline__ u32x4 ldb(rsrc_t r, unsigned voff, unsigned soff) { return __builtin_amdgcn_raw_buffer_load_b128(r, voff, soff, AUX); }
constexpr int SC1 = 16;
__device__ __forceinline__ u32x4 ldx(rsrc_t r, unsigned voff, unsigned soff) { asm volatile("" ::: "memory"); return __builtin_amdgcn_raw_buffer_load_b128(r, voff, soff, SC1); }
__device__ __forceinline__ void stx(rsrc_t r, unsigned voff, unsigned soff, u32x4 v) { __builtin_amdgcn_raw_buffer_store_b128(v, r, voff, soff, SC1); }
__device__ __forceinline__ unsigned xor3(unsigned a, unsigned b, unsigned c) { return __builtin_amdgcn_bitop3_b32(a, b, c, 0x96); }
__device__ __forceinline__ u32x4 seal(unsigned a, unsigned b, unsigned c, unsigned tag) { return u32x4{a, b, c, xor3(a, b, c ^ tag)}; }
__device__ __forceinline__ unsigned bad(u32x4 g, unsigned tag) { return xor3(xor3(g[0], g[1], g[2]), g[3], tag); }
__device__ __forceinline__ u32x4 seal4(float a, float b, float c, float d, unsigned tag) {
  return seal(__builtin_amdgcn_perm(fu(a), fu(d), 0x07060501u), __builtin_amdgcn_perm(fu(b), fu(d), 0x07060502u), __builtin_amdgcn_perm(fu(c), fu(d), 0x07060503u), tag);
}
__device__ __forceinline__ float fourth(u32x4 g) { return uf(__builtin_amdgcn_perm(g[2], __builtin_amdgcn_perm(g[1], g[0], 0x0c04000cu), 0x04020100u)); }
#define SB() __builtin_amdgcn_sched_barrier(0)
#define ST(i)
#define STDUMP()
#define STDECL()
#define STPAR
#define STARG

struct Lds {
  unsigned char wo[9][4096];
  float part[11][2][132];
  unsigned q[2][128];
  unsigned x[144];
  float fin[8][32];
  unsigned short o[32];
  unsigned qready, kissued, issued, done[2];
};

__device__ __forceinline__ void lds_wait(unsigned* p, unsigned n) {
  for (unsigned spins = 0; __hip_atomic_load(p, RLX_WG) < n && spins < (SPIN_CAP << 3); ++spins) __builtin_amdgcn_s_sleep(1);
}
__device__ __forceinline__ void lds_inc(unsigned* p, int lane) { if (lane == 0) __hip_atomic_fetch_add(p, 1u, RLX_WG); }
#define POLL2(MID_) \
    "1:\n\tbuffer_load_dwordx4 v[100:103], %[vo], %[rs], %[so] offen sc1\n\tbuffer_load_dwordx4 v[104:107], %[vo], %[rs], %[so] offen offset:1024 sc1\n\t" \
    "s_sub_u32 %[n], %[n], 1\n\ts_waitcnt vmcnt(1)\n\t" \
    "v_bitop3_b32 v108, v100, v101, v102 bitop3:0x96\n\tv_bitop3_b32 v108, v108, v103, %[tag] bitop3:0x96\n\t" MID_ "s_waitcnt vmcnt(0)\n\t" \
    "v_bitop3_b32 v109, v104, v105, v106 bitop3:0x96\n\tv_bitop3_b32 v109, v109, v107, %[tag] bitop3:0x96\n\t" \
    "v_or_b32 v108, v108, v109\n\tv_cmp_ne_u32 vcc, 0, v108\n\ts_cbranch_vccz 2f\n\ts_cbranch_scc0 1b\n2:\n\t"
#define POLLCLOB "memory", "vcc", "scc", "v100", "v101", "v102", "v103", "v104", "v105", "v106", "v107", "v108", "v109", "v110", "v111", "v112", "v113", "v114", "v115", "v116", "v117"

__global__ void __launch_bounds__(NTHR, 3) fused_decode(const void* kA, const void* vA, const void* wqA, const void* xA, const void* kB, const void* vB, const void* wqB, const void* xB,
                                                         const void* woA, const void* woB, void* out, void* wsp) {
  __shared__ __attribute__((aligned(16))) Lds L;
  STDECL();
  ST(0);
  const auto karg = __builtin_amdgcn_kernarg_segment_ptr();
  if (KWARM) asm volatile("s_load_dword s94, %0, 0x0\n\ts_load_dword s95, %0, 0x40" :: "s"(karg) : "s94", "s95");
  SB();
  u64x2 a2; u64x4 a4;
#define GETARGS(OFF, ...) asm volatile("s_load_dwordx4 %0, %[ka], %[off]\n\ts_load_dwordx8 %1, %[ka], 0x40\n\ts_waitcnt lgkmcnt(0)" : "=&s"(a2), "=&s"(a4) : [ka] "s"(karg), [off] "s"(OFF), __VA_ARGS__ : "memory")
  const int tid = threadIdx.x, lane = tid & 63, wave = __builtin_amdgcn_readfirstlane(tid >> 6);
  const int w = blockIdx.x, h = (w & 7) * 2 + (w >> 7), s = (w >> 3) & 15;
  const int hp = (h + HSH) & 15, pbr = s >> 3;
  unsigned tag;
  const unsigned tstart = WOGATE ? (unsigned)__builtin_amdgcn_s_memrealtime() : 0u;

  if (__builtin_expect(wave == 0, 1)) {
    const int rs = lane >> 3, c = lane & 7;
    const int qbr = (w >> 1) & 1, half = (w >> 2) & 1, j = w >> 3, blk = (j >> 1) * 4 + (j & 1) * 2 + ((j ^ w) & 1);
    unsigned woff = (unsigned)((half * 288 + rs * 36) * NQ + blk * 64 + c * 8) * 2;
    asm volatile("" : "+v"(woff));
    GETARGS(qbr * 32 + 16, "s"(half));
    const rsrc_t wr = mkrs((const void*)a2[0], DM * NQ * 2);
    const rsrc_t wsr = mkrs((const void*)a4[3], WS_END);
    u32x4 wv[36], xr;
    unsigned epoch, so = 0;
#pragma unroll
    for (int t = 0; t < 36; ++t) {
      wv[t] = ldb<NT>(wr, woff, so); SB();
      so += NQ * 2; asm volatile("" : "+s"(so)); SB();
      if (t == XPOS) {
        xr = ldb<0>(mkrs((const char*)a2[1] + half * 576, 576), lane * 16, 0);
        epoch = __hip_atomic_load((GAS unsigned*)(a4[3] + WS_EPOCH), RLX_AGENT); SB();
      }
    }
    __builtin_amdgcn_s_barrier();
    SB();
    ST(8);
    if (lane < 36) *(u32x4*)&L.x[lane * 4] = xr;
    float acc[8] = {0, 0, 0, 0, 0, 0, 0, 0};
#pragma unroll
    for (int t4 = 0; t4 < 9; ++t4) {
      const u32x2 xw = *(const u32x2*)&L.x[rs * 18 + t4 * 2];
#pragma unroll
      for (int u = 0; u < 4; ++u) {
        const int t = t4 * 4 + u;
        const unsigned xl = (u & 1) ? xw[u >> 1] >> 16 : xw[u >> 1] & 0xffffu, xh = (u & 1) ? xw[u >> 1] & 0xffff0000u : xw[u >> 1] << 16;
#pragma unroll
        for (int k = 0; k < 4; ++k) { acc[2 * k] = dot2(wv[t][k], xl, acc[2 * k]); acc[2 * k + 1] = dot2(wv[t][k], xh, acc[2 * k + 1]); }
      }
      SB();
    }
    float b[4];
#pragma unroll
    for (int k = 0; k < 4; ++k) { b[k] = fold32(acc[k], acc[k + 4]); b[k] = fold16(b[k], b[k]); b[k] += dpp<XOR8>(b[k]); }
    asm volatile("" : "+v"(epoch));
    tag = __builtin_amdgcn_readfirstlane(epoch) + 1u;
    tag += tag == 0u;
    stx(wsr, (lane & 24) ? OOR : (c * 2 + (lane >> 5)) * 16, WS_Q + (blk >> 2) * 4096 + (qbr * 2 + half) * 1024 + (blk & 3) * 256, seal4(b[0], b[1], b[2], b[3], tag));
    ST(1);
    __builtin_amdgcn_s_sleep(QSLEEP);
    u32x4 g[4];
    for (unsigned spins = 0; spins < SPIN_CAP; ++spins) {
#pragma unroll
      for (int i = 0; i < 4; ++i) g[i] = ldx(wsr, lane * 16 + i * 1024, WS_Q + h * 4096);
      if (!__any((bad(g[0], tag) | bad(g[1], tag) | bad(g[2], tag) | bad(g[3], tag)) != 0u)) break;
    }
#pragma unroll
    for (int bb = 0; bb < 2; ++bb) {
      const u32x4 p0 = g[2 * bb], p1 = g[2 * bb + 1];
      *(u32x2*)&L.q[bb][2 * lane] = u32x2{cvtpk(uf(p0[0]) + uf(p1[0]), uf(p0[1]) + uf(p1[1])), cvtpk(uf(p0[2]) + uf(p1[2]), fourth(p0) + fourth(p1))};
    }
    __builtin_amdgcn_fence(__ATOMIC_RELEASE, "workgroup");
    if (lane == 0) __hip_atomic_store(&L.qready, tag, RLX_WG);
    ST(2);
    if (PGAL) asm volatile("s_branch 1f\n\t.p2align 12\n1:");
  } else {
    const int br = wave == 1 || wave == 3 ? 0 : 1;
    const int pair = wave < 5 ? 3 : (wave - 5) >> 1, odd = wave < 5 ? (wave - 1) >> 1 : (wave - 5) & 1;
    const int g = lane >> 4, i = lane & 15;
    unsigned loff = (h * 2048 + pair * 512 + s * 32 + odd * 2) * 1024 + lane * 16;
    const bool late = wave < 4, closer = wave < 3;
    unsigned so = ((s + HR * h) & 7) * 4096, knead = KAFIRST && br ? 2u : 0u;
    if (tid == 64) { L.qready = 0u; L.kissued = 0u; L.issued = 0u; L.done[0] = 0u; L.done[1] = 0u; }
    asm volatile("" : "+v"(loff));
    asm volatile("" : "+s"(so), "+s"(knead));
    GETARGS(br * 32, "s"(so));
    const rsrc_t kr = mkrs((const void*)a2[0], 32 << 20), vr = mkrs((const void*)a2[1], 32 << 20);
    const rsrc_t wsr = mkrs((const void*)a4[3], WS_END);
    asm volatile("" :: "s"(kr), "s"(vr));
    __builtin_amdgcn_s_barrier();
    SB();
    ST(8);
    u32x4 K[16], V[16];
#define LOAD16(X, R) _Pragma("unroll") for (int t = 0; t < 16; t += 2) { \
      X[t] = ldb<NT>(R, loff, so); SB(); unsigned nx = so + 4096u; asm volatile("" : "+s"(nx)); SB(); \
      X[t + 1] = ldb<NT>(R, loff + 1024, so); SB(); so = nx & 0x7000u; asm volatile("" : "+s"(so)); SB(); }
    if (knead) lds_wait(&L.kissued, knead);
    SB();
#pragma unroll
    for (int t = 0; t < 16; t += 2)
      asm volatile("buffer_load_dwordx4 %0, %3, %4, %2 offen nt\n\ts_nop 0\n\tbuffer_load_dwordx4 %1, %3, %4, %2 offen offset:1024 nt\n\t"
                   "s_addk_i32 %2, 0x1000\n\ts_and_b32 %2, %2, 0x7000" : "=&v"(K[t]), "=&v"(K[t + 1]), "+s"(so) : "v"(loff), "s"(kr) : "scc");
    SB();
    lds_inc(&L.kissued, lane);
    ST(9);
    if (!late) {
      lds_wait(&L.kissued, 10u);
      SB();
      LOAD16(V, vr)
      lds_inc(&L.issued, lane);
      ST(1);
      asm volatile("s_waitcnt vmcnt(16)" ::: "memory");
    } else {
      asm volatile("s_waitcnt vmcnt(0)" ::: "memory");
    }
#pragma unroll
    for (int t = 0; t < 16; ++t) asm volatile("" : "+v"(K[t]));
    lds_wait(&L.qready, 1u);
    tag = __builtin_amdgcn_readfirstlane(__hip_atomic_load(&L.qready, RLX_WG));
    __builtin_amdgcn_fence(__ATOMIC_ACQUIRE, "workgroup");
    const u32x4 q0 = *(const u32x4*)&L.q[br][4 * i], q1 = *(const u32x4*)&L.q[br][64 + 4 * i];
    constexpr float C2 = 0.088388347648318440f * 1.4426950408889634f;
    float sc[16], m = -INFINITY;
#pragma unroll
    for (int t = 0; t < 16; ++t) {
      float d0 = 0, d1 = 0;
#pragma unroll
      for (int k = 0; k < 4; ++k) { d0 = dot2(K[t][k], q0[k], d0); d1 = dot2(K[t][k], q1[k], d1); }
      d0 += dpp<XOR8>(d0); d1 += dpp<XOR8>(d1);
      float d = (i & 8) ? d1 : d0;
      d += dpp<XOR7>(d); d += dpp<XOR2>(d); d += dpp<XOR1>(d);
      sc[t] = d; m = fmaxf(m, d);
    }
    m = max16_32(m) * C2;
    unsigned msk[4];
#pragma unroll
    for (int k = 0; k < 4; ++k) msk[k] = (((i & 7) >> 1) == k) ? ((i & 1) ? 0xffff0000u : 0x0000ffffu) : 0u;
    float l = 0;
    u32x4 a[16];
#pragma unroll
    for (int t = 0; t < 16; ++t) {
      const float p = __builtin_amdgcn_exp2f(fmaf(sc[t], C2, -m));
      const unsigned pp = cvtpk(p, p);
      l += p;
      a[t] = u32x4{pp & msk[0], pp & msk[1], pp & msk[2], pp & msk[3]};
    }
    l = fold16(l, l); l = fold32(l, l);
    if ((lane & 55) == 0) *(float2*)&L.part[wave][lane >> 3][0] = make_float2(m, l);
#pragma unroll
    for (int t = 0; t < 16; ++t) asm volatile("" : "+v"(a[t]));
    SB();
    ST(2);
    if (late) {
      __builtin_amdgcn_fence(__ATOMIC_RELEASE, "workgroup");
      lds_wait(&L.issued, wave == 2 ? 7u : wave == 3 ? 8u : 9u);
      SB();
      LOAD16(V, vr)
      lds_inc(&L.issued, lane);
      ST(1);
    }
    const int hd = g >> 1, oi = 4 + 8 * i + 4 * (g & 1);
    float f = 0, eo = 0;
    unsigned lse = 0, poff = WS_O + h * 32768 + br * 16384 + s * 128 + (lane >> 3) * 2048 + (lane & 7) * 16;
    if (wave == 1) {
      __builtin_amdgcn_fence(__ATOMIC_ACQUIRE, "workgroup");
      const float2 me = *(const float2*)&L.part[1][hd][0], r = *(const float2*)&L.part[3][hd][0];
      const float M = fmax2(me.x, r.x);
      f = __builtin_amdgcn_exp2f(me.x - M); eo = __builtin_amdgcn_exp2f(r.x - M);
      const float Ls = fmaf(f, me.y, eo * r.y), inv = __builtin_amdgcn_rcpf(Ls);
      f *= inv; eo *= inv;
      lse = fu(M + __builtin_amdgcn_logf(Ls));
    }
    asm volatile("" : "+v"(f), "+v"(eo), "+v"(lse), "+v"(poff));
    SB();
    f32x4 acc = {0, 0, 0, 0};
#pragma unroll
    for (int t = 0; t < MID; ++t) acc = mfma(a[t], V[t], acc);
    asm volatile("" : "+v"(acc));
    SB();
    f32x4 base = {0, 0, 0, 0};
    if (closer) {
      lds_wait(&L.done[br], br ? 7u : 1u);
      __builtin_amdgcn_fence(__ATOMIC_ACQUIRE, "workgroup");
      if (br) {
        const float2 me = *(const float2*)&L.part[2][hd][0];
        float2 r[7]; f32x4 o[7];
#pragma unroll
        for (int u = 0; u < 7; ++u) { r[u] = *(const float2*)&L.part[4 + u][hd][0]; o[u] = *(const f32x4*)&L.part[4 + u][hd][oi]; }
        float M = me.x;
#pragma unroll
        for (int u = 0; u < 7; ++u) M = fmax2(M, r[u].x);
        f = __builtin_amdgcn_exp2f(me.x - M);
        float Ls = f * me.y;
#pragma unroll
        for (int u = 0; u < 7; ++u) { const float e = __builtin_amdgcn_exp2f(r[u].x - M); Ls = fmaf(e, r[u].y, Ls); base += e * o[u]; }
        eo = __builtin_amdgcn_rcpf(Ls);
        f *= eo;
        lse = fu(M + __builtin_amdgcn_logf(Ls));
      } else {
        base = *(const f32x4*)&L.part[3][hd][oi];
      }
      ST(4);
    }
    SB();
#pragma unroll
    for (int t = MID; t < 15; ++t) acc = mfma(a[t], V[t], acc);
    base *= eo;
    asm volatile("" : "+v"(base));
    acc = mfma(a[15], V[15], acc);
    if (closer) {
      acc = acc * f + base;
      stx(wsr, poff, 0, seal(cvtpk(acc[0], acc[1]), cvtpk(acc[2], acc[3]), lse, tag));
    } else {
      *(f32x4*)&L.part[wave][hd][oi] = acc;
      __builtin_amdgcn_fence(__ATOMIC_RELEASE, "workgroup");
      lds_inc(&L.done[br], lane);
    }
    ST(3);
  }
  const rsrc_t wsr = mkrs((const void*)a4[3], WS_END);
  GAS unsigned char* ws = (GAS unsigned char*)a4[3];
  const int gw = GW;
  const int pw = wave == 0 ? 8 : wave == PW0 ? 0 : wave < 4 ? -1 : wave - 3;
  if (pw >= 0) {
    u32x4 wf[4];
    unsigned yoff, oaddr = (lane >> 4) * 16;
    lds_wait(&L.issued, 10u);
    if (WOGATE) for (unsigned spins = 0; (unsigned)__builtin_amdgcn_s_memrealtime() - tstart < WOGATE && spins < SPIN_CAP; ++spins) __builtin_amdgcn_s_sleep(1);
    const int row0 = hp * 256 + ((s >> 2) & 1) * 128 + (s & 1) * 64 + (s & 2) * 2;
    const rsrc_t wr = mkrs((const uint16_t*)(pbr ? a4[1] : a4[0]) + row0 * DM + pw * 64, 60 * DM * 2);
    const unsigned woff = ((8 * (lane >> 5) + ((lane >> 3) & 3)) * DM + (lane & 7) * 8) * 2;
    LAS unsigned char* dst = (LAS unsigned char*)&L.wo[pw][0];
#pragma unroll
    for (int t = 0; t < 4; ++t) __builtin_amdgcn_raw_ptr_buffer_load_lds(wr, dst + t * 1024, 16, woff, t * 16 * DM * 2, 0, NT);
    yoff = lane < 16 ? WS_Y + (pbr * 18 + pw * 2 + (lane >> 3)) * 16384 + (hp * 8 + (s & 7)) * 128 + (lane & 7) * 16 : OOR;
    asm volatile("s_waitcnt vmcnt(0)" ::: "memory");
    LAS unsigned char* src = dst + (lane >> 4) * 1024 + ((lane >> 2) & 3) * 128 + (lane & 3) * 8;
#pragma unroll
    for (int b = 0; b < 4; ++b) {
      const u32x2 lo = __builtin_bit_cast(u32x2, __builtin_amdgcn_ds_read_tr16_b64_v4i16((LAS s16x4*)(src + b * 32)));
      const u32x2 hi = __builtin_bit_cast(u32x2, __builtin_amdgcn_ds_read_tr16_b64_v4i16((LAS s16x4*)(src + b * 32 + 512)));
      wf[b] = u32x4{lo[0], lo[1], hi[0], hi[1]};
    }
    unsigned k0 = 0x07060501u, k1 = 0x07060502u, k2 = 0x07060503u;
    asm volatile("" : "+s"(k0), "+s"(k1), "+s"(k2) : "s"(wsr));
    asm volatile("" : "+v"(wf[0]), "+v"(wf[1]), "+v"(wf[2]), "+v"(wf[3]), "+v"(yoff), "+v"(oaddr));
    ST(9);
    asm volatile("s_barrier" ::: "memory");
    ST(11);
    const u32x4 ov = *(const u32x4*)((const char*)L.o + oaddr);
    const f32x4 z = {0, 0, 0, 0};
    f32x4 y3 = mfma(ov, wf[3], z), y0 = mfma(ov, wf[0], z), y1 = mfma(ov, wf[1], z), y2 = mfma(ov, wf[2], z);
    SB();
    const unsigned p0 = __builtin_amdgcn_perm(fu(y0[0]), fu(y3[0]), k0), p1 = __builtin_amdgcn_perm(fu(y1[0]), fu(y3[0]), k1);
    const unsigned c01 = xor3(p0, p1, tag), p2 = __builtin_amdgcn_perm(fu(y2[0]), fu(y3[0]), k2);
    stx(wsr, yoff, 0, u32x4{p0, p1, p2, c01 ^ p2});
    asm volatile("" :: "v"(y0), "v"(y1), "v"(y2), "v"(y3));
  } else if (wave == gw) {
    const unsigned oa = (unsigned)(size_t)(LAS void*)&L.o[4 * (lane & 7) + (lane >> 4)];
    unsigned left = SPIN_CAP;
    __builtin_amdgcn_s_sleep(FIRST1);
    asm volatile(POLL2("")
        "v_max_f32 v112, v102, v106\n\tv_lshlrev_b32 v108, 16, v100\n\tv_and_b32 v109, 0xffff0000, v100\n\t"
        "v_max_f32_dpp v112, v112, v112 row_ror:8 row_mask:0xf bank_mask:0xf\n\tv_lshlrev_b32 v110, 16, v101\n\t"
        "v_mov_b32 v113, v112\n\tv_and_b32 v111, 0xffff0000, v101\n\tv_lshlrev_b32 v114, 16, v104\n\t"
        "v_permlane16_swap_b32 v112, v113\n\tv_max_f32 v112, v112, v113\n\t"
        "v_mov_b32 v113, v112\n\tv_and_b32 v115, 0xffff0000, v104\n\tv_lshlrev_b32 v116, 16, v105\n\t"
        "v_permlane32_swap_b32 v112, v113\n\tv_max_f32 v112, v112, v113\n\t"
        "v_sub_f32 v102, v102, v112\n\tv_sub_f32 v106, v106, v112\n\tv_exp_f32 v102, v102\n\tv_exp_f32 v106, v106\n\t"
        "v_and_b32 v117, 0xffff0000, v105\n\tv_add_f32 v112, v102, v106\n\t"
        "v_pk_mul_f32 v[108:109], v[108:109], v[102:103] op_sel_hi:[1,0]\n\tv_pk_mul_f32 v[110:111], v[110:111], v[102:103] op_sel_hi:[1,0]\n\t"
        "v_add_f32_dpp v112, v112, v112 row_ror:8 row_mask:0xf bank_mask:0xf\n\t"
        "v_pk_fma_f32 v[108:109], v[114:115], v[106:107], v[108:109] op_sel_hi:[1,0,1]\n\tv_mov_b32 v113, v112\n\t"
        "v_pk_fma_f32 v[110:111], v[116:117], v[106:107], v[110:111] op_sel_hi:[1,0,1]\n\ts_nop 0\n\t"
        "v_permlane16_swap_b32 v112, v113\n\tv_permlane32_swap_b32 v108, v110\n\tv_permlane32_swap_b32 v109, v111\n\t"
        "v_add_f32 v112, v112, v113\n\tv_add_f32 v108, v108, v110\n\tv_add_f32 v109, v109, v111\n\tv_mov_b32 v113, v112\n\ts_nop 0\n\t"
        "v_permlane16_swap_b32 v108, v109\n\tv_permlane32_swap_b32 v112, v113\n\t"
        "v_add_f32 v108, v108, v109\n\tv_add_f32 v112, v112, v113\n\tv_rcp_f32 v112, v112\n\t"
        "v_add_f32_dpp v108, v108, v108 row_ror:8 row_mask:0xf bank_mask:0xf\n\tv_mul_f32 v108, v108, v112\n\t"
        "v_cvt_pk_bf16_f32 v108, v108, v108\n\tds_write_b16 %[la], v108"
        : [n] "+s"(left) : [vo] "v"(lane * 16), [rs] "s"(wsr), [so] "s"(WS_O + (hp * 16 + s) * 2048), [tag] "s"(tag), [la] "v"(oa) : POLLCLOB);
    ST(5);
    asm volatile("s_waitcnt lgkmcnt(0)\n\ts_barrier" ::: "memory");
  } else {
    asm volatile("s_barrier" ::: "memory");
  }
  ST(6);
  const unsigned rd = w - ROFF;
  if (rd >= NRED) { STDUMP(); return; }
  if (wave < 8) {
    const unsigned wa = (unsigned)(size_t)(LAS void*)&L.fin[0][0] + ((lane >> 4) * 4 + ((lane & 7) >> 1)) * 64 + wave * 8 + (lane & 1) * 4;
    unsigned left = SPIN_CAP;
    __builtin_amdgcn_s_sleep(FIRST2);
    asm volatile(POLL2("v_perm_b32 v110, v101, v100, %[k0]\n\tv_perm_b32 v110, v102, v110, %[k1]\n\t")
        "v_perm_b32 v111, v105, v104, %[k0]\n\tv_pk_add_f32 v[100:101], v[100:101], v[104:105]\n\tv_add_f32 v102, v102, v106\n\t"
        "v_perm_b32 v111, v106, v111, %[k1]\n\tv_add_f32 v110, v110, v111\n\t"
        "v_permlane32_swap_b32 v100, v102\n\tv_add_f32 v100, v100, v102\n\t"
        "v_permlane32_swap_b32 v101, v110\n\tv_add_f32 v101, v101, v110\n\ts_nop 1\n\t"
        "v_permlane16_swap_b32 v100, v101\n\tv_add_f32 v100, v100, v101\n\ts_nop 1\n\t"
        "v_add_f32_dpp v100, v100, v100 row_ror:8 row_mask:0xf bank_mask:0xf\n\tds_write_b32 %[la], v100"
        : [n] "+s"(left) : [vo] "v"(lane * 16), [rs] "s"(wsr), [so] "s"(WS_Y + rd * 16384 + wave * 2048), [tag] "s"(tag), [la] "v"(wa),
          [k0] "s"(0x0c04000cu), [k1] "s"(0x04020100u) : POLLCLOB);
    asm volatile("s_waitcnt lgkmcnt(0)\n\ts_barrier" ::: "memory");
  } else if (wave == 8) {
    unsigned fa = (unsigned)(size_t)(LAS void*)&L.fin[0][0] + (lane & 15) * 64, ooff = lane < 16 ? ((rd >> 1) * 32 + (rd & 1) * 4 + (lane >> 2) * 8 + (lane & 3)) * 4 : OOR;
    const rsrc_t outr = mkrs((const void*)a4[2], 2 * DM * 2);
    asm volatile("" : "+v"(fa), "+v"(ooff) : "s"(outr));
    asm volatile("s_barrier" ::: "memory");
    const LAS f32x4* f = (const LAS f32x4*)(size_t)fa;
    const f32x4 t = (f[0] + f[1]) + (f[2] + f[3]);
    __builtin_amdgcn_raw_buffer_store_b32(cvtpk(t[0] + t[2], t[1] + t[3]), outr, ooff, 0, OUTSC);
    ST(7);
  } else {
    asm volatile("s_barrier" ::: "memory");
    if (rd == BUMP && tid == 576) __hip_atomic_store((GAS unsigned*)(ws + WS_EPOCH), tag, RLX_AGENT);
  }
  STDUMP();
}

extern "C" void kernel_launch(void* const* d_in, const int* in_sizes, int n_in, void* d_out, int out_size, void* d_ws, size_t ws_size, hipStream_t stream) {
  fused_decode<<<256, NTHR, 0, stream>>>(d_in[1], d_in[2], d_in[3], d_in[0], d_in[6], d_in[7], d_in[8], d_in[5], d_in[4], d_in[9], d_out, d_ws);
}
```

```cpp
#include <hip/hip_runtime.h>
#include <hip/hip_bf16.h>
#include <stdint.h>

#define GAS __attribute__((address_space(1)))
#define LAS __attribute__((address_space(3)))
#define RLX_AGENT __ATOMIC_RELAXED, __HIP_MEMORY_SCOPE_AGENT
#define RLX_WG __ATOMIC_RELAXED, __HIP_MEMORY_SCOPE_WORKGROUP
typedef unsigned u32x4 __attribute__((ext_vector_type(4)));
typedef unsigned u32x2 __attribute__((ext_vector_type(2)));
typedef float f32x4 __attribute__((ext_vector_type(4)));
typedef short s16x4 __attribute__((ext_vector_type(4)));
typedef __bf16 bf16x2 __attribute__((ext_vector_type(2)));
typedef __bf16 bf16x8 __attribute__((ext_vector_type(8)));
typedef unsigned long long u64;
typedef u64 u64x2 __attribute__((ext_vector_type(2)));
typedef u64 u64x4 __attribute__((ext_vector_type(4)));
typedef __amdgpu_buffer_rsrc_t rsrc_t;

constexpr int DM = 576, NQ = 4096;
constexpr int NWAVE = 11, NTHR = NWAVE * 64, NRED = 36;
constexpr unsigned WS_EPOCH = 0, WS_Q = 4096, WS_O = WS_Q + 16 * 4096, WS_Y = WS_O + 256 * 2048, WS_END = WS_Y + NRED * 16384;
constexpr size_t WS_STAMP = 8u << 20;
constexpr unsigned SPIN_CAP = 1u << 17;

#define FIRST1 4
#define FIRST2 16
#define QSLEEP 64
#define XPOS 20
#define HSH 4
#define MID 10
#define HFIRST 1
#define ROFF 0
#define BUMP 35
#define HR 3
#define GAPS 2
#define PGAL 1
#define TLBP 3
#define RND 1
#define OPF 0
#define PW0 1
#define GW 3
#define KWARM 1
#define KAFIRST 1
#define WOGATE 0
#define PIPE 0
#define OUTSC 16
constexpr int NT = 2;
constexpr unsigned OOR = 0x40000000u;

__device__ __forceinline__ float bflo(unsigned u) { return __builtin_bit_cast(float, u << 16); }
__device__ __forceinline__ float bfhi(unsigned u) { return __builtin_bit_cast(float, u & 0xffff0000u); }
__device__ __forceinline__ unsigned fu(float f) { return __float_as_uint(f); }
__device__ __forceinline__ float uf(unsigned u) { return __uint_as_float(u); }
__device__ __forceinline__ unsigned cvtpk(float lo, float hi) { bf16x2 v = {(__bf16)lo, (__bf16)hi}; return __builtin_bit_cast(unsigned, v); }
__device__ __forceinline__ float dot2(unsigned k, unsigned q, float acc) {
  return __builtin_amdgcn_fdot2_f32_bf16(__builtin_bit_cast(bf16x2, k), __builtin_bit_cast(bf16x2, q), acc, false);
}
template <int CTRL> __device__ __forceinline__ float dpp(float x) {
  return __builtin_bit_cast(float, __builtin_amdgcn_mov_dpp(__builtin_bit_cast(int, x), CTRL, 0xf, 0xf, true));
}
constexpr int XOR1 = 0xB1, XOR2 = 0x4E, XOR7 = 0x141, XOR8 = 0x128;
__device__ __forceinline__ f32x4 mfma(u32x4 a, u32x4 b, f32x4 c) {
  return __builtin_amdgcn_mfma_f32_16x16x32_bf16(__builtin_bit_cast(bf16x8, a), __builtin_bit_cast(bf16x8, b), c, 0, 0, 0);
}
__device__ __forceinline__ float fold32(float a, float b) {
  auto r = __builtin_amdgcn_permlane32_swap(fu(a), fu(b), false, false);
  return uf(r[0]) + uf(r[1]);
}
__device__ __forceinline__ float fold16(float a, float b) {
  auto r = __builtin_amdgcn_permlane16_swap(fu(a), fu(b), false, false);
  return uf(r[0]) + uf(r[1]);
}
__device__ __forceinline__ float fmax2(float a, float b) { return __builtin_amdgcn_fmed3f(a, b, INFINITY); }
__device__ __forceinline__ float max16_32(float x) {
  auto s = __builtin_amdgcn_permlane16_swap(fu(x), fu(x), false, false);
  x = fmax2(uf(s[0]), uf(s[1]));
  auto t = __builtin_amdgcn_permlane32_swap(fu(x), fu(x), false, false);
  return fmax2(uf(t[0]), uf(t[1]));
}
__device__ __forceinline__ rsrc_t mkrs(const void* p, unsigned bytes) { return __builtin_amdgcn_make_buffer_rsrc((void*)p, 0, bytes, 0x00020000); }
template <int AUX> __device__ __forceinline__ u32x4 ldb(rsrc_t r, unsigned voff, unsigned soff) { return __builtin_amdgcn_raw_buffer_load_b128(r, voff, soff, AUX); }
constexpr int SC1 = 16;
__device__ __forceinline__ u32x4 ldx(rsrc_t r, unsigned voff, unsigned soff) { asm volatile("" ::: "memory"); return __builtin_amdgcn_raw_buffer_load_b128(r, voff, soff, SC1); }
__device__ __forceinline__ void stx(rsrc_t r, unsigned voff, unsigned soff, u32x4 v) { __builtin_amdgcn_raw_buffer_store_b128(v, r, voff, soff, SC1); }
__device__ __forceinline__ unsigned xor3(unsigned a, unsigned b, unsigned c) { return __builtin_amdgcn_bitop3_b32(a, b, c, 0x96); }
__device__ __forceinline__ u32x4 seal(unsigned a, unsigned b, unsigned c, unsigned tag) { return u32x4{a, b, c, xor3(a, b, c ^ tag)}; }
__device__ __forceinline__ unsigned bad(u32x4 g, unsigned tag) { return xor3(xor3(g[0], g[1], g[2]), g[3], tag); }
__device__ __forceinline__ u32x4 seal4(float a, float b, float c, float d, unsigned tag) {
  return seal(__builtin_amdgcn_perm(fu(a), fu(d), 0x07060501u), __builtin_amdgcn_perm(fu(b), fu(d), 0x07060502u), __builtin_amdgcn_perm(fu(c), fu(d), 0x07060503u), tag);
}
__device__ __forceinline__ float fourth(u32x4 g) { return uf(__builtin_amdgcn_perm(g[2], __builtin_amdgcn_perm(g[1], g[0], 0x0c04000cu), 0x04020100u)); }
#define SB() __builtin_amdgcn_sched_barrier(0)
#define ST(i)
#define STDUMP()
#define STDECL()
#define STPAR
#define STARG

struct Lds {
  unsigned char wo[9][4096];
  float part[11][2][132];
  unsigned q[2][128];
  unsigned x[144];
  float fin[8][32];
  unsigned short o[32];
  unsigned qready, kissued, issued, done[2];
};

__device__ __forceinline__ void lds_wait(unsigned* p, unsigned n) {
  for (unsigned spins = 0; __hip_atomic_load(p, RLX_WG) < n && spins < (SPIN_CAP << 3); ++spins) __builtin_amdgcn_s_sleep(1);
}
__device__ __forceinline__ void lds_inc(unsigned* p, int lane) { if (lane == 0) __hip_atomic_fetch_add(p, 1u, RLX_WG); }
#define POLL2(MID_) \
    "1:\n\tbuffer_load_dwordx4 v[100:103], %[vo], %[rs], %[so] offen sc1\n\tbuffer_load_dwordx4 v[104:107], %[vo], %[rs], %[so] offen offset:1024 sc1\n\t" \
    "s_sub_u32 %[n], %[n], 1\n\ts_waitcnt vmcnt(1)\n\t" \
    "v_bitop3_b32 v108, v100, v101, v102 bitop3:0x96\n\tv_bitop3_b32 v108, v108, v103, %[tag] bitop3:0x96\n\t" MID_ "s_waitcnt vmcnt(0)\n\t" \
    "v_bitop3_b32 v109, v104, v105, v106 bitop3:0x96\n\tv_bitop3_b32 v109, v109, v107, %[tag] bitop3:0x96\n\t" \
    "v_or_b32 v108, v108, v109\n\tv_cmp_ne_u32 vcc, 0, v108\n\ts_cbranch_vccz 2f\n\ts_cbranch_scc0 1b\n2:\n\t"
#define POLLCLOB "memory", "vcc", "scc", "v100", "v101", "v102", "v103", "v104", "v105", "v106", "v107", "v108", "v109", "v110", "v111", "v112", "v113", "v114", "v115", "v116", "v117"

__global__ void __launch_bounds__(NTHR, 3) fused_decode(const void* kA, const void* vA, const void* wqA, const void* xA, const void* kB, const void* vB, const void* wqB, const void* xB,
                                                         const void* woA, const void* woB, void* out, void* wsp) {
  __shared__ __attribute__((aligned(16))) Lds L;
  STDECL();
  ST(0);
  const auto karg = __builtin_amdgcn_kernarg_segment_ptr();
  if (KWARM) asm volatile("s_load_dword s94, %0, 0x0\n\ts_load_dword s95, %0, 0x40" :: "s"(karg) : "s94", "s95");
  SB();
  u64x2 a2; u64x4 a4;
#define GETARGS(OFF, ...) asm volatile("s_load_dwordx4 %0, %[ka], %[off]\n\ts_load_dwordx8 %1, %[ka], 0x40\n\ts_waitcnt lgkmcnt(0)" : "=&s"(a2), "=&s"(a4) : [ka] "s"(karg), [off] "s"(OFF), __VA_ARGS__ : "memory")
  const int tid = threadIdx.x, lane = tid & 63, wave = __builtin_amdgcn_readfirstlane(tid >> 6);
  const int w = blockIdx.x, h = (w & 7) * 2 + (w >> 7), s = (w >> 3) & 15;
  const int hp = (h + HSH) & 15, pbr = s >> 3;
  unsigned tag;
  const unsigned tstart = WOGATE ? (unsigned)__builtin_amdgcn_s_memrealtime() : 0u;

  if (__builtin_expect(wave == 0, 1)) {
    const int rs = lane >> 3, c = lane & 7;
    const int qbr = (w >> 1) & 1, half = (w >> 2) & 1, j = w >> 3, blk = (j >> 1) * 4 + (j & 1) * 2 + ((j ^ w) & 1);
    unsigned woff = (unsigned)((half * 288 + rs * 36) * NQ + blk * 64 + c * 8) * 2;
    asm volatile("" : "+v"(woff));
    GETARGS(qbr * 32 + 16, "s"(half));
    const rsrc_t wr = mkrs((const void*)a2[0], DM * NQ * 2);
    const rsrc_t wsr = mkrs((const void*)a4[3], WS_END);
    if (TLBP & 8) asm volatile("s_load_dword s94, %0, %1\n\ts_load_dword s95, %0, %2" :: "s"(a2[0]), "s"((half * 288 + 287) * NQ * 2), "s"(half * 288 * NQ * 2) : "s94", "s95");
    SB();
    u32x4 wv[36], xr;
    unsigned epoch, so = 0;
#pragma unroll
    for (int t = 0; t < 36; ++t) {
      wv[t] = ldb<NT>(wr, woff, so); SB();
      so += NQ * 2; asm volatile("" : "+s"(so)); SB();
      if ((TLBP & 1) && t == 0) asm volatile("s_load_dword s94, %0, 0x0" :: "s"(a2[1]) : "s94");
      if (t == XPOS) {
        xr = ldb<0>(mkrs((const char*)a2[1] + half * 576, 576), lane * 16, 0);
        epoch = __hip_atomic_load((GAS unsigned*)(a4[3] + WS_EPOCH), RLX_AGENT); SB();
      }
    }
    __builtin_amdgcn_s_barrier();
    SB();
    ST(8);
    if (lane < 36) *(u32x4*)&L.x[lane * 4] = xr;
    float acc[8] = {0, 0, 0, 0, 0, 0, 0, 0};
#pragma unroll
    for (int t4 = 0; t4 < 9; ++t4) {
      const u32x2 xw = *(const u32x2*)&L.x[rs * 18 + t4 * 2];
#pragma unroll
      for (int u = 0; u < 4; ++u) {
        const int t = t4 * 4 + u;
        const unsigned xl = (u & 1) ? xw[u >> 1] >> 16 : xw[u >> 1] & 0xffffu, xh = (u & 1) ? xw[u >> 1] & 0xffff0000u : xw[u >> 1] << 16;
#pragma unroll
        for (int k = 0; k < 4; ++k) { acc[2 * k] = dot2(wv[t][k], xl, acc[2 * k]); acc[2 * k + 1] = dot2(wv[t][k], xh, acc[2 * k + 1]); }
      }
      SB();
    }
    float b[4];
#pragma unroll
    for (int k = 0; k < 4; ++k) { b[k] = fold32(acc[k], acc[k + 4]); b[k] = fold16(b[k], b[k]); b[k] += dpp<XOR8>(b[k]); }
    asm volatile("" : "+v"(epoch));
    tag = __builtin_amdgcn_readfirstlane(epoch) + 1u;
    tag += tag == 0u;
    stx(wsr, (lane & 24) ? OOR : (c * 2 + (lane >> 5)) * 16, WS_Q + (blk >> 2) * 4096 + (qbr * 2 + half) * 1024 + (blk & 3) * 256, seal4(b[0], b[1], b[2], b[3], tag));
    unsigned opf = 0;
    if (OPF) opf = __builtin_amdgcn_raw_buffer_load_b32(mkrs((const void*)a4[2], w < NRED ? 4 : 0), lane * 4, 0, SC1);
    if (TLBP & 4) asm volatile("s_load_dword s94, %0, %2\n\ts_load_dword s95, %1, 0x0" :: "s"(pbr ? a4[1] : a4[0]), "s"(a4[2]), "s"((hp * 256 + (s & 4) * 32) * DM * 2) : "s94", "s95");
    ST(1);
    __builtin_amdgcn_s_sleep(QSLEEP);
    u32x4 g[4];
    for (unsigned spins = 0; spins < SPIN_CAP; ++spins) {
#pragma unroll
      for (int i = 0; i < 4; ++i) g[i] = ldx(wsr, lane * 16 + i * 1024, WS_Q + h * 4096);
      if (!__any((bad(g[0], tag) | bad(g[1], tag) | bad(g[2], tag) | bad(g[3], tag)) != 0u)) break;
    }
#pragma unroll
    for (int bb = 0; bb < 2; ++bb) {
      const u32x4 p0 = g[2 * bb], p1 = g[2 * bb + 1];
      *(u32x2*)&L.q[bb][2 * lane] = u32x2{cvtpk(uf(p0[0]) + uf(p1[0]), uf(p0[1]) + uf(p1[1])), cvtpk(uf(p0[2]) + uf(p1[2]), fourth(p0) + fourth(p1))};
    }
    __builtin_amdgcn_fence(__ATOMIC_RELEASE, "workgroup");
    if (lane == 0) __hip_atomic_store(&L.qready, tag, RLX_WG);
    asm volatile("" :: "v"(opf));
    ST(2);
    if (PGAL) asm volatile("s_branch 1f\n\t.p2align 12\n1:");
  } else {
    const int br = wave == 1 || wave == 3 ? 0 : 1;
    const int pair = wave < 5 ? 3 : (wave - 5) >> 1, odd = wave < 5 ? (wave - 1) >> 1 : (wave - 5) & 1;
    const int g = lane >> 4, i = lane & 15;
    const unsigned sbase = (h * 2048 + pair * 512 + s * 32 + odd * 2) * 1024;
    unsigned loff = sbase + lane * 16;
    const bool late = wave < 4, closer = wave < 3;
    unsigned so = ((s + HR * h) & 7) * 4096, knead = KAFIRST && br ? 2u : 0u;
    if (tid == 64) { L.qready = 0u; L.kissued = 0u; L.issued = 0u; L.done[0] = 0u; L.done[1] = 0u; }
    asm volatile("" : "+v"(loff));
    asm volatile("" : "+s"(so), "+s"(knead));
    GETARGS(br * 32, "s"(so));
    const rsrc_t kr = mkrs((const void*)a2[0], 32 << 20), vr = mkrs((const void*)a2[1], 32 << 20);
    const rsrc_t wsr = mkrs((const void*)a4[3], WS_END);
    if ((TLBP & 2) && (unsigned)(wave - 3) < 2u) asm volatile("s_load_dword s94, %0, %2\n\ts_load_dword s95, %1, %2" :: "s"(a2[0]), "s"(a2[1]), "s"(sbase) : "s94", "s95");
    asm volatile("" :: "s"(kr), "s"(vr));
    __builtin_amdgcn_s_barrier();
    SB();
    ST(8);
    u32x4 K[16], V[16];
#define LOAD16(X, R) _Pragma("unroll") for (int t = 0; t < 16; t += 2) { \
      X[t] = ldb<NT>(R, loff, so); SB(); unsigned nx = so + 4096u; asm volatile("" : "+s"(nx)); SB(); \
      X[t + 1] = ldb<NT>(R, loff + 1024, so); SB(); so = nx & 0x7000u; asm volatile("" : "+s"(so)); SB(); }
    if (knead) lds_wait(&L.kissued, knead);
    SB();
#pragma unroll
    for (int t = 0; t < 16; t += 2)
      asm volatile("buffer_load_dwordx4 %0, %3, %4, %2 offen nt\n\ts_nop 0\n\tbuffer_load_dwordx4 %1, %3, %4, %2 offen offset:1024 nt\n\t"
                   "s_addk_i32 %2, 0x1000\n\ts_and_b32 %2, %2, 0x7000" : "=&v"(K[t]), "=&v"(K[t + 1]), "+s"(so) : "v"(loff), "s"(kr) : "scc");
    SB();
    lds_inc(&L.kissued, lane);
    ST(9);
    if (!late) {
      lds_wait(&L.kissued, 10u);
      SB();
      LOAD16(V, vr)
      lds_inc(&L.issued, lane);
      ST(1);
      asm volatile("s_waitcnt vmcnt(16)" ::: "memory");
    } else {
      asm volatile("s_waitcnt vmcnt(0)" ::: "memory");
    }
#pragma unroll
    for (int t = 0; t < 16; ++t) asm volatile("" : "+v"(K[t]));
    lds_wait(&L.qready, 1u);
    tag = __builtin_amdgcn_readfirstlane(__hip_atomic_load(&L.qready, RLX_WG));
    __builtin_amdgcn_fence(__ATOMIC_ACQUIRE, "workgroup");
    const u32x4 q0 = *(const u32x4*)&L.q[br][4 * i], q1 = *(const u32x4*)&L.q[br][64 + 4 * i];
    constexpr float C2 = RND ? 1.4426950408889634f : 0.088388347648318440f * 1.4426950408889634f;
    float sc[16], m = -INFINITY;
#pragma unroll
    for (int t = 0; t < 16; ++t) {
      float d0 = 0, d1 = 0;
#pragma unroll
      for (int k = 0; k < 4; ++k) { d0 = dot2(K[t][k], q0[k], d0); d1 = dot2(K[t][k], q1[k], d1); }
      d0 += dpp<XOR8>(d0); d1 += dpp<XOR8>(d1);
      float d = (i & 8) ? d1 : d0;
      d += dpp<XOR7>(d); d += dpp<XOR2>(d); d += dpp<XOR1>(d);
      sc[t] = d;
    }
#pragma unroll
    for (int t = 0; t < 16; t += 2) {
      if (RND) {
        const unsigned r1 = cvtpk(sc[t], sc[t + 1]), r2 = cvtpk(bflo(r1) * 0.088388347648318440f, bfhi(r1) * 0.088388347648318440f);
        sc[t] = bflo(r2); sc[t + 1] = bfhi(r2);
      }
      m = __builtin_fmaxf(m, __builtin_fmaxf(sc[t], sc[t + 1]));
    }
    m = max16_32(m) * C2;
    unsigned msk[4];
#pragma unroll
    for (int k = 0; k < 4; ++k) msk[k] = (((i & 7) >> 1) == k) ? ((i & 1) ? 0xffff0000u : 0x0000ffffu) : 0u;
    float l = 0;
    u32x4 a[16];
#pragma unroll
    for (int t = 0; t < 16; ++t) {
      const float p = __builtin_amdgcn_exp2f(fmaf(sc[t], C2, -m));
      const unsigned pp = cvtpk(p, p);
      l += p;
      a[t] = u32x4{pp & msk[0], pp & msk[1], pp & msk[2], pp & msk[3]};
    }
    l = fold16(l, l); l = fold32(l, l);
    if ((lane & 55) == 0) *(float2*)&L.part[wave][lane >> 3][0] = make_float2(m, l);
#pragma unroll
    for (int t = 0; t < 16; ++t) asm volatile("" : "+v"(a[t]));
    SB();
    ST(2);
    if (late) {
      __builtin_amdgcn_fence(__ATOMIC_RELEASE, "workgroup");
      lds_wait(&L.issued, wave == 2 ? 7u : wave == 3 ? 8u : 9u);
      SB();
      LOAD16(V, vr)
      lds_inc(&L.issued, lane);
      ST(1);
    }
    const int hd = g >> 1, oi = 4 + 8 * i + 4 * (g & 1);
    float f = 0, eo = 0;
    unsigned lse = 0, poff = WS_O + h * 32768 + br * 16384 + s * 128 + (lane >> 3) * 2048 + (lane & 7) * 16;
    if (wave == 1) {
      __builtin_amdgcn_fence(__ATOMIC_ACQUIRE, "workgroup");
      const float2 me = *(const float2*)&L.part[1][hd][0], r = *(const float2*)&L.part[3][hd][0];
      const float M = fmax2(me.x, r.x);
      f = __builtin_amdgcn_exp2f(me.x - M); eo = __builtin_amdgcn_exp2f(r.x - M);
      const float Ls = fmaf(f, me.y, eo * r.y), inv = __builtin_amdgcn_rcpf(Ls);
      f *= inv; eo *= inv;
      lse = fu(M + __builtin_amdgcn_logf(Ls));
    }
    asm volatile("" : "+v"(f), "+v"(eo), "+v"(lse), "+v"(poff));
    SB();
    f32x4 acc = {0, 0, 0, 0};
#pragma unroll
    for (int t = 0; t < MID; ++t) acc = mfma(a[t], V[t], acc);
    asm volatile("" : "+v"(acc));
    SB();
    f32x4 base = {0, 0, 0, 0};
    if (closer) {
      lds_wait(&L.done[br], br ? 7u : 1u);
      __builtin_amdgcn_fence(__ATOMIC_ACQUIRE, "workgroup");
      if (br) {
        const float2 me = *(const float2*)&L.part[2][hd][0];
        float2 r[7]; f32x4 o[7];
#pragma unroll
        for (int u = 0; u < 7; ++u) { r[u] = *(const float2*)&L.part[4 + u][hd][0]; o[u] = *(const f32x4*)&L.part[4 + u][hd][oi]; }
        float M = me.x;
#pragma unroll
        for (int u = 0; u < 7; ++u) M = fmax2(M, r[u].x);
        f = __builtin_amdgcn_exp2f(me.x - M);
        float Ls = f * me.y;
#pragma unroll
        for (int u = 0; u < 7; ++u) { const float e = __builtin_amdgcn_exp2f(r[u].x - M); Ls = fmaf(e, r[u].y, Ls); base += e * o[u]; }
        eo = __builtin_amdgcn_rcpf(Ls);
        f *= eo;
        lse = fu(M + __builtin_amdgcn_logf(Ls));
      } else {
        base = *(const f32x4*)&L.part[3][hd][oi];
      }
      ST(4);
    }
    SB();
#pragma unroll
    for (int t = MID; t < 15; ++t) acc = mfma(a[t], V[t], acc);
    base *= eo;
    asm volatile("" : "+v"(base));
    acc = mfma(a[15], V[15], acc);
    if (closer) {
      acc = acc * f + base;
      stx(wsr, poff, 0, seal(cvtpk(acc[0], acc[1]), cvtpk(acc[2], acc[3]), lse, tag));
    } else {
      *(f32x4*)&L.part[wave][hd][oi] = acc;
      __builtin_amdgcn_fence(__ATOMIC_RELEASE, "workgroup");
      lds_inc(&L.done[br], lane);
    }
    ST(3);
  }
  const rsrc_t wsr = mkrs((const void*)a4[3], WS_END);
  GAS unsigned char* ws = (GAS unsigned char*)a4[3];
  const int gw = GW;
  const int pw = wave == 0 ? 8 : wave == PW0 ? 0 : wave < 4 ? -1 : wave - 3;
  if (pw >= 0) {
    u32x4 wf[4];
    unsigned yoff, oaddr = (lane >> 4) * 16;
    lds_wait(&L.issued, 10u);
    if (WOGATE) for (unsigned spins = 0; (unsigned)__builtin_amdgcn_s_memrealtime() - tstart < WOGATE && spins < SPIN_CAP; ++spins) __builtin_amdgcn_s_sleep(1);
    const int row0 = hp * 256 + ((s >> 2) & 1) * 128 + (s & 1) * 64 + (s & 2) * 2;
    const rsrc_t wr = mkrs((const uint16_t*)(pbr ? a4[1] : a4[0]) + row0 * DM + pw * 64, 60 * DM * 2);
    const unsigned woff = ((8 * (lane >> 5) + ((lane >> 3) & 3)) * DM + (lane & 7) * 8) * 2;
    LAS unsigned char* dst = (LAS unsigned char*)&L.wo[pw][0];
#pragma unroll
    for (int t = 0; t < 4; ++t) __builtin_amdgcn_raw_ptr_buffer_load_lds(wr, dst + t * 1024, 16, woff, t * 16 * DM * 2, 0, NT);
    yoff = lane < 16 ? WS_Y + (pbr * 18 + pw * 2 + (lane >> 3)) * 16384 + (hp * 8 + (s & 7)) * 128 + (lane & 7) * 16 : OOR;
    asm volatile("s_waitcnt vmcnt(0)" ::: "memory");
    LAS unsigned char* src = dst + (lane >> 4) * 1024 + ((lane >> 2) & 3) * 128 + (lane & 3) * 8;
#pragma unroll
    for (int b = 0; b < 4; ++b) {
      const u32x2 lo = __builtin_bit_cast(u32x2, __builtin_amdgcn_ds_read_tr16_b64_v4i16((LAS s16x4*)(src + b * 32)));
      const u32x2 hi = __builtin_bit_cast(u32x2, __builtin_amdgcn_ds_read_tr16_b64_v4i16((LAS s16x4*)(src + b * 32 + 512)));
      wf[b] = u32x4{lo[0], lo[1], hi[0], hi[1]};
    }
    unsigned k0 = 0x07060501u, k1 = 0x07060502u, k2 = 0x07060503u;
    asm volatile("" : "+s"(k0), "+s"(k1), "+s"(k2) : "s"(wsr));
    asm volatile("" : "+v"(wf[0]), "+v"(wf[1]), "+v"(wf[2]), "+v"(wf[3]), "+v"(yoff), "+v"(oaddr));
    ST(9);
    asm volatile("s_barrier" ::: "memory");
    ST(11);
    const u32x4 ov = *(const u32x4*)((const char*)L.o + oaddr);
    const f32x4 z = {0, 0, 0, 0};
    f32x4 y3 = mfma(ov, wf[3], z), y0 = mfma(ov, wf[0], z), y1 = mfma(ov, wf[1], z), y2 = mfma(ov, wf[2], z);
    SB();
    const unsigned p0 = __builtin_amdgcn_perm(fu(y0[0]), fu(y3[0]), k0), p1 = __builtin_amdgcn_perm(fu(y1[0]), fu(y3[0]), k1);
    const unsigned c01 = xor3(p0, p1, tag), p2 = __builtin_amdgcn_perm(fu(y2[0]), fu(y3[0]), k2);
    stx(wsr, yoff, 0, u32x4{p0, p1, p2, c01 ^ p2});
    asm volatile("" :: "v"(y0), "v"(y1), "v"(y2), "v"(y3));
  } else if (wave == gw) {
    const unsigned oa = (unsigned)(size_t)(LAS void*)&L.o[4 * (lane & 7) + (lane >> 4)];
    unsigned left = SPIN_CAP;
    __builtin_amdgcn_s_sleep(FIRST1);
    asm volatile(POLL2("")
        "v_max_f32 v112, v102, v106\n\tv_lshlrev_b32 v108, 16, v100\n\tv_and_b32 v109, 0xffff0000, v100\n\t"
        "v_max_f32_dpp v112, v112, v112 row_ror:8 row_mask:0xf bank_mask:0xf\n\tv_lshlrev_b32 v110, 16, v101\n\t"
        "v_mov_b32 v113, v112\n\tv_and_b32 v111, 0xffff0000, v101\n\tv_lshlrev_b32 v114, 16, v104\n\t"
        "v_permlane16_swap_b32 v112, v113\n\tv_max_f32 v112, v112, v113\n\t"
        "v_mov_b32 v113, v112\n\tv_and_b32 v115, 0xffff0000, v104\n\tv_lshlrev_b32 v116, 16, v105\n\t"
        "v_permlane32_swap_b32 v112, v113\n\tv_max_f32 v112, v112, v113\n\t"
        "v_sub_f32 v102, v102, v112\n\tv_sub_f32 v106, v106, v112\n\tv_exp_f32 v102, v102\n\tv_exp_f32 v106, v106\n\t"
        "v_and_b32 v117, 0xffff0000, v105\n\tv_add_f32 v112, v102, v106\n\t"
        "v_pk_mul_f32 v[108:109], v[108:109], v[102:103] op_sel_hi:[1,0]\n\tv_pk_mul_f32 v[110:111], v[110:111], v[102:103] op_sel_hi:[1,0]\n\t"
        "v_add_f32_dpp v112, v112, v112 row_ror:8 row_mask:0xf bank_mask:0xf\n\t"
        "v_pk_fma_f32 v[108:109], v[114:115], v[106:107], v[108:109] op_sel_hi:[1,0,1]\n\tv_mov_b32 v113, v112\n\t"
        "v_pk_fma_f32 v[110:111], v[116:117], v[106:107], v[110:111] op_sel_hi:[1,0,1]\n\ts_nop 0\n\t"
        "v_permlane16_swap_b32 v112, v113\n\tv_permlane32_swap_b32 v108, v110\n\tv_permlane32_swap_b32 v109, v111\n\t"
        "v_add_f32 v112, v112, v113\n\tv_add_f32 v108, v108, v110\n\tv_add_f32 v109, v109, v111\n\tv_mov_b32 v113, v112\n\ts_nop 0\n\t"
        "v_permlane16_swap_b32 v108, v109\n\tv_permlane32_swap_b32 v112, v113\n\t"
        "v_add_f32 v108, v108, v109\n\tv_add_f32 v112, v112, v113\n\tv_rcp_f32 v112, v112\n\t"
        "v_add_f32_dpp v108, v108, v108 row_ror:8 row_mask:0xf bank_mask:0xf\n\tv_mul_f32 v108, v108, v112\n\t"
        "v_cvt_pk_bf16_f32 v108, v108, v108\n\tds_write_b16 %[la], v108"
        : [n] "+s"(left) : [vo] "v"(lane * 16), [rs] "s"(wsr), [so] "s"(WS_O + (hp * 16 + s) * 2048), [tag] "s"(tag), [la] "v"(oa) : POLLCLOB);
    ST(5);
    asm volatile("s_waitcnt lgkmcnt(0)\n\ts_barrier" ::: "memory");
  } else {
    asm volatile("s_barrier" ::: "memory");
  }
  ST(6);
  const unsigned rd = w - ROFF;
  if (rd >= NRED) { STDUMP(); return; }
  if (wave < 8) {
    const unsigned wa = (unsigned)(size_t)(LAS void*)&L.fin[0][0] + ((lane >> 4) * 4 + ((lane & 7) >> 1)) * 64 + wave * 8 + (lane & 1) * 4;
    unsigned left = SPIN_CAP;
    __builtin_amdgcn_s_sleep(FIRST2);
    asm volatile(POLL2("v_perm_b32 v110, v101, v100, %[k0]\n\tv_perm_b32 v110, v102, v110, %[k1]\n\t")
        "v_perm_b32 v111, v105, v104, %[k0]\n\tv_pk_add_f32 v[100:101], v[100:101], v[104:105]\n\tv_add_f32 v102, v102, v106\n\t"
        "v_perm_b32 v111, v106, v111, %[k1]\n\tv_add_f32 v110, v110, v111\n\t"
        "v_permlane32_swap_b32 v100, v102\n\tv_add_f32 v100, v100, v102\n\t"
        "v_permlane32_swap_b32 v101, v110\n\tv_add_f32 v101, v101, v110\n\ts_nop 1\n\t"
        "v_permlane16_swap_b32 v100, v101\n\tv_add_f32 v100, v100, v101\n\ts_nop 1\n\t"
        "v_add_f32_dpp v100, v100, v100 row_ror:8 row_mask:0xf bank_mask:0xf\n\tds_write_b32 %[la], v100"
        : [n] "+s"(left) : [vo] "v"(lane * 16), [rs] "s"(wsr), [so] "s"(WS_Y + rd * 16384 + wave * 2048), [tag] "s"(tag), [la] "v"(wa),
          [k0] "s"(0x0c04000cu), [k1] "s"(0x04020100u) : POLLCLOB);
    asm volatile("s_waitcnt lgkmcnt(0)\n\ts_barrier" ::: "memory");
  } else if (wave == 8) {
    unsigned fa = (unsigned)(size_t)(LAS void*)&L.fin[0][0] + (lane & 15) * 64, ooff = lane < 16 ? ((rd >> 1) * 32 + (rd & 1) * 4 + (lane >> 2) * 8 + (lane & 3)) * 4 : OOR;
    const rsrc_t outr = mkrs((const void*)a4[2], 2 * DM * 2);
    asm volatile("" : "+v"(fa), "+v"(ooff) : "s"(outr));
    asm volatile("s_barrier" ::: "memory");
    const LAS f32x4* f = (const LAS f32x4*)(size_t)fa;
    const f32x4 t = (f[0] + f[1]) + (f[2] + f[3]);
    __builtin_amdgcn_raw_buffer_store_b32(cvtpk(t[0] + t[2], t[1] + t[3]), outr, ooff, 0, OUTSC);
    ST(7);
  } else {
    asm volatile("s_barrier" ::: "memory");
    if (rd == BUMP && tid == 576) __hip_atomic_store((GAS unsigned*)(ws + WS_EPOCH), tag, RLX_AGENT);
  }
  STDUMP();
}

extern "C" void kernel_launch(void* const* d_in, const int* in_sizes, int n_in, void* d_out, int out_size, void* d_ws, size_t ws_size, hipStream_t stream) {
  fused_decode<<<256, NTHR, 0, stream>>>(d_in[1], d_in[2], d_in[3], d_in[0], d_in[6], d_in[7], d_in[8], d_in[5], d_in[4], d_in[9], d_out, d_ws);
}
```

```cpp
#include <hip/hip_runtime.h>
#include <hip/hip_bf16.h>
#include <stdint.h>
#include <stddef.h>

#define GAS __attribute__((address_space(1)))
#define LAS __attribute__((address_space(3)))
#define SB() __builtin_amdgcn_sched_barrier(0)
typedef unsigned u32x4 __attribute__((ext_vector_type(4)));
typedef unsigned u32x2 __attribute__((ext_vector_type(2)));
typedef float f32x4 __attribute__((ext_vector_type(4)));
typedef short s16x4 __attribute__((ext_vector_type(4)));
typedef __bf16 bf16x2 __attribute__((ext_vector_type(2)));
typedef __bf16 bf16x8 __attribute__((ext_vector_type(8)));
typedef unsigned long long u64;
typedef u64 u64x2 __attribute__((ext_vector_type(2)));
typedef u64 u64x4 __attribute__((ext_vector_type(4)));
typedef __amdgpu_buffer_rsrc_t rsrc_t;

constexpr int DM = 576, NQ = 4096, NTHR = 11 * 64, NRED = 36;
constexpr unsigned WS_Q = 4096, WS_O = WS_Q + 16 * 4096, WS_Y = WS_O + 256 * 2048, WS_END = WS_Y + NRED * 16384;
constexpr unsigned SPIN_CAP = 1u << 17;
constexpr unsigned OOR = 0x40000000u;
constexpr int NT = 2, SC1 = 16;
#define VGATE 675
#define LANDS 6

struct Lds {
  unsigned char wo[9][4096];
  float part[11][2][132];
  unsigned q[2][128];
  unsigned x[144];
  float fin[16][8][2];
  unsigned short o[32];
  unsigned tag, kissued, issued, landed, done[2], pad[2];
};

__device__ __forceinline__ float bflo(unsigned u) { return __builtin_bit_cast(float, u << 16); }
__device__ __forceinline__ float bfhi(unsigned u) { return __builtin_bit_cast(float, u & 0xffff0000u); }
__device__ __forceinline__ unsigned fu(float f) { return __float_as_uint(f); }
__device__ __forceinline__ float uf(unsigned u) { return __uint_as_float(u); }
__device__ __forceinline__ unsigned cvtpk(float lo, float hi) { bf16x2 v = {(__bf16)lo, (__bf16)hi}; return __builtin_bit_cast(unsigned, v); }
__device__ __forceinline__ float dot2(unsigned a, unsigned b, float acc) {
  return __builtin_amdgcn_fdot2_f32_bf16(__builtin_bit_cast(bf16x2, a), __builtin_bit_cast(bf16x2, b), acc, false);
}
template <int CTRL> __device__ __forceinline__ float dpp(float x) {
  return __builtin_bit_cast(float, __builtin_amdgcn_mov_dpp(__builtin_bit_cast(int, x), CTRL, 0xf, 0xf, true));
}
constexpr int XOR1 = 0xB1, XOR2 = 0x4E, XOR7 = 0x141, XOR8 = 0x128;
__device__ __forceinline__ f32x4 mfma(u32x4 a, u32x4 b, f32x4 c) {
  return __builtin_amdgcn_mfma_f32_16x16x32_bf16(__builtin_bit_cast(bf16x8, a), __builtin_bit_cast(bf16x8, b), c, 0, 0, 0);
}
__device__ __forceinline__ float fold32(float a, float b) {
  auto r = __builtin_amdgcn_permlane32_swap(fu(a), fu(b), false, false);
  return uf(r[0]) + uf(r[1]);
}
__device__ __forceinline__ float fold16(float a, float b) {
  auto r = __builtin_amdgcn_permlane16_swap(fu(a), fu(b), false, false);
  return uf(r[0]) + uf(r[1]);
}
__device__ __forceinline__ float max16_32(float x) {
  auto s = __builtin_amdgcn_permlane16_swap(fu(x), fu(x), false, false);
  x = fmaxf(uf(s[0]), uf(s[1]));
  auto t = __builtin_amdgcn_permlane32_swap(fu(x), fu(x), false, false);
  return fmaxf(uf(t[0]), uf(t[1]));
}
__device__ __forceinline__ rsrc_t mkrs(u64 p, unsigned bytes) { return __builtin_amdgcn_make_buffer_rsrc((void*)p, 0, bytes, 0x00020000); }
template <int AUX> __device__ __forceinline__ u32x4 ldb(rsrc_t r, unsigned voff, unsigned soff) { return __builtin_amdgcn_raw_buffer_load_b128(r, voff, soff, AUX); }
__device__ __forceinline__ void publish(rsrc_t r, unsigned voff, unsigned soff, u32x4 v) { __builtin_amdgcn_raw_buffer_store_b128(v, r, voff, soff, SC1); }
__device__ __forceinline__ unsigned xor3(unsigned a, unsigned b, unsigned c) { return __builtin_amdgcn_bitop3_b32(a, b, c, 0x96); }
__device__ __forceinline__ u32x4 seal(unsigned a, unsigned b, unsigned c, unsigned tag) { return u32x4{a, b, c, xor3(a, b, c ^ tag)}; }
__device__ __forceinline__ unsigned bad(u32x4 g, unsigned tag) { return xor3(xor3(g[0], g[1], g[2]), g[3], tag); }
constexpr unsigned PK0 = 0x07060501u, PK1 = 0x07060502u, PK2 = 0x07060503u, UP0 = 0x0c04000cu, UP1 = 0x04020100u;
__device__ __forceinline__ float fourth(u32x4 g) { return uf(__builtin_amdgcn_perm(g[2], __builtin_amdgcn_perm(g[1], g[0], UP0), UP1)); }

template <int OFF> __device__ __forceinline__ void lds_inc(unsigned base) {
  asm volatile("s_mov_b64 exec, 1\n\tds_add_u32 %0, %1 offset:%2\n\ts_mov_b64 exec, -1" :: "v"(base), "v"(1u), "i"(OFF) : "memory");
}
template <int OFF> __device__ __forceinline__ unsigned lds_wait(unsigned base, unsigned n) {
  unsigned v, left = SPIN_CAP << 3;
  asm volatile("1:\n\tds_read_b32 %0, %2 offset:%4\n\ts_sub_u32 %1, %1, 1\n\ts_waitcnt lgkmcnt(0)\n\tv_cmp_gt_u32 vcc, %3, %0\n\t"
               "s_cbranch_vccz 2f\n\ts_sleep 1\n\ts_cbranch_scc0 1b\n2:" : "=&v"(v), "+s"(left) : "v"(base), "s"(n), "i"(OFF) : "memory", "vcc", "scc");
  return v;
}
#define POLL2(EARLY) \
    "1:\n\tbuffer_load_dwordx4 v[100:103], %[vo], %[rs], %[so] offen sc1\n\tbuffer_load_dwordx4 v[104:107], %[vo], %[rs], %[so] offen offset:1024 sc1\n\t" \
    "s_sub_u32 %[n], %[n], 1\n\ts_waitcnt vmcnt(1)\n\t" \
    "v_bitop3_b32 v108, v100, v101, v102 bitop3:0x96\n\tv_bitop3_b32 v108, v108, v103, %[tag] bitop3:0x96\n\t" EARLY "s_waitcnt vmcnt(0)\n\t" \
    "v_bitop3_b32 v109, v104, v105, v106 bitop3:0x96\n\tv_bitop3_b32 v109, v109, v107, %[tag] bitop3:0x96\n\t" \
    "v_or_b32 v108, v108, v109\n\tv_cmp_ne_u32 vcc, 0, v108\n\ts_cbranch_vccz 2f\n\ts_cbranch_scc0 1b\n2:\n\t"
#define POLLCLOB "memory", "vcc", "scc", "v100", "v101", "v102", "v103", "v104", "v105", "v106", "v107", "v108", "v109", "v110", "v111", "v112", "v113", "v114", "v115", "v116", "v117"

#define GETARGS(OFF) asm volatile("s_load_dwordx4 %0, %2, %3\n\ts_load_dwordx8 %1, %2, 0x40\n\ts_waitcnt lgkmcnt(0)" : "=&s"(a2), "=&s"(a4) : "s"(karg), "s"(OFF) : "memory")
__global__ void __launch_bounds__(NTHR, 3) fused_decode(const void* kA, const void* vA, const void* wqA, const void* xA, const void* kB, const void* vB, const void* wqB,
                                                         const void* xB, const void* woA, const void* woB, void* out, void* ws) {
  __shared__ __attribute__((aligned(16))) Lds L;
  const auto karg = __builtin_amdgcn_kernarg_segment_ptr();
  asm volatile("s_load_dword s94, %0, 0x0\n\ts_load_dword s95, %0, 0x40" :: "s"(karg) : "s94", "s95");
  SB();
  const int tid = threadIdx.x, lane = tid & 63, wave = __builtin_amdgcn_readfirstlane(tid >> 6);
  const int w = blockIdx.x, h = (w & 7) * 2 + (w >> 7), s = (w >> 3) & 15;
  const unsigned lb = (unsigned)(size_t)(LAS void*)&L;
  u64x2 a2; u64x4 a4;
  unsigned tag;

  if (__builtin_expect(wave == 0, 1)) {
    const int rs = lane >> 3, c = lane & 7;
    const int qbr = (w >> 1) & 1, half = (w >> 2) & 1, j = w >> 3, blk = (j >> 1) * 4 + (j & 1) * 2 + ((j ^ w) & 1);
    unsigned woff = (unsigned)((half * 288 + rs * 36) * NQ + blk * 64 + c * 8) * 2;
    asm volatile("" : "+v"(woff));
    GETARGS(qbr * 32 + 16);
    const rsrc_t wr = mkrs(a2[0], DM * NQ * 2), wsr = mkrs(a4[3], WS_END);
    u32x4 wv[36], xr;
    unsigned epoch, so = 0;
#pragma unroll
    for (int t = 0; t < 36; ++t) {
      wv[t] = ldb<NT>(wr, woff, so); SB();
      so += NQ * 2; asm volatile("" : "+s"(so)); SB();
      if (t == 0) asm volatile("s_load_dword s94, %0, 0x0" :: "s"(a2[1]) : "s94");
      if (t == 20) {
        xr = ldb<0>(mkrs(a2[1] + half * 576, 576), lane * 16, 0);
        epoch = __hip_atomic_load((GAS unsigned*)a4[3], __ATOMIC_RELAXED, __HIP_MEMORY_SCOPE_AGENT); SB();
      }
    }
    __builtin_amdgcn_s_barrier();
    SB();
    if (lane < 36) *(u32x4*)&L.x[lane * 4] = xr;
    float acc[8] = {0, 0, 0, 0, 0, 0, 0, 0};
#pragma unroll
    for (int t4 = 0; t4 < 9; ++t4) {
      const u32x2 xw = *(const u32x2*)&L.x[rs * 18 + t4 * 2];
#pragma unroll
      for (int u = 0; u < 4; ++u) {
        const unsigned xl = (u & 1) ? xw[u >> 1] >> 16 : xw[u >> 1] & 0xffffu, xh = (u & 1) ? xw[u >> 1] & 0xffff0000u : xw[u >> 1] << 16;
#pragma unroll
        for (int k = 0; k < 4; ++k) { acc[2 * k] = dot2(wv[t4 * 4 + u][k], xl, acc[2 * k]); acc[2 * k + 1] = dot2(wv[t4 * 4 + u][k], xh, acc[2 * k + 1]); }
      }
      SB();
    }
    float b[4];
#pragma unroll
    for (int k = 0; k < 4; ++k) { b[k] = fold32(acc[k], acc[k + 4]); b[k] = fold16(b[k], b[k]); b[k] += dpp<XOR8>(b[k]); }
    asm volatile("" : "+v"(epoch));
    tag = __builtin_amdgcn_readfirstlane(epoch) + 1u;
    tag += tag == 0u;
    publish(wsr, (lane & 24) ? OOR : (c * 2 + (lane >> 5)) * 16, WS_Q + (blk >> 2) * 4096 + (qbr * 2 + half) * 1024 + (blk & 3) * 256,
            seal(__builtin_amdgcn_perm(fu(b[0]), fu(b[3]), PK0), __builtin_amdgcn_perm(fu(b[1]), fu(b[3]), PK1), __builtin_amdgcn_perm(fu(b[2]), fu(b[3]), PK2), tag));
    __builtin_amdgcn_s_sleep(64);
    u32x4 g[4];
    for (unsigned spins = 0; spins < SPIN_CAP; ++spins) {
      asm volatile("" ::: "memory");
#pragma unroll
      for (int i = 0; i < 4; ++i) g[i] = ldb<SC1>(wsr, lane * 16 + i * 1024, WS_Q + h * 4096);
      if (!__any((bad(g[0], tag) | bad(g[1], tag) | bad(g[2], tag) | bad(g[3], tag)) != 0u)) break;
    }
#pragma unroll
    for (int br = 0; br < 2; ++br) {
      const u32x4 p0 = g[2 * br], p1 = g[2 * br + 1];
      *(u32x2*)&L.q[br][2 * lane] = u32x2{cvtpk(uf(p0[0]) + uf(p1[0]), uf(p0[1]) + uf(p1[1])), cvtpk(uf(p0[2]) + uf(p1[2]), fourth(p0) + fourth(p1))};
    }
    asm volatile("ds_write_b32 %0, %1 offset:%2" :: "v"(lb), "v"(tag), "i"(offsetof(Lds, tag)) : "memory");
    asm volatile("s_branch 1f\n\t.p2align 12\n1:");
  } else {
    const unsigned tstart = (unsigned)__builtin_amdgcn_s_memrealtime();
    const int br = wave == 1 || wave == 3 ? 0 : 1;
    const int quarter = wave < 5 ? 3 : (wave - 5) >> 1, odd = wave < 5 ? (wave - 1) >> 1 : (wave - 5) & 1;
    const int g = lane >> 4, i = lane & 15;
    const unsigned sbase = (h * 2048 + quarter * 512 + s * 32 + odd * 2) * 1024;
    unsigned loff = sbase + lane * 16, so = ((s + 3 * h) & 7) * 4096, kneed = 2 * br;
    const bool late = wave < 4, closer = wave < 3;
    if (tid == 64) { *(u32x4*)&L.tag = u32x4{0u, 0u, 0u, 0u}; *(u32x2*)&L.done[0] = u32x2{0u, 0u}; }
    asm volatile("" : "+v"(loff));
    asm volatile("" : "+s"(so), "+s"(kneed));
    GETARGS(br * 32);
    if ((unsigned)(wave - 3) < 2u) asm volatile("s_load_dword s94, %0, %2\n\ts_load_dword s95, %1, %2" :: "s"(a2[0]), "s"(a2[1]), "s"(sbase) : "s94", "s95");
    const rsrc_t kr = mkrs(a2[0], 32 << 20), vr = mkrs(a2[1], 32 << 20), wsr = mkrs(a4[3], WS_END);
    asm volatile("" :: "s"(kr), "s"(vr));
    __builtin_amdgcn_s_barrier();
    SB();
    if (kneed) lds_wait<offsetof(Lds, kissued)>(lb, kneed);
    u32x4 K[16], V[16];
#define KL2(A, B) "buffer_load_dwordx4 " A ", %[vo], %[rs], %[so] offen nt\n\ts_add_u32 %[nx], %[so], %[st]\n\t" \
                  "buffer_load_dwordx4 " B ", %[vo], %[rs], %[so] offen offset:1024 nt\n\ts_and_b32 %[so], %[nx], %[mk]\n\t"
    unsigned nx;
    asm volatile(KL2("%0", "%1") KL2("%2", "%3") KL2("%4", "%5") KL2("%6", "%7") KL2("%8", "%9") KL2("%10", "%11") KL2("%12", "%13") KL2("%14", "%15")
                 "s_mov_b64 exec, 1\n\tds_add_u32 %[lb], %[one] offset:%[ko]\n\ts_mov_b64 exec, -1\n\ts_waitcnt vmcnt(0)"
                 : "=&v"(K[0]), "=&v"(K[1]), "=&v"(K[2]), "=&v"(K[3]), "=&v"(K[4]), "=&v"(K[5]), "=&v"(K[6]), "=&v"(K[7]), "=&v"(K[8]), "=&v"(K[9]), "=&v"(K[10]),
                   "=&v"(K[11]), "=&v"(K[12]), "=&v"(K[13]), "=&v"(K[14]), "=&v"(K[15]), [so] "+s"(so), [nx] "=&s"(nx)
                 : [vo] "v"(loff), [rs] "s"(kr), [st] "s"(0x1000), [mk] "s"(0x7000), [lb] "v"(lb), [one] "v"(1u), [ko] "i"(offsetof(Lds, kissued)) : "memory", "scc");
    if (wave > 7) __builtin_amdgcn_s_sleep(LANDS);
    lds_inc<offsetof(Lds, landed)>(lb);
#define LOADV() _Pragma("unroll") for (int t = 0; t < 16; t += 2) { \
      V[t] = ldb<NT>(vr, loff, so); SB(); unsigned n2 = so + 4096u; asm volatile("" : "+s"(n2)); SB(); \
      V[t + 1] = ldb<NT>(vr, loff + 1024, so); SB(); so = n2 & 0x7000u; asm volatile("" : "+s"(so)); SB(); } \
      lds_inc<offsetof(Lds, issued)>(lb);
    if (!late) {
      lds_wait<offsetof(Lds, kissued)>(lb, 10u);
      for (unsigned spins = 0; (unsigned)__builtin_amdgcn_s_memrealtime() - tstart < VGATE && spins < SPIN_CAP; ++spins)
        if (__builtin_amdgcn_readfirstlane(lds_wait<offsetof(Lds, landed)>(lb, 0u)) >= 10u) break;
      SB();
      LOADV()
    }
    tag = __builtin_amdgcn_readfirstlane(lds_wait<offsetof(Lds, tag)>(lb, 1u));
    const u32x4 q0 = *(const u32x4*)&L.q[br][4 * i], q1 = *(const u32x4*)&L.q[br][64 + 4 * i];
    float sc[16], m = -INFINITY;
#pragma unroll
    for (int t = 0; t < 16; ++t) {
      float d0 = 0, d1 = 0;
#pragma unroll
      for (int k = 0; k < 4; ++k) { d0 = dot2(K[t][k], q0[k], d0); d1 = dot2(K[t][k], q1[k], d1); }
      d0 += dpp<XOR8>(d0); d1 += dpp<XOR8>(d1);
      float d = (i & 8) ? d1 : d0;
      d += dpp<XOR7>(d); d += dpp<XOR2>(d); d += dpp<XOR1>(d);
      sc[t] = d;
    }
    constexpr float SCALE = 0.088388347648318440f, LOG2E = 1.4426950408889634f;
#pragma unroll
    for (int t = 0; t < 16; t += 2) {
      const unsigned r1 = cvtpk(sc[t], sc[t + 1]), r2 = cvtpk(bflo(r1) * SCALE, bfhi(r1) * SCALE);
      sc[t] = bflo(r2); sc[t + 1] = bfhi(r2);
      m = fmaxf(m, fmaxf(sc[t], sc[t + 1]));
    }
    m = max16_32(m) * LOG2E;
    unsigned msk[4];
#pragma unroll
    for (int k = 0; k < 4; ++k) msk[k] = (((i & 7) >> 1) == k) ? ((i & 1) ? 0xffff0000u : 0x0000ffffu) : 0u;
    float l = 0;
    u32x4 a[16];
#pragma unroll
    for (int t = 0; t < 16; ++t) {
      const float p = __builtin_amdgcn_exp2f(fmaf(sc[t], LOG2E, -m));
      const unsigned pp = cvtpk(p, p);
      l += p;
      a[t] = u32x4{pp & msk[0], pp & msk[1], pp & msk[2], pp & msk[3]};
    }
    l = fold16(l, l); l = fold32(l, l);
    if ((lane & 55) == 0) *(float2*)&L.part[wave][lane >> 3][0] = make_float2(m, l);
#pragma unroll
    for (int t = 0; t < 16; ++t) asm volatile("" : "+v"(a[t]));
    SB();
    if (late) {
      lds_wait<offsetof(Lds, issued)>(lb, wave == 1 ? 9u : wave + 5u);
      SB();
      LOADV()
    }
    const int hd = g >> 1, oi = 4 + 8 * i + 4 * (g & 1);
    float f = 0, eo = 0;
    unsigned lse = 0, poff = WS_O + h * 32768 + br * 16384 + s * 128 + (lane >> 3) * 2048 + (lane & 7) * 16;
    if (wave == 1) {
      const float2 me = *(const float2*)&L.part[1][hd][0], r = *(const float2*)&L.part[3][hd][0];
      const float M = fmaxf(me.x, r.x);
      f = __builtin_amdgcn_exp2f(me.x - M); eo = __builtin_amdgcn_exp2f(r.x - M);
      const float Ls = fmaf(f, me.y, eo * r.y), inv = __builtin_amdgcn_rcpf(Ls);
      f *= inv; eo *= inv;
      lse = fu(M + __builtin_amdgcn_logf(Ls));
    }
    asm volatile("" : "+v"(f), "+v"(eo), "+v"(lse), "+v"(poff));
    SB();
    f32x4 acc = {0, 0, 0, 0}, base = {0, 0, 0, 0};
#pragma unroll
    for (int t = 0; t < 10; ++t) acc = mfma(a[t], V[t], acc);
    asm volatile("" : "+v"(acc));
    SB();
    if (wave == 1) {
      lds_wait<offsetof(Lds, done)>(lb, 1u);
      base = *(const f32x4*)&L.part[3][hd][oi];
    } else if (wave == 2) {
      lds_wait<offsetof(Lds, done) + 4>(lb, 7u);
      const float2 me = *(const float2*)&L.part[2][hd][0];
      float2 r[7]; f32x4 o[7];
#pragma unroll
      for (int u = 0; u < 7; ++u) { r[u] = *(const float2*)&L.part[4 + u][hd][0]; o[u] = *(const f32x4*)&L.part[4 + u][hd][oi]; }
      float M = me.x;
#pragma unroll
      for (int u = 0; u < 7; ++u) M = fmaxf(M, r[u].x);
      f = __builtin_amdgcn_exp2f(me.x - M);
      float Ls = f * me.y;
#pragma unroll
      for (int u = 0; u < 7; ++u) { const float e = __builtin_amdgcn_exp2f(r[u].x - M); Ls = fmaf(e, r[u].y, Ls); base += e * o[u]; }
      eo = __builtin_amdgcn_rcpf(Ls);
      f *= eo;
      lse = fu(M + __builtin_amdgcn_logf(Ls));
    }
    SB();
#pragma unroll
    for (int t = 10; t < 15; ++t) acc = mfma(a[t], V[t], acc);
    base *= eo;
    asm volatile("" : "+v"(base) : "s"(wsr));
    acc = mfma(a[15], V[15], acc);
    if (closer) {
      acc = acc * f + base;
      publish(wsr, poff, 0, seal(cvtpk(acc[0], acc[1]), cvtpk(acc[2], acc[3]), lse, tag));
    } else {
      *(f32x4*)&L.part[wave][hd][oi] = acc;
      lds_inc<offsetof(Lds, done)>(lb + 4 * br);
    }
  }

  const rsrc_t wsr = mkrs(a4[3], WS_END);
  const int hp = (h + 4) & 15, pbr = s >> 3;
  if (wave == 3) {
    unsigned left = SPIN_CAP;
    __builtin_amdgcn_s_sleep(4);
    asm volatile(POLL2("")
        "v_max_f32 v112, v102, v106\n\tv_lshlrev_b32 v108, 16, v100\n\tv_and_b32 v109, 0xffff0000, v100\n\t"
        "v_max_f32_dpp v112, v112, v112 row_ror:8 row_mask:0xf bank_mask:0xf\n\tv_lshlrev_b32 v110, 16, v101\n\t"
        "v_mov_b32 v113, v112\n\tv_and_b32 v111, 0xffff0000, v101\n\tv_lshlrev_b32 v114, 16, v104\n\t"
        "v_permlane16_swap_b32 v112, v113\n\tv_max_f32 v112, v112, v113\n\t"
        "v_mov_b32 v113, v112\n\tv_and_b32 v115, 0xffff0000, v104\n\tv_lshlrev_b32 v116, 16, v105\n\t"
        "v_permlane32_swap_b32 v112, v113\n\tv_max_f32 v112, v112, v113\n\t"
        "v_sub_f32 v102, v102, v112\n\tv_sub_f32 v106, v106, v112\n\tv_exp_f32 v102, v102\n\tv_exp_f32 v106, v106\n\t"
        "v_and_b32 v117, 0xffff0000, v105\n\tv_add_f32 v112, v102, v106\n\t"
        "v_pk_mul_f32 v[108:109], v[108:109], v[102:103] op_sel_hi:[1,0]\n\tv_pk_mul_f32 v[110:111], v[110:111], v[102:103] op_sel_hi:[1,0]\n\t"
        "v_add_f32_dpp v112, v112, v112 row_ror:8 row_mask:0xf bank_mask:0xf\n\t"
        "v_pk_fma_f32 v[108:109], v[114:115], v[106:107], v[108:109] op_sel_hi:[1,0,1]\n\tv_mov_b32 v113, v112\n\t"
        "v_pk_fma_f32 v[110:111], v[116:117], v[106:107], v[110:111] op_sel_hi:[1,0,1]\n\ts_nop 0\n\t"
        "v_permlane16_swap_b32 v112, v113\n\tv_permlane32_swap_b32 v108, v110\n\tv_permlane32_swap_b32 v109, v111\n\t"
        "v_add_f32 v112, v112, v113\n\tv_add_f32 v108, v108, v110\n\tv_add_f32 v109, v109, v111\n\tv_mov_b32 v113, v112\n\ts_nop 0\n\t"
        "v_permlane16_swap_b32 v108, v109\n\tv_permlane32_swap_b32 v112, v113\n\t"
        "v_add_f32 v108, v108, v109\n\tv_add_f32 v112, v112, v113\n\tv_rcp_f32 v112, v112\n\t"
        "v_add_f32_dpp v108, v108, v108 row_ror:8 row_mask:0xf bank_mask:0xf\n\tv_mul_f32 v108, v108, v112\n\t"
        "v_cvt_pk_bf16_f32 v108, v108, v108\n\tds_write_b16 %[la], v108\n\ts_waitcnt lgkmcnt(0)\n\ts_barrier"
        : [n] "+s"(left) : [vo] "v"(lane * 16), [rs] "s"(wsr), [so] "s"(WS_O + (hp * 16 + s) * 2048), [tag] "s"(tag),
          [la] "v"(lb + (unsigned)offsetof(Lds, o) + 8 * (lane & 7) + 2 * (lane >> 4)) : POLLCLOB);
  } else if (wave == 2) {
    asm volatile("s_barrier" ::: "memory");
  } else {
    const int pw = wave < 2 ? wave : wave - 2;
    const int row0 = hp * 256 + ((s >> 2) & 1) * 128 + (s & 1) * 64 + (s & 2) * 2;
    const rsrc_t wr = mkrs((pbr ? a4[1] : a4[0]) + (row0 * DM + pw * 64) * 2, 60 * DM * 2);
    const unsigned woff = ((8 * (lane >> 5) + ((lane >> 3) & 3)) * DM + (lane & 7) * 8) * 2;
    LAS unsigned char* dst = (LAS unsigned char*)&L.wo[pw][0];
    lds_wait<offsetof(Lds, issued)>(lb, 10u);
#pragma unroll
    for (int t = 0; t < 4; ++t) __builtin_amdgcn_raw_ptr_buffer_load_lds(wr, dst + t * 1024, 16, woff, t * 16 * DM * 2, 0, NT);
    unsigned yoff = lane < 16 ? WS_Y + (pbr * 18 + pw * 2 + (lane >> 3)) * 16384 + (hp * 8 + (s & 7)) * 128 + (lane & 7) * 16 : OOR;
    unsigned oa = (lane >> 4) * 16, k0 = PK0, k1 = PK1, k2 = PK2;
    asm volatile("s_waitcnt vmcnt(0)" ::: "memory");
    LAS unsigned char* src = dst + (lane >> 4) * 1024 + ((lane >> 2) & 3) * 128 + (lane & 3) * 8;
    u32x4 wf[4];
#pragma unroll
    for (int b = 0; b < 4; ++b) {
      const u32x2 lo = __builtin_bit_cast(u32x2, __builtin_amdgcn_ds_read_tr16_b64_v4i16((LAS s16x4*)(src + b * 32)));
      const u32x2 hi = __builtin_bit_cast(u32x2, __builtin_amdgcn_ds_read_tr16_b64_v4i16((LAS s16x4*)(src + b * 32 + 512)));
      wf[b] = u32x4{lo[0], lo[1], hi[0], hi[1]};
    }
    asm volatile("" : "+s"(k0), "+s"(k1), "+s"(k2) : "s"(wsr));
    asm volatile("" : "+v"(wf[0]), "+v"(wf[1]), "+v"(wf[2]), "+v"(wf[3]), "+v"(yoff), "+v"(oa));
    asm volatile("s_barrier" ::: "memory");
    const u32x4 ov = *(const u32x4*)((const char*)L.o + oa);
    const f32x4 z = {0, 0, 0, 0};
    const f32x4 y3 = mfma(ov, wf[3], z), y0 = mfma(ov, wf[0], z), y1 = mfma(ov, wf[1], z), y2 = mfma(ov, wf[2], z);
    SB();
    const unsigned p0 = __builtin_amdgcn_perm(fu(y0[0]), fu(y3[0]), k0), p1 = __builtin_amdgcn_perm(fu(y1[0]), fu(y3[0]), k1);
    const unsigned c01 = xor3(p0, p1, tag), p2 = __builtin_amdgcn_perm(fu(y2[0]), fu(y3[0]), k2);
    publish(wsr, yoff, 0, u32x4{p0, p1, p2, c01 ^ p2});
    asm volatile("" :: "v"(y0), "v"(y1), "v"(y2), "v"(y3));
  }
  if (w >= NRED) return;

  if (wave < 8) {
    unsigned left = SPIN_CAP;
    __builtin_amdgcn_s_sleep(16);
    asm volatile(POLL2("v_perm_b32 v110, v101, v100, %[k0]\n\tv_perm_b32 v110, v102, v110, %[k1]\n\t")
        "v_perm_b32 v111, v105, v104, %[k0]\n\tv_pk_add_f32 v[100:101], v[100:101], v[104:105]\n\tv_add_f32 v102, v102, v106\n\t"
        "v_perm_b32 v111, v106, v111, %[k1]\n\tv_add_f32 v110, v110, v111\n\t"
        "v_permlane32_swap_b32 v100, v102\n\tv_add_f32 v100, v100, v102\n\t"
        "v_permlane32_swap_b32 v101, v110\n\tv_add_f32 v101, v101, v110\n\ts_nop 1\n\t"
        "v_permlane16_swap_b32 v100, v101\n\tv_add_f32 v100, v100, v101\n\ts_nop 1\n\t"
        "v_add_f32_dpp v100, v100, v100 row_ror:8 row_mask:0xf bank_mask:0xf\n\tds_write_b32 %[la], v100\n\ts_waitcnt lgkmcnt(0)\n\ts_barrier"
        : [n] "+s"(left) : [vo] "v"(lane * 16), [rs] "s"(wsr), [so] "s"(WS_Y + w * 16384 + wave * 2048), [tag] "s"(tag), [k0] "s"(UP0), [k1] "s"(UP1),
          [la] "v"(lb + (unsigned)offsetof(Lds, fin) + ((lane >> 4) * 4 + ((lane & 7) >> 1)) * 64 + wave * 8 + (lane & 1) * 4) : POLLCLOB);
  } else if (wave == 8) {
    unsigned fa = (lane & 15) * 64, ooff = lane < 16 ? ((w >> 1) * 32 + (w & 1) * 4 + (lane >> 2) * 8 + (lane & 3)) * 4 : OOR;
    const rsrc_t outr = mkrs(a4[2], 2 * DM * 2);
    asm volatile("" : "+v"(fa), "+v"(ooff) : "s"(outr));
    asm volatile("s_barrier" ::: "memory");
    const f32x4* f = (const f32x4*)((const char*)L.fin + fa);
    const f32x4 t = (f[0] + f[1]) + (f[2] + f[3]);
    __builtin_amdgcn_raw_buffer_store_b32(cvtpk(t[0] + t[2], t[1] + t[3]), outr, ooff, 0, SC1);
  } else {
    asm volatile("s_barrier" ::: "memory");
    if (w == NRED - 1 && tid == 9 * 64) __hip_atomic_store((GAS unsigned*)a4[3], tag, __ATOMIC_RELAXED, __HIP_MEMORY_SCOPE_AGENT);
  }
}

extern "C" void kernel_launch(void* const* d_in, const int* in_sizes, int n_in, void* d_out, int out_size, void* d_ws, size_t ws_size, hipStream_t stream) {
  fused_decode<<<256, NTHR, 0, stream>>>(d_in[1], d_in[2], d_in[3], d_in[0], d_in[6], d_in[7], d_in[8], d_in[5], d_in[4], d_in[9], d_out, d_ws);
}
```

```cpp
#define PF1 1
#define PF2 1
#define STAMP
#define PM1 2
#define PM2 2
#define PW1 0
#define PK1 0
#define PW2 0
#define PK2 0
#define PSC 8
#include <hip/hip_runtime.h>
#include <hip/hip_bf16.h>
#include <stdint.h>

#define GAS __attribute__((address_space(1)))
#define LAS __attribute__((address_space(3)))
#define RLX_AGENT __ATOMIC_RELAXED, __HIP_MEMORY_SCOPE_AGENT
#define RLX_WG __ATOMIC_RELAXED, __HIP_MEMORY_SCOPE_WORKGROUP
typedef unsigned u32x4 __attribute__((ext_vector_type(4)));
typedef unsigned u32x2 __attribute__((ext_vector_type(2)));
typedef float f32x4 __attribute__((ext_vector_type(4)));
typedef short s16x4 __attribute__((ext_vector_type(4)));
typedef __bf16 bf16x2 __attribute__((ext_vector_type(2)));
typedef __bf16 bf16x8 __attribute__((ext_vector_type(8)));
typedef unsigned long long u64;
typedef u64 u64x2 __attribute__((ext_vector_type(2)));
typedef u64 u64x4 __attribute__((ext_vector_type(4)));
typedef __amdgpu_buffer_rsrc_t rsrc_t;

constexpr int DM = 576, NQ = 4096;
constexpr int NWAVE = 11, NTHR = NWAVE * 64, NRED = 36;
constexpr unsigned WS_EPOCH = 0, WS_Q = 4096, WS_O = WS_Q + 16 * 4096, WS_Y = WS_O + 256 * 2048, WS_END = WS_Y + NRED * 16384;
constexpr size_t WS_STAMP = 8u << 20;
constexpr unsigned SPIN_CAP = 1u << 17;

#define FIRST1 4
#define FIRST2 16
#define QSLEEP 64
#define XPOS 20
#define HSH 4
#define MID 10
#define HFIRST 1
#define ROFF 0
#define BUMP 35
#define HR 3
#define GAPS 2
#define PGAL 1
#define TLBP 3
#define RND 1
#define VGATE 705
#define LAND 1
#define LN 0
#define OPF 0
#define PW0 1
#define GW 3
#define KWARM 1
#define KAFIRST 1
#define WOGATE 0
#define PIPE 0
#define OUTSC 16
constexpr int NT = 2;
constexpr unsigned OOR = 0x40000000u;

__device__ __forceinline__ float bflo(unsigned u) { return __builtin_bit_cast(float, u << 16); }
__device__ __forceinline__ float bfhi(unsigned u) { return __builtin_bit_cast(float, u & 0xffff0000u); }
__device__ __forceinline__ unsigned fu(float f) { return __float_as_uint(f); }
__device__ __forceinline__ float uf(unsigned u) { return __uint_as_float(u); }
__device__ __forceinline__ unsigned cvtpk(float lo, float hi) { bf16x2 v = {(__bf16)lo, (__bf16)hi}; return __builtin_bit_cast(unsigned, v); }
__device__ __forceinline__ float dot2(unsigned k, unsigned q, float acc) {
  return __builtin_amdgcn_fdot2_f32_bf16(__builtin_bit_cast(bf16x2, k), __builtin_bit_cast(bf16x2, q), acc, false);
}
template <int CTRL> __device__ __forceinline__ float dpp(float x) {
  return __builtin_bit_cast(float, __builtin_amdgcn_mov_dpp(__builtin_bit_cast(int, x), CTRL, 0xf, 0xf, true));
}
constexpr int XOR1 = 0xB1, XOR2 = 0x4E, XOR7 = 0x141, XOR8 = 0x128;
__device__ __forceinline__ f32x4 mfma(u32x4 a, u32x4 b, f32x4 c) {
  return __builtin_amdgcn_mfma_f32_16x16x32_bf16(__builtin_bit_cast(bf16x8, a), __builtin_bit_cast(bf16x8, b), c, 0, 0, 0);
}
__device__ __forceinline__ float fold32(float a, float b) {
  auto r = __builtin_amdgcn_permlane32_swap(fu(a), fu(b), false, false);
  return uf(r[0]) + uf(r[1]);
}
__device__ __forceinline__ float fold16(float a, float b) {
  auto r = __builtin_amdgcn_permlane16_swap(fu(a), fu(b), false, false);
  return uf(r[0]) + uf(r[1]);
}
__device__ __forceinline__ float fmax2(float a, float b) { return __builtin_amdgcn_fmed3f(a, b, INFINITY); }
__device__ __forceinline__ float max16_32(float x) {
  auto s = __builtin_amdgcn_permlane16_swap(fu(x), fu(x), false, false);
  x = fmax2(uf(s[0]), uf(s[1]));
  auto t = __builtin_amdgcn_permlane32_swap(fu(x), fu(x), false, false);
  return fmax2(uf(t[0]), uf(t[1]));
}
__device__ __forceinline__ rsrc_t mkrs(const void* p, unsigned bytes) { return __builtin_amdgcn_make_buffer_rsrc((void*)p, 0, bytes, 0x00020000); }
template <int AUX> __device__ __forceinline__ u32x4 ldb(rsrc_t r, unsigned voff, unsigned soff) { return __builtin_amdgcn_raw_buffer_load_b128(r, voff, soff, AUX); }
constexpr int SC1 = 16;
__device__ __forceinline__ u32x4 ldx(rsrc_t r, unsigned voff, unsigned soff) { asm volatile("" ::: "memory"); return __builtin_amdgcn_raw_buffer_load_b128(r, voff, soff, SC1); }
__device__ __forceinline__ void stx(rsrc_t r, unsigned voff, unsigned soff, u32x4 v) { __builtin_amdgcn_raw_buffer_store_b128(v, r, voff, soff, SC1); }
__device__ __forceinline__ unsigned xor3(unsigned a, unsigned b, unsigned c) { return __builtin_amdgcn_bitop3_b32(a, b, c, 0x96); }
__device__ __forceinline__ u32x4 seal(unsigned a, unsigned b, unsigned c, unsigned tag) { return u32x4{a, b, c, xor3(a, b, c ^ tag)}; }
__device__ __forceinline__ unsigned bad(u32x4 g, unsigned tag) { return xor3(xor3(g[0], g[1], g[2]), g[3], tag); }
__device__ __forceinline__ u32x4 seal4(float a, float b, float c, float d, unsigned tag) {
  return seal(__builtin_amdgcn_perm(fu(a), fu(d), 0x07060501u), __builtin_amdgcn_perm(fu(b), fu(d), 0x07060502u), __builtin_amdgcn_perm(fu(c), fu(d), 0x07060503u), tag);
}
__device__ __forceinline__ float fourth(u32x4 g) { return uf(__builtin_amdgcn_perm(g[2], __builtin_amdgcn_perm(g[1], g[0], 0x0c04000cu), 0x04020100u)); }
#define SB() __builtin_amdgcn_sched_barrier(0)
#define ST(i) do { SB(); u64 t_; asm volatile("s_memrealtime %0\n\ts_waitcnt lgkmcnt(0)" : "=s"(t_) :: "memory"); stp[i] = (unsigned)t_; SB(); } while (0)
#define PAGG(M, mn, mx, sm, c) (M == 0 ? mx : M == 1 ? sm / max(c, 1) : mn)
#define STDUMP() do { GAS u64* pg = (GAS u64*)(ws + WS_STAMP); \
  if (lane == 0) { if (wave == 0) __hip_atomic_store(pg + blockIdx.x, ((u64)tag << 32) | stp[0], RLX_AGENT); \
    if (wave == PW1) __hip_atomic_store(pg + 256 + blockIdx.x, ((u64)tag << 32) | ((PF1) ? stp[PK1] : 0u), RLX_AGENT); \
    if (wave == PW2) __hip_atomic_store(pg + 512 + blockIdx.x, ((u64)tag << 32) | ((PF2) ? stp[PK2] : 0u), RLX_AGENT); } \
  if (blockIdx.x == 0 && wave == 0) { u64 a[12]; \
    for (unsigned sp = 0; sp < SPIN_CAP; ++sp) { bool ok = true; for (int i_ = 0; i_ < 12; ++i_) { a[i_] = __hip_atomic_load(pg + lane + 64 * i_, RLX_AGENT); ok &= (unsigned)(a[i_] >> 32) == tag; } if (__all(ok)) break; } \
    int mn[3], mx[3], sm[3], cn[3]; \
    for (int q_ = 0; q_ < 3; ++q_) { mn[q_] = 0x7fffffff; mx[q_] = -0x7fffffff; sm[q_] = 0; cn[q_] = 0; \
      for (int i_ = 0; i_ < 4; ++i_) { const unsigned v_ = (unsigned)a[q_ * 4 + i_]; if (v_) { const int d_ = (int)(v_ - stp[0]); mn[q_] = min(mn[q_], d_); mx[q_] = max(mx[q_], d_); sm[q_] += d_; cn[q_]++; } } \
      for (int o_ = 1; o_ < 64; o_ <<= 1) { mn[q_] = min(mn[q_], __shfl_xor(mn[q_], o_)); mx[q_] = max(mx[q_], __shfl_xor(mx[q_], o_)); sm[q_] += __shfl_xor(sm[q_], o_); cn[q_] += __shfl_xor(cn[q_], o_); } } \
    const int X = PAGG(PM2, mn[2], mx[2], sm[2], cn[2]) - PAGG(PM1, mn[1], mx[1], sm[1], cn[1]); \
    const unsigned until = stp[0] + mn[0] + 6000 + PSC * X; \
    for (unsigned sp = 0; sp < 1000000; ++sp) { if ((int)((unsigned)__builtin_amdgcn_s_memrealtime() - until) >= 0) break; } asm volatile("s_branch 1f\n\t.p2align 12\n1:"); } } while (0)
#define STDECL() unsigned stp[12] = {0, 0, 0, 0, 0, 0, 0, 0, 0, 0, 0, 0}
#define STPAR , unsigned (&stp)[12]
#define STARG , stp

struct Lds {
  unsigned char wo[9][4096];
  float part[11][2][132];
  unsigned q[2][128];
  unsigned x[144];
  float fin[8][32];
  unsigned short o[32];
  unsigned qready, kissued, issued, done[2], landed;
};

__device__ __forceinline__ void lds_wait(unsigned* p, unsigned n) {
  for (unsigned spins = 0; __hip_atomic_load(p, RLX_WG) < n && spins < (SPIN_CAP << 3); ++spins) __builtin_amdgcn_s_sleep(1);
}
__device__ __forceinline__ void lds_inc(unsigned* p, int lane) { if (lane == 0) __hip_atomic_fetch_add(p, 1u, RLX_WG); }
#define POLL2(MID_) \
    "1:\n\tbuffer_load_dwordx4 v[100:103], %[vo], %[rs], %[so] offen sc1\n\tbuffer_load_dwordx4 v[104:107], %[vo], %[rs], %[so] offen offset:1024 sc1\n\t" \
    "s_sub_u32 %[n], %[n], 1\n\ts_waitcnt vmcnt(1)\n\t" \
    "v_bitop3_b32 v108, v100, v101, v102 bitop3:0x96\n\tv_bitop3_b32 v108, v108, v103, %[tag] bitop3:0x96\n\t" MID_ "s_waitcnt vmcnt(0)\n\t" \
    "v_bitop3_b32 v109, v104, v105, v106 bitop3:0x96\n\tv_bitop3_b32 v109, v109, v107, %[tag] bitop3:0x96\n\t" \
    "v_or_b32 v108, v108, v109\n\tv_cmp_ne_u32 vcc, 0, v108\n\ts_cbranch_vccz 2f\n\ts_cbranch_scc0 1b\n2:\n\t"
#define POLLCLOB "memory", "vcc", "scc", "v100", "v101", "v102", "v103", "v104", "v105", "v106", "v107", "v108", "v109", "v110", "v111", "v112", "v113", "v114", "v115", "v116", "v117"

__global__ void __launch_bounds__(NTHR, 3) fused_decode(const void* kA, const void* vA, const void* wqA, const void* xA, const void* kB, const void* vB, const void* wqB, const void* xB,
                                                         const void* woA, const void* woB, void* out, void* wsp) {
  __shared__ __attribute__((aligned(16))) Lds L;
  STDECL();
  ST(0);
  const auto karg = __builtin_amdgcn_kernarg_segment_ptr();
  if (KWARM) asm volatile("s_load_dword s94, %0, 0x0\n\ts_load_dword s95, %0, 0x40" :: "s"(karg) : "s94", "s95");
  SB();
  u64x2 a2; u64x4 a4;
#define GETARGS(OFF, ...) asm volatile("s_load_dwordx4 %0, %[ka], %[off]\n\ts_load_dwordx8 %1, %[ka], 0x40\n\ts_waitcnt lgkmcnt(0)" : "=&s"(a2), "=&s"(a4) : [ka] "s"(karg), [off] "s"(OFF), __VA_ARGS__ : "memory")
  const int tid = threadIdx.x, lane = tid & 63, wave = __builtin_amdgcn_readfirstlane(tid >> 6);
  const int w = blockIdx.x, h = (w & 7) * 2 + (w >> 7), s = (w >> 3) & 15;
  const int hp = (h + HSH) & 15, pbr = s >> 3;
  unsigned tag;
  const unsigned tstart = (unsigned)__builtin_amdgcn_s_memrealtime();

  if (__builtin_expect(wave == 0, 1)) {
    const int rs = lane >> 3, c = lane & 7;
    const int qbr = (w >> 1) & 1, half = (w >> 2) & 1, j = w >> 3, blk = (j >> 1) * 4 + (j & 1) * 2 + ((j ^ w) & 1);
    unsigned woff = (unsigned)((half * 288 + rs * 36) * NQ + blk * 64 + c * 8) * 2;
    asm volatile("" : "+v"(woff));
    GETARGS(qbr * 32 + 16, "s"(half));
    const rsrc_t wr = mkrs((const void*)a2[0], DM * NQ * 2);
    const rsrc_t wsr = mkrs((const void*)a4[3], WS_END);
    if (TLBP & 8) asm volatile("s_load_dword s94, %0, %1\n\ts_load_dword s95, %0, %2" :: "s"(a2[0]), "s"((half * 288 + 287) * NQ * 2), "s"(half * 288 * NQ * 2) : "s94", "s95");
    SB();
    u32x4 wv[36], xr;
    unsigned epoch, so = 0;
#pragma unroll
    for (int t = 0; t < 36; ++t) {
      wv[t] = ldb<NT>(wr, woff, so); SB();
      so += NQ * 2; asm volatile("" : "+s"(so)); SB();
      if ((TLBP & 1) && t == 0) asm volatile("s_load_dword s94, %0, 0x0" :: "s"(a2[1]) : "s94");
      if (t == XPOS) {
        xr = ldb<0>(mkrs((const char*)a2[1] + half * 576, 576), lane * 16, 0);
        epoch = __hip_atomic_load((GAS unsigned*)(a4[3] + WS_EPOCH), RLX_AGENT); SB();
      }
    }
    __builtin_amdgcn_s_barrier();
    SB();
    ST(8);
    if (lane < 36) *(u32x4*)&L.x[lane * 4] = xr;
    float acc[8] = {0, 0, 0, 0, 0, 0, 0, 0};
#pragma unroll
    for (int t4 = 0; t4 < 9; ++t4) {
      const u32x2 xw = *(const u32x2*)&L.x[rs * 18 + t4 * 2];
#pragma unroll
      for (int u = 0; u < 4; ++u) {
        const int t = t4 * 4 + u;
        const unsigned xl = (u & 1) ? xw[u >> 1] >> 16 : xw[u >> 1] & 0xffffu, xh = (u & 1) ? xw[u >> 1] & 0xffff0000u : xw[u >> 1] << 16;
#pragma unroll
        for (int k = 0; k < 4; ++k) { acc[2 * k] = dot2(wv[t][k], xl, acc[2 * k]); acc[2 * k + 1] = dot2(wv[t][k], xh, acc[2 * k + 1]); }
      }
      SB();
    }
    float b[4];
#pragma unroll
    for (int k = 0; k < 4; ++k) { b[k] = fold32(acc[k], acc[k + 4]); b[k] = fold16(b[k], b[k]); b[k] += dpp<XOR8>(b[k]); }
    asm volatile("" : "+v"(epoch));
    tag = __builtin_amdgcn_readfirstlane(epoch) + 1u;
    tag += tag == 0u;
    stx(wsr, (lane & 24) ? OOR : (c * 2 + (lane >> 5)) * 16, WS_Q + (blk >> 2) * 4096 + (qbr * 2 + half) * 1024 + (blk & 3) * 256, seal4(b[0], b[1], b[2], b[3], tag));
    unsigned opf = 0;
    if (OPF) opf = __builtin_amdgcn_raw_buffer_load_b32(mkrs((const void*)a4[2], w < NRED ? 4 : 0), lane * 4, 0, SC1);
    if (TLBP & 4) asm volatile("s_load_dword s94, %0, %2\n\ts_load_dword s95, %1, 0x0" :: "s"(pbr ? a4[1] : a4[0]), "s"(a4[2]), "s"((hp * 256 + (s & 4) * 32) * DM * 2) : "s94", "s95");
    ST(1);
    __builtin_amdgcn_s_sleep(QSLEEP);
    u32x4 g[4];
    for (unsigned spins = 0; spins < SPIN_CAP; ++spins) {
#pragma unroll
      for (int i = 0; i < 4; ++i) g[i] = ldx(wsr, lane * 16 + i * 1024, WS_Q + h * 4096);
      if (!__any((bad(g[0], tag) | bad(g[1], tag) | bad(g[2], tag) | bad(g[3], tag)) != 0u)) break;
    }
#pragma unroll
    for (int bb = 0; bb < 2; ++bb) {
      const u32x4 p0 = g[2 * bb], p1 = g[2 * bb + 1];
      *(u32x2*)&L.q[bb][2 * lane] = u32x2{cvtpk(uf(p0[0]) + uf(p1[0]), uf(p0[1]) + uf(p1[1])), cvtpk(uf(p0[2]) + uf(p1[2]), fourth(p0) + fourth(p1))};
    }
    __builtin_amdgcn_fence(__ATOMIC_RELEASE, "workgroup");
    if (lane == 0) __hip_atomic_store(&L.qready, tag, RLX_WG);
    asm volatile("" :: "v"(opf));
    ST(2);
    if (PGAL) asm volatile("s_branch 1f\n\t.p2align 12\n1:");
  } else {
    const int br = wave == 1 || wave == 3 ? 0 : 1;
    const int pair = wave < 5 ? 3 : (wave - 5) >> 1, odd = wave < 5 ? (wave - 1) >> 1 : (wave - 5) & 1;
    const int g = lane >> 4, i = lane & 15;
    const unsigned sbase = (h * 2048 + pair * 512 + s * 32 + odd * 2) * 1024;
    unsigned loff = sbase + lane * 16;
    const bool late = wave < 4, closer = wave < 3;
    unsigned so = ((s + HR * h) & 7) * 4096, knead = KAFIRST && br ? 2u : 0u;
    if (tid == 64) { L.qready = 0u; L.kissued = 0u; L.issued = 0u; L.done[0] = 0u; L.done[1] = 0u; L.landed = 0u; }
    asm volatile("" : "+v"(loff));
    asm volatile("" : "+s"(so), "+s"(knead));
    GETARGS(br * 32, "s"(so));
    const rsrc_t kr = mkrs((const void*)a2[0], 32 << 20), vr = mkrs((const void*)a2[1], 32 << 20);
    const rsrc_t wsr = mkrs((const void*)a4[3], WS_END);
    if ((TLBP & 2) && (unsigned)(wave - 3) < 2u) asm volatile("s_load_dword s94, %0, %2\n\ts_load_dword s95, %1, %2" :: "s"(a2[0]), "s"(a2[1]), "s"(sbase) : "s94", "s95");
    asm volatile("" :: "s"(kr), "s"(vr));
    __builtin_amdgcn_s_barrier();
    SB();
    ST(8);
    u32x4 K[16], V[16];
#define LOAD16(X, R) _Pragma("unroll") for (int t = 0; t < 16; t += 2) { \
      X[t] = ldb<NT>(R, loff, so); SB(); unsigned nx = so + 4096u; asm volatile("" : "+s"(nx)); SB(); \
      X[t + 1] = ldb<NT>(R, loff + 1024, so); SB(); so = nx & 0x7000u; asm volatile("" : "+s"(so)); SB(); }
    if (knead) lds_wait(&L.kissued, knead);
    SB();
#pragma unroll
    for (int t = 0; t < 16; t += 2)
      asm volatile("buffer_load_dwordx4 %0, %3, %4, %2 offen nt\n\ts_nop 0\n\tbuffer_load_dwordx4 %1, %3, %4, %2 offen offset:1024 nt\n\t"
                   "s_addk_i32 %2, 0x1000\n\ts_and_b32 %2, %2, 0x7000" : "=&v"(K[t]), "=&v"(K[t + 1]), "+s"(so) : "v"(loff), "s"(kr) : "scc");
    SB();
    lds_inc(&L.kissued, lane);
    ST(9);
    if (LAND) { asm volatile("s_waitcnt vmcnt(%0)" :: "i"(LN) : "memory"); lds_inc(&L.landed, lane); }
    if (!late) {
      lds_wait(&L.kissued, 10u);
      for (unsigned spins = 0; (unsigned)__builtin_amdgcn_s_memrealtime() - tstart < VGATE && (!LAND || __hip_atomic_load(&L.landed, RLX_WG) < 10u) && spins < SPIN_CAP; ++spins) __builtin_amdgcn_s_sleep(1);
      SB();
      LOAD16(V, vr)
      lds_inc(&L.issued, lane);
      ST(1);
      asm volatile("s_waitcnt vmcnt(16)" ::: "memory");
    } else {
      asm volatile("s_waitcnt vmcnt(0)" ::: "memory");
    }
#pragma unroll
    for (int t = 0; t < 16; ++t) asm volatile("" : "+v"(K[t]));
    lds_wait(&L.qready, 1u);
    tag = __builtin_amdgcn_readfirstlane(__hip_atomic_load(&L.qready, RLX_WG));
    __builtin_amdgcn_fence(__ATOMIC_ACQUIRE, "workgroup");
    const u32x4 q0 = *(const u32x4*)&L.q[br][4 * i], q1 = *(const u32x4*)&L.q[br][64 + 4 * i];
    constexpr float C2 = RND ? 1.4426950408889634f : 0.088388347648318440f * 1.4426950408889634f;
    float sc[16], m = -INFINITY;
#pragma unroll
    for (int t = 0; t < 16; ++t) {
      float d0 = 0, d1 = 0;
#pragma unroll
      for (int k = 0; k < 4; ++k) { d0 = dot2(K[t][k], q0[k], d0); d1 = dot2(K[t][k], q1[k], d1); }
      d0 += dpp<XOR8>(d0); d1 += dpp<XOR8>(d1);
      float d = (i & 8) ? d1 : d0;
      d += dpp<XOR7>(d); d += dpp<XOR2>(d); d += dpp<XOR1>(d);
      sc[t] = d;
    }
#pragma unroll
    for (int t = 0; t < 16; t += 2) {
      if (RND) {
        const unsigned r1 = cvtpk(sc[t], sc[t + 1]), r2 = cvtpk(bflo(r1) * 0.088388347648318440f, bfhi(r1) * 0.088388347648318440f);
        sc[t] = bflo(r2); sc[t + 1] = bfhi(r2);
      }
      m = __builtin_fmaxf(m, __builtin_fmaxf(sc[t], sc[t + 1]));
    }
    m = max16_32(m) * C2;
    unsigned msk[4];
#pragma unroll
    for (int k = 0; k < 4; ++k) msk[k] = (((i & 7) >> 1) == k) ? ((i & 1) ? 0xffff0000u : 0x0000ffffu) : 0u;
    float l = 0;
    u32x4 a[16];
#pragma unroll
    for (int t = 0; t < 16; ++t) {
      const float p = __builtin_amdgcn_exp2f(fmaf(sc[t], C2, -m));
      const unsigned pp = cvtpk(p, p);
      l += p;
      a[t] = u32x4{pp & msk[0], pp & msk[1], pp & msk[2], pp & msk[3]};
    }
    l = fold16(l, l); l = fold32(l, l);
    if ((lane & 55) == 0) *(float2*)&L.part[wave][lane >> 3][0] = make_float2(m, l);
#pragma unroll
    for (int t = 0; t < 16; ++t) asm volatile("" : "+v"(a[t]));
    SB();
    ST(2);
    if (late) {
      __builtin_amdgcn_fence(__ATOMIC_RELEASE, "workgroup");
      lds_wait(&L.issued, wave == 2 ? 7u : wave == 3 ? 8u : 9u);
      SB();
      LOAD16(V, vr)
      lds_inc(&L.issued, lane);
      ST(1);
    }
    const int hd = g >> 1, oi = 4 + 8 * i + 4 * (g & 1);
    float f = 0, eo = 0;
    unsigned lse = 0, poff = WS_O + h * 32768 + br * 16384 + s * 128 + (lane >> 3) * 2048 + (lane & 7) * 16;
    if (wave == 1) {
      __builtin_amdgcn_fence(__ATOMIC_ACQUIRE, "workgroup");
      const float2 me = *(const float2*)&L.part[1][hd][0], r = *(const float2*)&L.part[3][hd][0];
      const float M = fmax2(me.x, r.x);
      f = __builtin_amdgcn_exp2f(me.x - M); eo = __builtin_amdgcn_exp2f(r.x - M);
      const float Ls = fmaf(f, me.y, eo * r.y), inv = __builtin_amdgcn_rcpf(Ls);
      f *= inv; eo *= inv;
      lse = fu(M + __builtin_amdgcn_logf(Ls));
    }
    asm volatile("" : "+v"(f), "+v"(eo), "+v"(lse), "+v"(poff));
    SB();
    f32x4 acc = {0, 0, 0, 0};
#pragma unroll
    for (int t = 0; t < MID; ++t) acc = mfma(a[t], V[t], acc);
    asm volatile("" : "+v"(acc));
    SB();
    f32x4 base = {0, 0, 0, 0};
    if (closer) {
      lds_wait(&L.done[br], br ? 7u : 1u);
      __builtin_amdgcn_fence(__ATOMIC_ACQUIRE, "workgroup");
      if (br) {
        const float2 me = *(const float2*)&L.part[2][hd][0];
        float2 r[7]; f32x4 o[7];
#pragma unroll
        for (int u = 0; u < 7; ++u) { r[u] = *(const float2*)&L.part[4 + u][hd][0]; o[u] = *(const f32x4*)&L.part[4 + u][hd][oi]; }
        float M = me.x;
#pragma unroll
        for (int u = 0; u < 7; ++u) M = fmax2(M, r[u].x);
        f = __builtin_amdgcn_exp2f(me.x - M);
        float Ls = f * me.y;
#pragma unroll
        for (int u = 0; u < 7; ++u) { const float e = __builtin_amdgcn_exp2f(r[u].x - M); Ls = fmaf(e, r[u].y, Ls); base += e * o[u]; }
        eo = __builtin_amdgcn_rcpf(Ls);
        f *= eo;
        lse = fu(M + __builtin_amdgcn_logf(Ls));
      } else {
        base = *(const f32x4*)&L.part[3][hd][oi];
      }
      ST(4);
    }
    SB();
#pragma unroll
    for (int t = MID; t < 15; ++t) acc = mfma(a[t], V[t], acc);
    base *= eo;
    asm volatile("" : "+v"(base));
    acc = mfma(a[15], V[15], acc);
    if (closer) {
      acc = acc * f + base;
      stx(wsr, poff, 0, seal(cvtpk(acc[0], acc[1]), cvtpk(acc[2], acc[3]), lse, tag));
    } else {
      *(f32x4*)&L.part[wave][hd][oi] = acc;
      __builtin_amdgcn_fence(__ATOMIC_RELEASE, "workgroup");
      lds_inc(&L.done[br], lane);
    }
    ST(3);
  }
  const rsrc_t wsr = mkrs((const void*)a4[3], WS_END);
  GAS unsigned char* ws = (GAS unsigned char*)a4[3];
  const int gw = GW;
  const int pw = wave == 0 ? 8 : wave == PW0 ? 0 : wave < 4 ? -1 : wave - 3;
  if (pw >= 0) {
    u32x4 wf[4];
    unsigned yoff, oaddr = (lane >> 4) * 16;
    lds_wait(&L.issued, 10u);
    if (WOGATE) for (unsigned spins = 0; (unsigned)__builtin_amdgcn_s_memrealtime() - tstart < WOGATE && spins < SPIN_CAP; ++spins) __builtin_amdgcn_s_sleep(1);
    const int row0 = hp * 256 + ((s >> 2) & 1) * 128 + (s & 1) * 64 + (s & 2) * 2;
    const rsrc_t wr = mkrs((const uint16_t*)(pbr ? a4[1] : a4[0]) + row0 * DM + pw * 64, 60 * DM * 2);
    const unsigned woff = ((8 * (lane >> 5) + ((lane >> 3) & 3)) * DM + (lane & 7) * 8) * 2;
    LAS unsigned char* dst = (LAS unsigned char*)&L.wo[pw][0];
#pragma unroll
    for (int t = 0; t < 4; ++t) __builtin_amdgcn_raw_ptr_buffer_load_lds(wr, dst + t * 1024, 16, woff, t * 16 * DM * 2, 0, NT);
    yoff = lane < 16 ? WS_Y + (pbr * 18 + pw * 2 + (lane >> 3)) * 16384 + (hp * 8 + (s & 7)) * 128 + (lane & 7) * 16 : OOR;
    asm volatile("s_waitcnt vmcnt(0)" ::: "memory");
    LAS unsigned char* src = dst + (lane >> 4) * 1024 + ((lane >> 2) & 3) * 128 + (lane & 3) * 8;
#pragma unroll
    for (int b = 0; b < 4; ++b) {
      const u32x2 lo = __builtin_bit_cast(u32x2, __builtin_amdgcn_ds_read_tr16_b64_v4i16((LAS s16x4*)(src + b * 32)));
      const u32x2 hi = __builtin_bit_cast(u32x2, __builtin_amdgcn_ds_read_tr16_b64_v4i16((LAS s16x4*)(src + b * 32 + 512)));
      wf[b] = u32x4{lo[0], lo[1], hi[0], hi[1]};
    }
    unsigned k0 = 0x07060501u, k1 = 0x07060502u, k2 = 0x07060503u;
    asm volatile("" : "+s"(k0), "+s"(k1), "+s"(k2) : "s"(wsr));
    asm volatile("" : "+v"(wf[0]), "+v"(wf[1]), "+v"(wf[2]), "+v"(wf[3]), "+v"(yoff), "+v"(oaddr));
    ST(9);
    asm volatile("s_barrier" ::: "memory");
    ST(11);
    const u32x4 ov = *(const u32x4*)((const char*)L.o + oaddr);
    const f32x4 z = {0, 0, 0, 0};
    f32x4 y3 = mfma(ov, wf[3], z), y0 = mfma(ov, wf[0], z), y1 = mfma(ov, wf[1], z), y2 = mfma(ov, wf[2], z);
    SB();
    const unsigned p0 = __builtin_amdgcn_perm(fu(y0[0]), fu(y3[0]), k0), p1 = __builtin_amdgcn_perm(fu(y1[0]), fu(y3[0]), k1);
    const unsigned c01 = xor3(p0, p1, tag), p2 = __builtin_amdgcn_perm(fu(y2[0]), fu(y3[0]), k2);
    stx(wsr, yoff, 0, u32x4{p0, p1, p2, c01 ^ p2});
    asm volatile("" :: "v"(y0), "v"(y1), "v"(y2), "v"(y3));
  } else if (wave == gw) {
    const unsigned oa = (unsigned)(size_t)(LAS void*)&L.o[4 * (lane & 7) + (lane >> 4)];
    unsigned left = SPIN_CAP;
    __builtin_amdgcn_s_sleep(FIRST1);
    asm volatile(POLL2("")
        "v_max_f32 v112, v102, v106\n\tv_lshlrev_b32 v108, 16, v100\n\tv_and_b32 v109, 0xffff0000, v100\n\t"
        "v_max_f32_dpp v112, v112, v112 row_ror:8 row_mask:0xf bank_mask:0xf\n\tv_lshlrev_b32 v110, 16, v101\n\t"
        "v_mov_b32 v113, v112\n\tv_and_b32 v111, 0xffff0000, v101\n\tv_lshlrev_b32 v114, 16, v104\n\t"
        "v_permlane16_swap_b32 v112, v113\n\tv_max_f32 v112, v112, v113\n\t"
        "v_mov_b32 v113, v112\n\tv_and_b32 v115, 0xffff0000, v104\n\tv_lshlrev_b32 v116, 16, v105\n\t"
        "v_permlane32_swap_b32 v112, v113\n\tv_max_f32 v112, v112, v113\n\t"
        "v_sub_f32 v102, v102, v112\n\tv_sub_f32 v106, v106, v112\n\tv_exp_f32 v102, v102\n\tv_exp_f32 v106, v106\n\t"
        "v_and_b32 v117, 0xffff0000, v105\n\tv_add_f32 v112, v102, v106\n\t"
        "v_pk_mul_f32 v[108:109], v[108:109], v[102:103] op_sel_hi:[1,0]\n\tv_pk_mul_f32 v[110:111], v[110:111], v[102:103] op_sel_hi:[1,0]\n\t"
        "v_add_f32_dpp v112, v112, v112 row_ror:8 row_mask:0xf bank_mask:0xf\n\t"
        "v_pk_fma_f32 v[108:109], v[114:115], v[106:107], v[108:109] op_sel_hi:[1,0,1]\n\tv_mov_b32 v113, v112\n\t"
        "v_pk_fma_f32 v[110:111], v[116:117], v[106:107], v[110:111] op_sel_hi:[1,0,1]\n\ts_nop 0\n\t"
        "v_permlane16_swap_b32 v112, v113\n\tv_permlane32_swap_b32 v108, v110\n\tv_permlane32_swap_b32 v109, v111\n\t"
        "v_add_f32 v112, v112, v113\n\tv_add_f32 v108, v108, v110\n\tv_add_f32 v109, v109, v111\n\tv_mov_b32 v113, v112\n\ts_nop 0\n\t"
        "v_permlane16_swap_b32 v108, v109\n\tv_permlane32_swap_b32 v112, v113\n\t"
        "v_add_f32 v108, v108, v109\n\tv_add_f32 v112, v112, v113\n\tv_rcp_f32 v112, v112\n\t"
        "v_add_f32_dpp v108, v108, v108 row_ror:8 row_mask:0xf bank_mask:0xf\n\tv_mul_f32 v108, v108, v112\n\t"
        "v_cvt_pk_bf16_f32 v108, v108, v108\n\tds_write_b16 %[la], v108"
        : [n] "+s"(left) : [vo] "v"(lane * 16), [rs] "s"(wsr), [so] "s"(WS_O + (hp * 16 + s) * 2048), [tag] "s"(tag), [la] "v"(oa) : POLLCLOB);
    ST(5);
    asm volatile("s_waitcnt lgkmcnt(0)\n\ts_barrier" ::: "memory");
  } else {
    asm volatile("s_barrier" ::: "memory");
  }
  ST(6);
  const unsigned rd = w - ROFF;
  if (rd >= NRED) { STDUMP(); return; }
  if (wave < 8) {
    const unsigned wa = (unsigned)(size_t)(LAS void*)&L.fin[0][0] + ((lane >> 4) * 4 + ((lane & 7) >> 1)) * 64 + wave * 8 + (lane & 1) * 4;
    unsigned left = SPIN_CAP;
    __builtin_amdgcn_s_sleep(FIRST2);
    asm volatile(POLL2("v_perm_b32 v110, v101, v100, %[k0]\n\tv_perm_b32 v110, v102, v110, %[k1]\n\t")
        "v_perm_b32 v111, v105, v104, %[k0]\n\tv_pk_add_f32 v[100:101], v[100:101], v[104:105]\n\tv_add_f32 v102, v102, v106\n\t"
        "v_perm_b32 v111, v106, v111, %[k1]\n\tv_add_f32 v110, v110, v111\n\t"
        "v_permlane32_swap_b32 v100, v102\n\tv_add_f32 v100, v100, v102\n\t"
        "v_permlane32_swap_b32 v101, v110\n\tv_add_f32 v101, v101, v110\n\ts_nop 1\n\t"
        "v_permlane16_swap_b32 v100, v101\n\tv_add_f32 v100, v100, v101\n\ts_nop 1\n\t"
        "v_add_f32_dpp v100, v100, v100 row_ror:8 row_mask:0xf bank_mask:0xf\n\tds_write_b32 %[la], v100"
        : [n] "+s"(left) : [vo] "v"(lane * 16), [rs] "s"(wsr), [so] "s"(WS_Y + rd * 16384 + wave * 2048), [tag] "s"(tag), [la] "v"(wa),
          [k0] "s"(0x0c04000cu), [k1] "s"(0x04020100u) : POLLCLOB);
    asm volatile("s_waitcnt lgkmcnt(0)\n\ts_barrier" ::: "memory");
  } else if (wave == 8) {
    unsigned fa = (unsigned)(size_t)(LAS void*)&L.fin[0][0] + (lane & 15) * 64, ooff = lane < 16 ? ((rd >> 1) * 32 + (rd & 1) * 4 + (lane >> 2) * 8 + (lane & 3)) * 4 : OOR;
    const rsrc_t outr = mkrs((const void*)a4[2], 2 * DM * 2);
    asm volatile("" : "+v"(fa), "+v"(ooff) : "s"(outr));
    asm volatile("s_barrier" ::: "memory");
    const LAS f32x4* f = (const LAS f32x4*)(size_t)fa;
    const f32x4 t = (f[0] + f[1]) + (f[2] + f[3]);
    __builtin_amdgcn_raw_buffer_store_b32(cvtpk(t[0] + t[2], t[1] + t[3]), outr, ooff, 0, OUTSC);
    ST(7);
  } else {
    asm volatile("s_barrier" ::: "memory");
    if (rd == BUMP && tid == 576) __hip_atomic_store((GAS unsigned*)(ws + WS_EPOCH), tag, RLX_AGENT);
  }
  STDUMP();
}

extern "C" void kernel_launch(void* const* d_in, const int* in_sizes, int n_in, void* d_out, int out_size, void* d_ws, size_t ws_size, hipStream_t stream) {
  fused_decode<<<256, NTHR, 0, stream>>>(d_in[1], d_in[2], d_in[3], d_in[0], d_in[6], d_in[7], d_in[8], d_in[5], d_in[4], d_in[9], d_out, d_ws);
}
```

```cpp
#define PF1 1
#define PF2 1
#define STAMP
#define PM1 1
#define PM2 1
#define PW1 0
#define PK1 0
#define PW2 0
#define PK2 4
#define PSC 16
#include <hip/hip_runtime.h>
#include <hip/hip_bf16.h>
#include <stdint.h>

#define GAS __attribute__((address_space(1)))
#define LAS __attribute__((address_space(3)))
#define RLX_AGENT __ATOMIC_RELAXED, __HIP_MEMORY_SCOPE_AGENT
#define RLX_WG __ATOMIC_RELAXED, __HIP_MEMORY_SCOPE_WORKGROUP
typedef unsigned u32x4 __attribute__((ext_vector_type(4)));
typedef unsigned u32x2 __attribute__((ext_vector_type(2)));
typedef float f32x4 __attribute__((ext_vector_type(4)));
typedef short s16x4 __attribute__((ext_vector_type(4)));
typedef __bf16 bf16x2 __attribute__((ext_vector_type(2)));
typedef __bf16 bf16x8 __attribute__((ext_vector_type(8)));
typedef unsigned long long u64;
typedef u64 u64x2 __attribute__((ext_vector_type(2)));
typedef u64 u64x4 __attribute__((ext_vector_type(4)));
typedef __amdgpu_buffer_rsrc_t rsrc_t;

constexpr int DM = 576, NQ = 4096;
constexpr int NWAVE = 11, NTHR = NWAVE * 64, NRED = 36;
constexpr unsigned WS_EPOCH = 0, WS_Q = 4096, WS_O = WS_Q + 16 * 4096, WS_Y = WS_O + 256 * 2048, WS_END = WS_Y + NRED * 16384;
constexpr size_t WS_STAMP = 8u << 20;
constexpr unsigned SPIN_CAP = 1u << 17;

#define FIRST1 4
#define FIRST2 16
#define QSLEEP 64
#define XPOS 20
#define HSH 4
#define MID 10
#define HFIRST 1
#define ROFF 0
#define BUMP 35
#define HR 3
#define GAPS 2
#define PGAL 1
#define TLBP 2
#define RND 1
#define VGATE 705
#define LAND 1
#define LN 0
#define OPF 0
#define PW0 1
#define GW 3
#define KWARM 1
#define KAFIRST 1
#define WOGATE 0
#define PIPE 0
#define OUTSC 16
constexpr int NT = 2;
constexpr unsigned OOR = 0x40000000u;

__device__ __forceinline__ float bflo(unsigned u) { return __builtin_bit_cast(float, u << 16); }
__device__ __forceinline__ float bfhi(unsigned u) { return __builtin_bit_cast(float, u & 0xffff0000u); }
__device__ __forceinline__ unsigned fu(float f) { return __float_as_uint(f); }
__device__ __forceinline__ float uf(unsigned u) { return __uint_as_float(u); }
__device__ __forceinline__ unsigned cvtpk(float lo, float hi) { bf16x2 v = {(__bf16)lo, (__bf16)hi}; return __builtin_bit_cast(unsigned, v); }
__device__ __forceinline__ float dot2(unsigned k, unsigned q, float acc) {
  return __builtin_amdgcn_fdot2_f32_bf16(__builtin_bit_cast(bf16x2, k), __builtin_bit_cast(bf16x2, q), acc, false);
}
template <int CTRL> __device__ __forceinline__ float dpp(float x) {
  return __builtin_bit_cast(float, __builtin_amdgcn_mov_dpp(__builtin_bit_cast(int, x), CTRL, 0xf, 0xf, true));
}
constexpr int XOR1 = 0xB1, XOR2 = 0x4E, XOR7 = 0x141, XOR8 = 0x128;
__device__ __forceinline__ f32x4 mfma(u32x4 a, u32x4 b, f32x4 c) {
  return __builtin_amdgcn_mfma_f32_16x16x32_bf16(__builtin_bit_cast(bf16x8, a), __builtin_bit_cast(bf16x8, b), c, 0, 0, 0);
}
__device__ __forceinline__ float fold32(float a, float b) {
  auto r = __builtin_amdgcn_permlane32_swap(fu(a), fu(b), false, false);
  return uf(r[0]) + uf(r[1]);
}
__device__ __forceinline__ float fold16(float a, float b) {
  auto r = __builtin_amdgcn_permlane16_swap(fu(a), fu(b), false, false);
  return uf(r[0]) + uf(r[1]);
}
__device__ __forceinline__ float fmax2(float a, float b) { return __builtin_amdgcn_fmed3f(a, b, INFINITY); }
__device__ __forceinline__ float max16_32(float x) {
  auto s = __builtin_amdgcn_permlane16_swap(fu(x), fu(x), false, false);
  x = fmax2(uf(s[0]), uf(s[1]));
  auto t = __builtin_amdgcn_permlane32_swap(fu(x), fu(x), false, false);
  return fmax2(uf(t[0]), uf(t[1]));
}
__device__ __forceinline__ rsrc_t mkrs(const void* p, unsigned bytes) { return __builtin_amdgcn_make_buffer_rsrc((void*)p, 0, bytes, 0x00020000); }
template <int AUX> __device__ __forceinline__ u32x4 ldb(rsrc_t r, unsigned voff, unsigned soff) { return __builtin_amdgcn_raw_buffer_load_b128(r, voff, soff, AUX); }
constexpr int SC1 = 16;
__device__ __forceinline__ u32x4 ldx(rsrc_t r, unsigned voff, unsigned soff) { asm volatile("" ::: "memory"); return __builtin_amdgcn_raw_buffer_load_b128(r, voff, soff, SC1); }
__device__ __forceinline__ void stx(rsrc_t r, unsigned voff, unsigned soff, u32x4 v) { __builtin_amdgcn_raw_buffer_store_b128(v, r, voff, soff, SC1); }
__device__ __forceinline__ unsigned xor3(unsigned a, unsigned b, unsigned c) { return __builtin_amdgcn_bitop3_b32(a, b, c, 0x96); }
__device__ __forceinline__ u32x4 seal(unsigned a, unsigned b, unsigned c, unsigned tag) { return u32x4{a, b, c, xor3(a, b, c ^ tag)}; }
__device__ __forceinline__ unsigned bad(u32x4 g, unsigned tag) { return xor3(xor3(g[0], g[1], g[2]), g[3], tag); }
__device__ __forceinline__ u32x4 seal4(float a, float b, float c, float d, unsigned tag) {
  return seal(__builtin_amdgcn_perm(fu(a), fu(d), 0x07060501u), __builtin_amdgcn_perm(fu(b), fu(d), 0x07060502u), __builtin_amdgcn_perm(fu(c), fu(d), 0x07060503u), tag);
}
__device__ __forceinline__ float fourth(u32x4 g) { return uf(__builtin_amdgcn_perm(g[2], __builtin_amdgcn_perm(g[1], g[0], 0x0c04000cu), 0x04020100u)); }
#define SB() __builtin_amdgcn_sched_barrier(0)
#define ST(i) do { SB(); u64 t_; asm volatile("s_memrealtime %0\n\ts_waitcnt lgkmcnt(0)" : "=s"(t_) :: "memory"); stp[i] = (unsigned)t_; SB(); } while (0)
#define PAGG(M, mn, mx, sm, c) (M == 0 ? mx : M == 1 ? sm / max(c, 1) : mn)
#define STDUMP() do { GAS u64* pg = (GAS u64*)(ws + WS_STAMP); \
  if (lane == 0) { if (wave == 0) __hip_atomic_store(pg + blockIdx.x, ((u64)tag << 32) | stp[0], RLX_AGENT); \
    if (wave == PW1) __hip_atomic_store(pg + 256 + blockIdx.x, ((u64)tag << 32) | ((PF1) ? stp[PK1] : 0u), RLX_AGENT); \
    if (wave == PW2) __hip_atomic_store(pg + 512 + blockIdx.x, ((u64)tag << 32) | ((PF2) ? stp[PK2] : 0u), RLX_AGENT); } \
  if (blockIdx.x == 0 && wave == 0) { u64 a[12]; \
    for (unsigned sp = 0; sp < SPIN_CAP; ++sp) { bool ok = true; for (int i_ = 0; i_ < 12; ++i_) { a[i_] = __hip_atomic_load(pg + lane + 64 * i_, RLX_AGENT); ok &= (unsigned)(a[i_] >> 32) == tag; } if (__all(ok)) break; } \
    int mn[3], mx[3], sm[3], cn[3]; \
    for (int q_ = 0; q_ < 3; ++q_) { mn[q_] = 0x7fffffff; mx[q_] = -0x7fffffff; sm[q_] = 0; cn[q_] = 0; \
      for (int i_ = 0; i_ < 4; ++i_) { const unsigned v_ = (unsigned)a[q_ * 4 + i_]; if (v_) { const int d_ = (int)(v_ - stp[0]); mn[q_] = min(mn[q_], d_); mx[q_] = max(mx[q_], d_); sm[q_] += d_; cn[q_]++; } } \
      for (int o_ = 1; o_ < 64; o_ <<= 1) { mn[q_] = min(mn[q_], __shfl_xor(mn[q_], o_)); mx[q_] = max(mx[q_], __shfl_xor(mx[q_], o_)); sm[q_] += __shfl_xor(sm[q_], o_); cn[q_] += __shfl_xor(cn[q_], o_); } } \
    const int X = PAGG(PM2, mn[2], mx[2], sm[2], cn[2]) - PAGG(PM1, mn[1], mx[1], sm[1], cn[1]); \
    const unsigned until = stp[0] + mn[0] + 6000 + PSC * X; \
    for (unsigned sp = 0; sp < 1000000; ++sp) { if ((int)((unsigned)__builtin_amdgcn_s_memrealtime() - until) >= 0) break; } asm volatile("s_branch 1f\n\t.p2align 12\n1:"); } } while (0)
#define STDECL() unsigned stp[12] = {0, 0, 0, 0, 0, 0, 0, 0, 0, 0, 0, 0}
#define STPAR , unsigned (&stp)[12]
#define STARG , stp

struct Lds {
  unsigned char wo[9][4096];
  float part[11][2][132];
  unsigned q[2][128];
  unsigned x[144];
  float fin[8][32];
  unsigned short o[32];
  unsigned qready, kissued, issued, done[2], landed;
};

__device__ __forceinline__ void lds_wait(unsigned* p, unsigned n) {
  for (unsigned spins = 0; __hip_atomic_load(p, RLX_WG) < n && spins < (SPIN_CAP << 3); ++spins) __builtin_amdgcn_s_sleep(1);
}
__device__ __forceinline__ void lds_inc(unsigned* p, int lane) { if (lane == 0) __hip_atomic_fetch_add(p, 1u, RLX_WG); }
#define POLL2(MID_) \
    "1:\n\tbuffer_load_dwordx4 v[100:103], %[vo], %[rs], %[so] offen sc1\n\tbuffer_load_dwordx4 v[104:107], %[vo], %[rs], %[so] offen offset:1024 sc1\n\t" \
    "s_sub_u32 %[n], %[n], 1\n\ts_waitcnt vmcnt(1)\n\t" \
    "v_bitop3_b32 v108, v100, v101, v102 bitop3:0x96\n\tv_bitop3_b32 v108, v108, v103, %[tag] bitop3:0x96\n\t" MID_ "s_waitcnt vmcnt(0)\n\t" \
    "v_bitop3_b32 v109, v104, v105, v106 bitop3:0x96\n\tv_bitop3_b32 v109, v109, v107, %[tag] bitop3:0x96\n\t" \
    "v_or_b32 v108, v108, v109\n\tv_cmp_ne_u32 vcc, 0, v108\n\ts_cbranch_vccz 2f\n\ts_cbranch_scc0 1b\n2:\n\t"
#define POLLCLOB "memory", "vcc", "scc", "v100", "v101", "v102", "v103", "v104", "v105", "v106", "v107", "v108", "v109", "v110", "v111", "v112", "v113", "v114", "v115", "v116", "v117"

__global__ void __launch_bounds__(NTHR, 3) fused_decode(const void* kA, const void* vA, const void* wqA, const void* xA, const void* kB, const void* vB, const void* wqB, const void* xB,
                                                         const void* woA, const void* woB, void* out, void* wsp) {
  __shared__ __attribute__((aligned(16))) Lds L;
  STDECL();
  ST(0);
  const auto karg = __builtin_amdgcn_kernarg_segment_ptr();
  if (KWARM) asm volatile("s_load_dword s94, %0, 0x0\n\ts_load_dword s95, %0, 0x40" :: "s"(karg) : "s94", "s95");
  SB();
  u64x2 a2; u64x4 a4;
#define GETARGS(OFF, ...) asm volatile("s_load_dwordx4 %0, %[ka], %[off]\n\ts_load_dwordx8 %1, %[ka], 0x40\n\ts_waitcnt lgkmcnt(0)" : "=&s"(a2), "=&s"(a4) : [ka] "s"(karg), [off] "s"(OFF), __VA_ARGS__ : "memory")
  const int tid = threadIdx.x, lane = tid & 63, wave = __builtin_amdgcn_readfirstlane(tid >> 6);
  const int w = blockIdx.x, h = (w & 7) * 2 + (w >> 7), s = (w >> 3) & 15;
  const int hp = (h + HSH) & 15, pbr = s >> 3;
  unsigned tag;
  const unsigned tstart = (unsigned)__builtin_amdgcn_s_memrealtime();

  if (__builtin_expect(wave == 0, 1)) {
    const int rs = lane >> 3, c = lane & 7;
    const int qbr = (w >> 1) & 1, half = (w >> 2) & 1, j = w >> 3, blk = (j >> 1) * 4 + (j & 1) * 2 + ((j ^ w) & 1);
    unsigned woff = (unsigned)((half * 288 + rs * 36) * NQ + blk * 64 + c * 8) * 2;
    asm volatile("" : "+v"(woff));
    GETARGS(qbr * 32 + 16, "s"(half));
    ST(4);
    const rsrc_t wr = mkrs((const void*)a2[0], DM * NQ * 2);
    const rsrc_t wsr = mkrs((const void*)a4[3], WS_END);
    if (TLBP & 8) asm volatile("s_load_dword s94, %0, %1\n\ts_load_dword s95, %0, %2" :: "s"(a2[0]), "s"((half * 288 + 287) * NQ * 2), "s"(half * 288 * NQ * 2) : "s94", "s95");
    SB();
    u32x4 wv[36], xr;
    unsigned epoch, so = 0;
#pragma unroll
    for (int t = 0; t < 36; ++t) {
      wv[t] = ldb<NT>(wr, woff, so); SB();
      so += NQ * 2; asm volatile("" : "+s"(so)); SB();
      if ((TLBP & 1) && t == 0) asm volatile("s_load_dword s94, %0, 0x0" :: "s"(a2[1]) : "s94");
      if (t == XPOS) {
        xr = ldb<0>(mkrs((const char*)a2[1] + half * 576, 576), lane * 16, 0);
        epoch = __hip_atomic_load((GAS unsigned*)(a4[3] + WS_EPOCH), RLX_AGENT); SB();
      }
    }
    ST(5);
    __builtin_amdgcn_s_barrier();
    SB();
    ST(8);
    asm volatile("" : "+v"(wv[0]));
    ST(3);
    if (lane < 36) *(u32x4*)&L.x[lane * 4] = xr;
    float acc[8] = {0, 0, 0, 0, 0, 0, 0, 0};
#pragma unroll
    for (int t4 = 0; t4 < 9; ++t4) {
      const u32x2 xw = *(const u32x2*)&L.x[rs * 18 + t4 * 2];
#pragma unroll
      for (int u = 0; u < 4; ++u) {
        const int t = t4 * 4 + u;
        const unsigned xl = (u & 1) ? xw[u >> 1] >> 16 : xw[u >> 1] & 0xffffu, xh = (u & 1) ? xw[u >> 1] & 0xffff0000u : xw[u >> 1] << 16;
#pragma unroll
        for (int k = 0; k < 4; ++k) { acc[2 * k] = dot2(wv[t][k], xl, acc[2 * k]); acc[2 * k + 1] = dot2(wv[t][k], xh, acc[2 * k + 1]); }
      }
      SB();
    }
    float b[4];
#pragma unroll
    for (int k = 0; k < 4; ++k) { b[k] = fold32(acc[k], acc[k + 4]); b[k] = fold16(b[k], b[k]); b[k] += dpp<XOR8>(b[k]); }
    asm volatile("" : "+v"(epoch));
    tag = __builtin_amdgcn_readfirstlane(epoch) + 1u;
    tag += tag == 0u;
    stx(wsr, (lane & 24) ? OOR : (c * 2 + (lane >> 5)) * 16, WS_Q + (blk >> 2) * 4096 + (qbr * 2 + half) * 1024 + (blk & 3) * 256, seal4(b[0], b[1], b[2], b[3], tag));
    unsigned opf = 0;
    if (OPF) opf = __builtin_amdgcn_raw_buffer_load_b32(mkrs((const void*)a4[2], w < NRED ? 4 : 0), lane * 4, 0, SC1);
    if (TLBP & 4) asm volatile("s_load_dword s94, %0, %2\n\ts_load_dword s95, %1, 0x0" :: "s"(pbr ? a4[1] : a4[0]), "s"(a4[2]), "s"((hp * 256 + (s & 4) * 32) * DM * 2) : "s94", "s95");
    ST(1);
    __builtin_amdgcn_s_sleep(QSLEEP);
    u32x4 g[4];
    for (unsigned spins = 0; spins < SPIN_CAP; ++spins) {
#pragma unroll
      for (int i = 0; i < 4; ++i) g[i] = ldx(wsr, lane * 16 + i * 1024, WS_Q + h * 4096);
      if (!__any((bad(g[0], tag) | bad(g[1], tag) | bad(g[2], tag) | bad(g[3], tag)) != 0u)) break;
    }
#pragma unroll
    for (int bb = 0; bb < 2; ++bb) {
      const u32x4 p0 = g[2 * bb], p1 = g[2 * bb + 1];
      *(u32x2*)&L.q[bb][2 * lane] = u32x2{cvtpk(uf(p0[0]) + uf(p1[0]), uf(p0[1]) + uf(p1[1])), cvtpk(uf(p0[2]) + uf(p1[2]), fourth(p0) + fourth(p1))};
    }
    __builtin_amdgcn_fence(__ATOMIC_RELEASE, "workgroup");
    if (lane == 0) __hip_atomic_store(&L.qready, tag, RLX_WG);
    asm volatile("" :: "v"(opf));
    ST(2);
    if (PGAL) asm volatile("s_branch 1f\n\t.p2align 12\n1:");
  } else {
    const int br = wave == 1 || wave == 3 ? 0 : 1;
    const int pair = wave < 5 ? 3 : (wave - 5) >> 1, odd = wave < 5 ? (wave - 1) >> 1 : (wave - 5) & 1;
    const int g = lane >> 4, i = lane & 15;
    const unsigned sbase = (h * 2048 + pair * 512 + s * 32 + odd * 2) * 1024;
    unsigned loff = sbase + lane * 16;
    const bool late = wave < 4, closer = wave < 3;
    unsigned so = ((s + HR * h) & 7) * 4096, knead = KAFIRST && br ? 2u : 0u;
    if (tid == 64) { L.qready = 0u; L.kissued = 0u; L.issued = 0u; L.done[0] = 0u; L.done[1] = 0u; L.landed = 0u; }
    asm volatile("" : "+v"(loff));
    asm volatile("" : "+s"(so), "+s"(knead));
    GETARGS(br * 32, "s"(so));
    const rsrc_t kr = mkrs((const void*)a2[0], 32 << 20), vr = mkrs((const void*)a2[1], 32 << 20);
    const rsrc_t wsr = mkrs((const void*)a4[3], WS_END);
    if ((TLBP & 2) && (unsigned)(wave - 3) < 2u) asm volatile("s_load_dword s94, %0, %2\n\ts_load_dword s95, %1, %2" :: "s"(a2[0]), "s"(a2[1]), "s"(sbase) : "s94", "s95");
    if (wave != 3 && wave != 4) ST(10);
    asm volatile("" :: "s"(kr), "s"(vr));
    __builtin_amdgcn_s_barrier();
    SB();
    ST(8);
    u32x4 K[16], V[16];
#define LOAD16(X, R) _Pragma("unroll") for (int t = 0; t < 16; t += 2) { \
      X[t] = ldb<NT>(R, loff, so); SB(); unsigned nx = so + 4096u; asm volatile("" : "+s"(nx)); SB(); \
      X[t + 1] = ldb<NT>(R, loff + 1024, so); SB(); so = nx & 0x7000u; asm volatile("" : "+s"(so)); SB(); }
    if (knead) lds_wait(&L.kissued, knead);
    SB();
#pragma unroll
    for (int t = 0; t < 16; t += 2)
      asm volatile("buffer_load_dwordx4 %0, %3, %4, %2 offen nt\n\ts_nop 0\n\tbuffer_load_dwordx4 %1, %3, %4, %2 offen offset:1024 nt\n\t"
                   "s_addk_i32 %2, 0x1000\n\ts_and_b32 %2, %2, 0x7000" : "=&v"(K[t]), "=&v"(K[t + 1]), "+s"(so) : "v"(loff), "s"(kr) : "scc");
    SB();
    lds_inc(&L.kissued, lane);
    ST(9);
    if (LAND) { asm volatile("s_waitcnt vmcnt(%0)" :: "i"(LN) : "memory"); lds_inc(&L.landed, lane); }
    if (!late) {
      lds_wait(&L.kissued, 10u);
      for (unsigned spins = 0; (unsigned)__builtin_amdgcn_s_memrealtime() - tstart < VGATE && (!LAND || __hip_atomic_load(&L.landed, RLX_WG) < 10u) && spins < SPIN_CAP; ++spins) __builtin_amdgcn_s_sleep(1);
      SB();
      LOAD16(V, vr)
      lds_inc(&L.issued, lane);
      ST(1);
      asm volatile("s_waitcnt vmcnt(16)" ::: "memory");
    } else {
      asm volatile("s_waitcnt vmcnt(0)" ::: "memory");
    }
#pragma unroll
    for (int t = 0; t < 16; ++t) asm volatile("" : "+v"(K[t]));
    lds_wait(&L.qready, 1u);
    tag = __builtin_amdgcn_readfirstlane(__hip_atomic_load(&L.qready, RLX_WG));
    __builtin_amdgcn_fence(__ATOMIC_ACQUIRE, "workgroup");
    const u32x4 q0 = *(const u32x4*)&L.q[br][4 * i], q1 = *(const u32x4*)&L.q[br][64 + 4 * i];
    constexpr float C2 = RND ? 1.4426950408889634f : 0.088388347648318440f * 1.4426950408889634f;
    float sc[16], m = -INFINITY;
#pragma unroll
    for (int t = 0; t < 16; ++t) {
      float d0 = 0, d1 = 0;
#pragma unroll
      for (int k = 0; k < 4; ++k) { d0 = dot2(K[t][k], q0[k], d0); d1 = dot2(K[t][k], q1[k], d1); }
      d0 += dpp<XOR8>(d0); d1 += dpp<XOR8>(d1);
      float d = (i & 8) ? d1 : d0;
      d += dpp<XOR7>(d); d += dpp<XOR2>(d); d += dpp<XOR1>(d);
      sc[t] = d;
    }
#pragma unroll
    for (int t = 0; t < 16; t += 2) {
      if (RND) {
        const unsigned r1 = cvtpk(sc[t], sc[t + 1]), r2 = cvtpk(bflo(r1) * 0.088388347648318440f, bfhi(r1) * 0.088388347648318440f);
        sc[t] = bflo(r2); sc[t + 1] = bfhi(r2);
      }
      m = __builtin_fmaxf(m, __builtin_fmaxf(sc[t], sc[t + 1]));
    }
    m = max16_32(m) * C2;
    unsigned msk[4];
#pragma unroll
    for (int k = 0; k < 4; ++k) msk[k] = (((i & 7) >> 1) == k) ? ((i & 1) ? 0xffff0000u : 0x0000ffffu) : 0u;
    float l = 0;
    u32x4 a[16];
#pragma unroll
    for (int t = 0; t < 16; ++t) {
      const float p = __builtin_amdgcn_exp2f(fmaf(sc[t], C2, -m));
      const unsigned pp = cvtpk(p, p);
      l += p;
      a[t] = u32x4{pp & msk[0], pp & msk[1], pp & msk[2], pp & msk[3]};
    }
    l = fold16(l, l); l = fold32(l, l);
    if ((lane & 55) == 0) *(float2*)&L.part[wave][lane >> 3][0] = make_float2(m, l);
#pragma unroll
    for (int t = 0; t < 16; ++t) asm volatile("" : "+v"(a[t]));
    SB();
    ST(2);
    if (late) {
      __builtin_amdgcn_fence(__ATOMIC_RELEASE, "workgroup");
      lds_wait(&L.issued, wave == 2 ? 7u : wave == 3 ? 8u : 9u);
      SB();
      LOAD16(V, vr)
      lds_inc(&L.issued, lane);
      ST(1);
    }
    const int hd = g >> 1, oi = 4 + 8 * i + 4 * (g & 1);
    float f = 0, eo = 0;
    unsigned lse = 0, poff = WS_O + h * 32768 + br * 16384 + s * 128 + (lane >> 3) * 2048 + (lane & 7) * 16;
    if (wave == 1) {
      __builtin_amdgcn_fence(__ATOMIC_ACQUIRE, "workgroup");
      const float2 me = *(const float2*)&L.part[1][hd][0], r = *(const float2*)&L.part[3][hd][0];
      const float M = fmax2(me.x, r.x);
      f = __builtin_amdgcn_exp2f(me.x - M); eo = __builtin_amdgcn_exp2f(r.x - M);
      const float Ls = fmaf(f, me.y, eo * r.y), inv = __builtin_amdgcn_rcpf(Ls);
      f *= inv; eo *= inv;
      lse = fu(M + __builtin_amdgcn_logf(Ls));
    }
    asm volatile("" : "+v"(f), "+v"(eo), "+v"(lse), "+v"(poff));
    SB();
    f32x4 acc = {0, 0, 0, 0};
#pragma unroll
    for (int t = 0; t < MID; ++t) acc = mfma(a[t], V[t], acc);
    asm volatile("" : "+v"(acc));
    SB();
    f32x4 base = {0, 0, 0, 0};
    if (closer) {
      lds_wait(&L.done[br], br ? 7u : 1u);
      __builtin_amdgcn_fence(__ATOMIC_ACQUIRE, "workgroup");
      if (br) {
        const float2 me = *(const float2*)&L.part[2][hd][0];
        float2 r[7]; f32x4 o[7];
#pragma unroll
        for (int u = 0; u < 7; ++u) { r[u] = *(const float2*)&L.part[4 + u][hd][0]; o[u] = *(const f32x4*)&L.part[4 + u][hd][oi]; }
        float M = me.x;
#pragma unroll
        for (int u = 0; u < 7; ++u) M = fmax2(M, r[u].x);
        f = __builtin_amdgcn_exp2f(me.x - M);
        float Ls = f * me.y;
#pragma unroll
        for (int u = 0; u < 7; ++u) { const float e = __builtin_amdgcn_exp2f(r[u].x - M); Ls = fmaf(e, r[u].y, Ls); base += e * o[u]; }
        eo = __builtin_amdgcn_rcpf(Ls);
        f *= eo;
        lse = fu(M + __builtin_amdgcn_logf(Ls));
      } else {
        base = *(const f32x4*)&L.part[3][hd][oi];
      }
      ST(4);
    }
    SB();
#pragma unroll
    for (int t = MID; t < 15; ++t) acc = mfma(a[t], V[t], acc);
    base *= eo;
    asm volatile("" : "+v"(base));
    acc = mfma(a[15], V[15], acc);
    if (closer) {
      acc = acc * f + base;
      stx(wsr, poff, 0, seal(cvtpk(acc[0], acc[1]), cvtpk(acc[2], acc[3]), lse, tag));
    } else {
      *(f32x4*)&L.part[wave][hd][oi] = acc;
      __builtin_amdgcn_fence(__ATOMIC_RELEASE, "workgroup");
      lds_inc(&L.done[br], lane);
    }
    ST(3);
  }
  const rsrc_t wsr = mkrs((const void*)a4[3], WS_END);
  GAS unsigned char* ws = (GAS unsigned char*)a4[3];
  const int gw = GW;
  const int pw = wave == 0 ? 8 : wave == PW0 ? 0 : wave < 4 ? -1 : wave - 3;
  if (pw >= 0) {
    u32x4 wf[4];
    unsigned yoff, oaddr = (lane >> 4) * 16;
    lds_wait(&L.issued, 10u);
    if (WOGATE) for (unsigned spins = 0; (unsigned)__builtin_amdgcn_s_memrealtime() - tstart < WOGATE && spins < SPIN_CAP; ++spins) __builtin_amdgcn_s_sleep(1);
    const int row0 = hp * 256 + ((s >> 2) & 1) * 128 + (s & 1) * 64 + (s & 2) * 2;
    const rsrc_t wr = mkrs((const uint16_t*)(pbr ? a4[1] : a4[0]) + row0 * DM + pw * 64, 60 * DM * 2);
    const unsigned woff = ((8 * (lane >> 5) + ((lane >> 3) & 3)) * DM + (lane & 7) * 8) * 2;
    LAS unsigned char* dst = (LAS unsigned char*)&L.wo[pw][0];
#pragma unroll
    for (int t = 0; t < 4; ++t) __builtin_amdgcn_raw_ptr_buffer_load_lds(wr, dst + t * 1024, 16, woff, t * 16 * DM * 2, 0, NT);
    yoff = lane < 16 ? WS_Y + (pbr * 18 + pw * 2 + (lane >> 3)) * 16384 + (hp * 8 + (s & 7)) * 128 + (lane & 7) * 16 : OOR;
    asm volatile("s_waitcnt vmcnt(0)" ::: "memory");
    LAS unsigned char* src = dst + (lane >> 4) * 1024 + ((lane >> 2) & 3) * 128 + (lane & 3) * 8;
#pragma unroll
    for (int b = 0; b < 4; ++b) {
      const u32x2 lo = __builtin_bit_cast(u32x2, __builtin_amdgcn_ds_read_tr16_b64_v4i16((LAS s16x4*)(src + b * 32)));
      const u32x2 hi = __builtin_bit_cast(u32x2, __builtin_amdgcn_ds_read_tr16_b64_v4i16((LAS s16x4*)(src + b * 32 + 512)));
      wf[b] = u32x4{lo[0], lo[1], hi[0], hi[1]};
    }
    unsigned k0 = 0x07060501u, k1 = 0x07060502u, k2 = 0x07060503u;
    asm volatile("" : "+s"(k0), "+s"(k1), "+s"(k2) : "s"(wsr));
    asm volatile("" : "+v"(wf[0]), "+v"(wf[1]), "+v"(wf[2]), "+v"(wf[3]), "+v"(yoff), "+v"(oaddr));
    ST(9);
    asm volatile("s_barrier" ::: "memory");
    ST(11);
    const u32x4 ov = *(const u32x4*)((const char*)L.o + oaddr);
    const f32x4 z = {0, 0, 0, 0};
    f32x4 y3 = mfma(ov, wf[3], z), y0 = mfma(ov, wf[0], z), y1 = mfma(ov, wf[1], z), y2 = mfma(ov, wf[2], z);
    SB();
    const unsigned p0 = __builtin_amdgcn_perm(fu(y0[0]), fu(y3[0]), k0), p1 = __builtin_amdgcn_perm(fu(y1[0]), fu(y3[0]), k1);
    const unsigned c01 = xor3(p0, p1, tag), p2 = __builtin_amdgcn_perm(fu(y2[0]), fu(y3[0]), k2);
    stx(wsr, yoff, 0, u32x4{p0, p1, p2, c01 ^ p2});
    asm volatile("" :: "v"(y0), "v"(y1), "v"(y2), "v"(y3));
  } else if (wave == gw) {
    const unsigned oa = (unsigned)(size_t)(LAS void*)&L.o[4 * (lane & 7) + (lane >> 4)];
    unsigned left = SPIN_CAP;
    __builtin_amdgcn_s_sleep(FIRST1);
    asm volatile(POLL2("")
        "v_max_f32 v112, v102, v106\n\tv_lshlrev_b32 v108, 16, v100\n\tv_and_b32 v109, 0xffff0000, v100\n\t"
        "v_max_f32_dpp v112, v112, v112 row_ror:8 row_mask:0xf bank_mask:0xf\n\tv_lshlrev_b32 v110, 16, v101\n\t"
        "v_mov_b32 v113, v112\n\tv_and_b32 v111, 0xffff0000, v101\n\tv_lshlrev_b32 v114, 16, v104\n\t"
        "v_permlane16_swap_b32 v112, v113\n\tv_max_f32 v112, v112, v113\n\t"
        "v_mov_b32 v113, v112\n\tv_and_b32 v115, 0xffff0000, v104\n\tv_lshlrev_b32 v116, 16, v105\n\t"
        "v_permlane32_swap_b32 v112, v113\n\tv_max_f32 v112, v112, v113\n\t"
        "v_sub_f32 v102, v102, v112\n\tv_sub_f32 v106, v106, v112\n\tv_exp_f32 v102, v102\n\tv_exp_f32 v106, v106\n\t"
        "v_and_b32 v117, 0xffff0000, v105\n\tv_add_f32 v112, v102, v106\n\t"
        "v_pk_mul_f32 v[108:109], v[108:109], v[102:103] op_sel_hi:[1,0]\n\tv_pk_mul_f32 v[110:111], v[110:111], v[102:103] op_sel_hi:[1,0]\n\t"
        "v_add_f32_dpp v112, v112, v112 row_ror:8 row_mask:0xf bank_mask:0xf\n\t"
        "v_pk_fma_f32 v[108:109], v[114:115], v[106:107], v[108:109] op_sel_hi:[1,0,1]\n\tv_mov_b32 v113, v112\n\t"
        "v_pk_fma_f32 v[110:111], v[116:117], v[106:107], v[110:111] op_sel_hi:[1,0,1]\n\ts_nop 0\n\t"
        "v_permlane16_swap_b32 v112, v113\n\tv_permlane32_swap_b32 v108, v110\n\tv_permlane32_swap_b32 v109, v111\n\t"
        "v_add_f32 v112, v112, v113\n\tv_add_f32 v108, v108, v110\n\tv_add_f32 v109, v109, v111\n\tv_mov_b32 v113, v112\n\ts_nop 0\n\t"
        "v_permlane16_swap_b32 v108, v109\n\tv_permlane32_swap_b32 v112, v113\n\t"
        "v_add_f32 v108, v108, v109\n\tv_add_f32 v112, v112, v113\n\tv_rcp_f32 v112, v112\n\t"
        "v_add_f32_dpp v108, v108, v108 row_ror:8 row_mask:0xf bank_mask:0xf\n\tv_mul_f32 v108, v108, v112\n\t"
        "v_cvt_pk_bf16_f32 v108, v108, v108\n\tds_write_b16 %[la], v108"
        : [n] "+s"(left) : [vo] "v"(lane * 16), [rs] "s"(wsr), [so] "s"(WS_O + (hp * 16 + s) * 2048), [tag] "s"(tag), [la] "v"(oa) : POLLCLOB);
    ST(5);
    asm volatile("s_waitcnt lgkmcnt(0)\n\ts_barrier" ::: "memory");
  } else {
    asm volatile("s_barrier" ::: "memory");
  }
  ST(6);
  const unsigned rd = w - ROFF;
  if (rd >= NRED) { STDUMP(); return; }
  if (wave < 8) {
    const unsigned wa = (unsigned)(size_t)(LAS void*)&L.fin[0][0] + ((lane >> 4) * 4 + ((lane & 7) >> 1)) * 64 + wave * 8 + (lane & 1) * 4;
    unsigned left = SPIN_CAP;
    __builtin_amdgcn_s_sleep(FIRST2);
    asm volatile(POLL2("v_perm_b32 v110, v101, v100, %[k0]\n\tv_perm_b32 v110, v102, v110, %[k1]\n\t")
        "v_perm_b32 v111, v105, v104, %[k0]\n\tv_pk_add_f32 v[100:101], v[100:101], v[104:105]\n\tv_add_f32 v102, v102, v106\n\t"
        "v_perm_b32 v111, v106, v111, %[k1]\n\tv_add_f32 v110, v110, v111\n\t"
        "v_permlane32_swap_b32 v100, v102\n\tv_add_f32 v100, v100, v102\n\t"
        "v_permlane32_swap_b32 v101, v110\n\tv_add_f32 v101, v101, v110\n\ts_nop 1\n\t"
        "v_permlane16_swap_b32 v100, v101\n\tv_add_f32 v100, v100, v101\n\ts_nop 1\n\t"
        "v_add_f32_dpp v100, v100, v100 row_ror:8 row_mask:0xf bank_mask:0xf\n\tds_write_b32 %[la], v100"
        : [n] "+s"(left) : [vo] "v"(lane * 16), [rs] "s"(wsr), [so] "s"(WS_Y + rd * 16384 + wave * 2048), [tag] "s"(tag), [la] "v"(wa),
          [k0] "s"(0x0c04000cu), [k1] "s"(0x04020100u) : POLLCLOB);
    asm volatile("s_waitcnt lgkmcnt(0)\n\ts_barrier" ::: "memory");
  } else if (wave == 8) {
    unsigned fa = (unsigned)(size_t)(LAS void*)&L.fin[0][0] + (lane & 15) * 64, ooff = lane < 16 ? ((rd >> 1) * 32 + (rd & 1) * 4 + (lane >> 2) * 8 + (lane & 3)) * 4 : OOR;
    const rsrc_t outr = mkrs((const void*)a4[2], 2 * DM * 2);
    asm volatile("" : "+v"(fa), "+v"(ooff) : "s"(outr));
    asm volatile("s_barrier" ::: "memory");
    const LAS f32x4* f = (const LAS f32x4*)(size_t)fa;
    const f32x4 t = (f[0] + f[1]) + (f[2] + f[3]);
    __builtin_amdgcn_raw_buffer_store_b32(cvtpk(t[0] + t[2], t[1] + t[3]), outr, ooff, 0, OUTSC);
    ST(7);
  } else {
    asm volatile("s_barrier" ::: "memory");
    if (rd == BUMP && tid == 576) __hip_atomic_store((GAS unsigned*)(ws + WS_EPOCH), tag, RLX_AGENT);
  }
  STDUMP();
}

extern "C" void kernel_launch(void* const* d_in, const int* in_sizes, int n_in, void* d_out, int out_size, void* d_ws, size_t ws_size, hipStream_t stream) {
  fused_decode<<<256, NTHR, 0, stream>>>(d_in[1], d_in[2], d_in[3], d_in[0], d_in[6], d_in[7], d_in[8], d_in[5], d_in[4], d_in[9], d_out, d_ws);
}
```

```cpp
#include <hip/hip_runtime.h>
#include <hip/hip_bf16.h>
#include <stdint.h>
#include <stddef.h>

#define GAS __attribute__((address_space(1)))
#define LAS __attribute__((address_space(3)))
#define SB() __builtin_amdgcn_sched_barrier(0)
typedef unsigned u32x4 __attribute__((ext_vector_type(4)));
typedef unsigned u32x2 __attribute__((ext_vector_type(2)));
typedef float f32x4 __attribute__((ext_vector_type(4)));
typedef short s16x4 __attribute__((ext_vector_type(4)));
typedef __bf16 bf16x2 __attribute__((ext_vector_type(2)));
typedef __bf16 bf16x8 __attribute__((ext_vector_type(8)));
typedef unsigned long long u64;
typedef u64 u64x2 __attribute__((ext_vector_type(2)));
typedef u64 u64x4 __attribute__((ext_vector_type(4)));
typedef __amdgpu_buffer_rsrc_t rsrc_t;

constexpr int DM = 576, NQ = 4096, NTHR = 11 * 64, NRED = 36;
constexpr unsigned WS_Q = 4096, WS_O = WS_Q + 16 * 4096, WS_Y = WS_O + 256 * 2048, WS_END = WS_Y + NRED * 16384;
constexpr unsigned SPIN_CAP = 1u << 17;
constexpr unsigned OOR = 0x40000000u;
constexpr int NT = 2, SC1 = 16;
constexpr unsigned VGATE = 675;

struct Lds {
  unsigned char wo[9][4096];
  float part[11][2][132];
  unsigned q[2][128];
  unsigned x[144];
  float fin[16][8][2];
  unsigned short o[32];
  unsigned tag, kissued, issued, landed, done[2], pad[2];
};

__device__ __forceinline__ float bflo(unsigned u) { return __builtin_bit_cast(float, u << 16); }
__device__ __forceinline__ float bfhi(unsigned u) { return __builtin_bit_cast(float, u & 0xffff0000u); }
__device__ __forceinline__ unsigned fu(float f) { return __float_as_uint(f); }
__device__ __forceinline__ float uf(unsigned u) { return __uint_as_float(u); }
__device__ __forceinline__ unsigned cvtpk(float lo, float hi) { bf16x2 v = {(__bf16)lo, (__bf16)hi}; return __builtin_bit_cast(unsigned, v); }
__device__ __forceinline__ float dot2(unsigned a, unsigned b, float acc) {
  return __builtin_amdgcn_fdot2_f32_bf16(__builtin_bit_cast(bf16x2, a), __builtin_bit_cast(bf16x2, b), acc, false);
}
template <int CTRL> __device__ __forceinline__ float dpp(float x) {
  return __builtin_bit_cast(float, __builtin_amdgcn_mov_dpp(__builtin_bit_cast(int, x), CTRL, 0xf, 0xf, true));
}
constexpr int XOR1 = 0xB1, XOR2 = 0x4E, XOR7 = 0x141, XOR8 = 0x128;
__device__ __forceinline__ f32x4 mfma(u32x4 a, u32x4 b, f32x4 c) {
  return __builtin_amdgcn_mfma_f32_16x16x32_bf16(__builtin_bit_cast(bf16x8, a), __builtin_bit_cast(bf16x8, b), c, 0, 0, 0);
}
__device__ __forceinline__ float fold32(float a, float b) {
  auto r = __builtin_amdgcn_permlane32_swap(fu(a), fu(b), false, false);
  return uf(r[0]) + uf(r[1]);
}
__device__ __forceinline__ float fold16(float a, float b) {
  auto r = __builtin_amdgcn_permlane16_swap(fu(a), fu(b), false, false);
  return uf(r[0]) + uf(r[1]);
}
__device__ __forceinline__ float max16_32(float x) {
  auto s = __builtin_amdgcn_permlane16_swap(fu(x), fu(x), false, false);
  x = fmaxf(uf(s[0]), uf(s[1]));
  auto t = __builtin_amdgcn_permlane32_swap(fu(x), fu(x), false, false);
  return fmaxf(uf(t[0]), uf(t[1]));
}
__device__ __forceinline__ rsrc_t mkrs(u64 p, unsigned bytes) { return __builtin_amdgcn_make_buffer_rsrc((void*)p, 0, bytes, 0x00020000); }
template <int AUX> __device__ __forceinline__ u32x4 ldb(rsrc_t r, unsigned voff, unsigned soff) { return __builtin_amdgcn_raw_buffer_load_b128(r, voff, soff, AUX); }
__device__ __forceinline__ void publish(rsrc_t r, unsigned voff, unsigned soff, u32x4 v) { __builtin_amdgcn_raw_buffer_store_b128(v, r, voff, soff, SC1); }
__device__ __forceinline__ unsigned xor3(unsigned a, unsigned b, unsigned c) { return __builtin_amdgcn_bitop3_b32(a, b, c, 0x96); }
__device__ __forceinline__ u32x4 seal(unsigned a, unsigned b, unsigned c, unsigned tag) { return u32x4{a, b, c, xor3(a, b, c ^ tag)}; }
__device__ __forceinline__ unsigned bad(u32x4 g, unsigned tag) { return xor3(xor3(g[0], g[1], g[2]), g[3], tag); }
constexpr unsigned PK0 = 0x07060501u, PK1 = 0x07060502u, PK2 = 0x07060503u, UP0 = 0x0c04000cu, UP1 = 0x04020100u;
__device__ __forceinline__ float fourth(u32x4 g) { return uf(__builtin_amdgcn_perm(g[2], __builtin_amdgcn_perm(g[1], g[0], UP0), UP1)); }

template <int OFF> __device__ __forceinline__ void lds_inc(unsigned base) {
  asm volatile("s_mov_b64 exec, 1\n\tds_add_u32 %0, %1 offset:%2\n\ts_mov_b64 exec, -1" :: "v"(base), "v"(1u), "i"(OFF) : "memory");
}
template <int OFF> __device__ __forceinline__ unsigned lds_wait(unsigned base, unsigned n) {
  unsigned v, left = SPIN_CAP << 3;
  asm volatile("1:\n\tds_read_b32 %0, %2 offset:%4\n\ts_sub_u32 %1, %1, 1\n\ts_waitcnt lgkmcnt(0)\n\tv_cmp_gt_u32 vcc, %3, %0\n\t"
               "s_cbranch_vccz 2f\n\ts_sleep 1\n\ts_cbranch_scc0 1b\n2:" : "=&v"(v), "+s"(left) : "v"(base), "s"(n), "i"(OFF) : "memory", "vcc", "scc");
  return v;
}
#define POLL2(EARLY) \
    "1:\n\tbuffer_load_dwordx4 v[100:103], %[vo], %[rs], %[so] offen sc1\n\tbuffer_load_dwordx4 v[104:107], %[vo], %[rs], %[so] offen offset:1024 sc1\n\t" \
    "s_sub_u32 %[n], %[n], 1\n\ts_waitcnt vmcnt(1)\n\t" \
    "v_bitop3_b32 v108, v100, v101, v102 bitop3:0x96\n\tv_bitop3_b32 v108, v108, v103, %[tag] bitop3:0x96\n\t" EARLY "s_waitcnt vmcnt(0)\n\t" \
    "v_bitop3_b32 v109, v104, v105, v106 bitop3:0x96\n\tv_bitop3_b32 v109, v109, v107, %[tag] bitop3:0x96\n\t" \
    "v_or_b32 v108, v108, v109\n\tv_cmp_ne_u32 vcc, 0, v108\n\ts_cbranch_vccz 2f\n\ts_cbranch_scc0 1b\n2:\n\t"
#define POLLCLOB "memory", "vcc", "scc", "v100", "v101", "v102", "v103", "v104", "v105", "v106", "v107", "v108", "v109", "v110", "v111", "v112", "v113", "v114", "v115", "v116", "v117"

#define GETARGS(OFF) asm volatile("s_load_dwordx4 %0, %2, %3\n\ts_load_dwordx8 %1, %2, 0x40\n\ts_waitcnt lgkmcnt(0)" : "=&s"(a2), "=&s"(a4) : "s"(karg), "s"(OFF) : "memory")
__global__ void __launch_bounds__(NTHR, 3) fused_decode(const void* kA, const void* vA, const void* wqA, const void* xA, const void* kB, const void* vB, const void* wqB,
                                                         const void* xB, const void* woA, const void* woB, void* out, void* ws) {
  __shared__ __attribute__((aligned(16))) Lds L;
  const auto karg = __builtin_amdgcn_kernarg_segment_ptr();
  asm volatile("s_load_dword s94, %0, 0x0\n\ts_load_dword s95, %0, 0x40" :: "s"(karg) : "s94", "s95");
  SB();
  const int tid = threadIdx.x, lane = tid & 63, wave = __builtin_amdgcn_readfirstlane(tid >> 6);
  const int w = blockIdx.x, h = (w & 7) * 2 + (w >> 7), s = (w >> 3) & 15;
  const unsigned lb = (unsigned)(size_t)(LAS void*)&L;
  u64x2 a2; u64x4 a4;
  unsigned tag;

  if (__builtin_expect(wave == 0, 1)) {
    const int rs = lane >> 3, c = lane & 7;
    const int qbr = (w >> 1) & 1, half = (w >> 2) & 1, j = w >> 3, blk = (j >> 1) * 4 + (j & 1) * 2 + ((j ^ w) & 1);
    unsigned woff = (unsigned)((half * 288 + rs * 36) * NQ + blk * 64 + c * 8) * 2;
    asm volatile("" : "+v"(woff));
    GETARGS(qbr * 32 + 16);
    const rsrc_t wr = mkrs(a2[0], DM * NQ * 2), wsr = mkrs(a4[3], WS_END);
    u32x4 wv[36], xr;
    unsigned epoch, so = 0;
#pragma unroll
    for (int t = 0; t < 36; ++t) {
      wv[t] = ldb<NT>(wr, woff, so); SB();
      so += NQ * 2; asm volatile("" : "+s"(so)); SB();
      if (t == 0) asm volatile("s_load_dword s94, %0, 0x0" :: "s"(a2[1]) : "s94");
      if (t == 20) {
        xr = ldb<0>(mkrs(a2[1] + half * 576, 576), lane * 16, 0);
        epoch = __hip_atomic_load((GAS unsigned*)a4[3], __ATOMIC_RELAXED, __HIP_MEMORY_SCOPE_AGENT); SB();
      }
    }
    __builtin_amdgcn_s_barrier();
    SB();
    if (lane < 36) *(u32x4*)&L.x[lane * 4] = xr;
    float acc[8] = {0, 0, 0, 0, 0, 0, 0, 0};
#pragma unroll
    for (int t4 = 0; t4 < 9; ++t4) {
      const u32x2 xw = *(const u32x2*)&L.x[rs * 18 + t4 * 2];
#pragma unroll
      for (int u = 0; u < 4; ++u) {
        const unsigned xl = (u & 1) ? xw[u >> 1] >> 16 : xw[u >> 1] & 0xffffu, xh = (u & 1) ? xw[u >> 1] & 0xffff0000u : xw[u >> 1] << 16;
#pragma unroll
        for (int k = 0; k < 4; ++k) { acc[2 * k] = dot2(wv[t4 * 4 + u][k], xl, acc[2 * k]); acc[2 * k + 1] = dot2(wv[t4 * 4 + u][k], xh, acc[2 * k + 1]); }
      }
      SB();
    }
    float b[4];
#pragma unroll
    for (int k = 0; k < 4; ++k) { b[k] = fold32(acc[k], acc[k + 4]); b[k] = fold16(b[k], b[k]); b[k] += dpp<XOR8>(b[k]); }
    asm volatile("" : "+v"(epoch));
    tag = __builtin_amdgcn_readfirstlane(epoch) + 1u;
    tag += tag == 0u;
    publish(wsr, (lane & 24) ? OOR : (c * 2 + (lane >> 5)) * 16, WS_Q + (blk >> 2) * 4096 + (qbr * 2 + half) * 1024 + (blk & 3) * 256,
            seal(__builtin_amdgcn_perm(fu(b[0]), fu(b[3]), PK0), __builtin_amdgcn_perm(fu(b[1]), fu(b[3]), PK1), __builtin_amdgcn_perm(fu(b[2]), fu(b[3]), PK2), tag));
    __builtin_amdgcn_s_sleep(64);
    u32x4 g[4];
    for (unsigned spins = 0; spins < SPIN_CAP; ++spins) {
      asm volatile("" ::: "memory");
#pragma unroll
      for (int i = 0; i < 4; ++i) g[i] = ldb<SC1>(wsr, lane * 16 + i * 1024, WS_Q + h * 4096);
      if (!__any((bad(g[0], tag) | bad(g[1], tag) | bad(g[2], tag) | bad(g[3], tag)) != 0u)) break;
    }
#pragma unroll
    for (int br = 0; br < 2; ++br) {
      const u32x4 p0 = g[2 * br], p1 = g[2 * br + 1];
      *(u32x2*)&L.q[br][2 * lane] = u32x2{cvtpk(uf(p0[0]) + uf(p1[0]), uf(p0[1]) + uf(p1[1])), cvtpk(uf(p0[2]) + uf(p1[2]), fourth(p0) + fourth(p1))};
    }
    asm volatile("ds_write_b32 %0, %1 offset:%2" :: "v"(lb), "v"(tag), "i"(offsetof(Lds, tag)) : "memory");
    asm volatile("s_branch 1f\n\t.p2align 12\n1:");
  } else {
    const unsigned tstart = (unsigned)__builtin_amdgcn_s_memrealtime();
    const int br = wave == 1 || wave == 3 ? 0 : 1;
    const int quarter = wave < 5 ? 3 : (wave - 5) >> 1, odd = wave < 5 ? (wave - 1) >> 1 : (wave - 5) & 1;
    const int g = lane >> 4, i = lane & 15;
    const unsigned sbase = (h * 2048 + quarter * 512 + s * 32 + odd * 2) * 1024;
    unsigned loff = sbase + lane * 16, so = ((s + 3 * h) & 7) * 4096, kneed = 2 * br;
    const bool late = wave < 4, closer = wave < 3;
    if (tid == 64) { *(u32x4*)&L.tag = u32x4{0u, 0u, 0u, 0u}; *(u32x2*)&L.done[0] = u32x2{0u, 0u}; }
    asm volatile("" : "+v"(loff));
    asm volatile("" : "+s"(so), "+s"(kneed));
    GETARGS(br * 32);
    if ((unsigned)(wave - 3) < 2u) asm volatile("s_load_dword s94, %0, %2\n\ts_load_dword s95, %1, %2" :: "s"(a2[0]), "s"(a2[1]), "s"(sbase) : "s94", "s95");
    const rsrc_t kr = mkrs(a2[0], 32 << 20), vr = mkrs(a2[1], 32 << 20), wsr = mkrs(a4[3], WS_END);
    asm volatile("" :: "s"(kr), "s"(vr));
    __builtin_amdgcn_s_barrier();
    SB();
    if (kneed) lds_wait<offsetof(Lds, kissued)>(lb, kneed);
    u32x4 K[16], V[16];
#define KL2(A, B) "buffer_load_dwordx4 " A ", %[vo], %[rs], %[so] offen nt\n\ts_add_u32 %[nx], %[so], %[st]\n\t" \
                  "buffer_load_dwordx4 " B ", %[vo], %[rs], %[so] offen offset:1024 nt\n\ts_and_b32 %[so], %[nx], %[mk]\n\t"
    unsigned nx;
    asm volatile(KL2("%0", "%1") KL2("%2", "%3") KL2("%4", "%5") KL2("%6", "%7") KL2("%8", "%9") KL2("%10", "%11") KL2("%12", "%13") KL2("%14", "%15")
                 "s_mov_b64 exec, 1\n\tds_add_u32 %[lb], %[one] offset:%[ko]\n\ts_mov_b64 exec, -1\n\ts_waitcnt vmcnt(0)"
                 : "=&v"(K[0]), "=&v"(K[1]), "=&v"(K[2]), "=&v"(K[3]), "=&v"(K[4]), "=&v"(K[5]), "=&v"(K[6]), "=&v"(K[7]), "=&v"(K[8]), "=&v"(K[9]), "=&v"(K[10]),
                   "=&v"(K[11]), "=&v"(K[12]), "=&v"(K[13]), "=&v"(K[14]), "=&v"(K[15]), [so] "+s"(so), [nx] "=&s"(nx)
                 : [vo] "v"(loff), [rs] "s"(kr), [st] "s"(0x1000), [mk] "s"(0x7000), [lb] "v"(lb), [one] "v"(1u), [ko] "i"(offsetof(Lds, kissued)) : "memory", "scc");
    if (wave > 7) __builtin_amdgcn_s_sleep(6);
    lds_inc<offsetof(Lds, landed)>(lb);
#define LOADV() _Pragma("unroll") for (int t = 0; t < 16; t += 2) { \
      V[t] = ldb<NT>(vr, loff, so); SB(); unsigned n2 = so + 4096u; asm volatile("" : "+s"(n2)); SB(); \
      V[t + 1] = ldb<NT>(vr, loff + 1024, so); SB(); so = n2 & 0x7000u; asm volatile("" : "+s"(so)); SB(); } \
      lds_inc<offsetof(Lds, issued)>(lb);
    if (!late) {
      lds_wait<offsetof(Lds, kissued)>(lb, 10u);
      for (unsigned spins = 0; (unsigned)__builtin_amdgcn_s_memrealtime() - tstart < VGATE && spins < SPIN_CAP; ++spins)
        if (__builtin_amdgcn_readfirstlane(lds_wait<offsetof(Lds, landed)>(lb, 0u)) >= 10u) break;
      SB();
      LOADV()
    }
    tag = __builtin_amdgcn_readfirstlane(lds_wait<offsetof(Lds, tag)>(lb, 1u));
    const u32x4 qa = *(const u32x4*)&L.q[br][(i & 8) * 8 + 4 * i], qb = *(const u32x4*)&L.q[br][64 - (i & 8) * 8 + 4 * i];
    float sc[16], m = -INFINITY;
#pragma unroll
    for (int t = 0; t < 16; ++t) {
      float d = 0, e = 0;
#pragma unroll
      for (int k = 0; k < 4; ++k) { d = dot2(K[t][k], qa[k], d); e = dot2(K[t][k], qb[k], e); }
      d += dpp<XOR8>(e); d += dpp<XOR7>(d); d += dpp<XOR2>(d); d += dpp<XOR1>(d);
      asm("" : "+v"(d));
      sc[t] = d;
    }
    constexpr float SCALE = 0.088388347648318440f, LOG2E = 1.4426950408889634f;
#pragma unroll
    for (int t = 0; t < 16; t += 2) {
      const unsigned r1 = cvtpk(sc[t], sc[t + 1]), r2 = cvtpk(bflo(r1) * SCALE, bfhi(r1) * SCALE);
      sc[t] = bflo(r2); sc[t + 1] = bfhi(r2);
      m = fmaxf(m, fmaxf(sc[t], sc[t + 1]));
    }
    m = max16_32(m) * LOG2E;
    unsigned msk[4];
#pragma unroll
    for (int k = 0; k < 4; ++k) msk[k] = (((i & 7) >> 1) == k) ? ((i & 1) ? 0xffff0000u : 0x0000ffffu) : 0u;
    float l = 0;
    u32x4 a[16];
#pragma unroll
    for (int t = 0; t < 16; ++t) {
      const float p = __builtin_amdgcn_exp2f(fmaf(sc[t], LOG2E, -m));
      const unsigned pp = cvtpk(p, p);
      l += p;
      a[t] = u32x4{pp & msk[0], pp & msk[1], pp & msk[2], pp & msk[3]};
    }
    l = fold16(l, l); l = fold32(l, l);
    if ((lane & 55) == 0) *(float2*)&L.part[wave][lane >> 3][0] = make_float2(m, l);
#pragma unroll
    for (int t = 0; t < 16; ++t) asm volatile("" : "+v"(a[t]));
    SB();
    if (late) {
      lds_wait<offsetof(Lds, issued)>(lb, wave == 1 ? 9u : wave + 5u);
      SB();
      LOADV()
    }
    const int hd = g >> 1, oi = 4 + 8 * i + 4 * (g & 1);
    float f = 0, eo = 0;
    unsigned lse = 0, poff = WS_O + h * 32768 + br * 16384 + s * 128 + (lane >> 3) * 2048 + (lane & 7) * 16;
    if (wave == 1) {
      const float2 me = *(const float2*)&L.part[1][hd][0], r = *(const float2*)&L.part[3][hd][0];
      const float M = fmaxf(me.x, r.x);
      f = __builtin_amdgcn_exp2f(me.x - M); eo = __builtin_amdgcn_exp2f(r.x - M);
      const float Ls = fmaf(f, me.y, eo * r.y), inv = __builtin_amdgcn_rcpf(Ls);
      f *= inv; eo *= inv;
      lse = fu(M + __builtin_amdgcn_logf(Ls));
    }
    asm volatile("" : "+v"(f), "+v"(eo), "+v"(lse), "+v"(poff));
    SB();
    f32x4 acc = {0, 0, 0, 0}, base = {0, 0, 0, 0};
#pragma unroll
    for (int t = 0; t < 10; ++t) acc = mfma(a[t], V[t], acc);
    asm volatile("" : "+v"(acc));
    SB();
    if (wave == 1) {
      lds_wait<offsetof(Lds, done)>(lb, 1u);
      base = *(const f32x4*)&L.part[3][hd][oi];
    } else if (wave == 2) {
      lds_wait<offsetof(Lds, done) + 4>(lb, 7u);
      const float2 me = *(const float2*)&L.part[2][hd][0];
      float2 r[7]; f32x4 o[7];
#pragma unroll
      for (int u = 0; u < 7; ++u) { r[u] = *(const float2*)&L.part[4 + u][hd][0]; o[u] = *(const f32x4*)&L.part[4 + u][hd][oi]; }
      float M = me.x;
#pragma unroll
      for (int u = 0; u < 7; ++u) M = fmaxf(M, r[u].x);
      f = __builtin_amdgcn_exp2f(me.x - M);
      float Ls = f * me.y;
#pragma unroll
      for (int u = 0; u < 7; ++u) { const float e = __builtin_amdgcn_exp2f(r[u].x - M); Ls = fmaf(e, r[u].y, Ls); base += e * o[u]; }
      eo = __builtin_amdgcn_rcpf(Ls);
      f *= eo;
      lse = fu(M + __builtin_amdgcn_logf(Ls));
    }
    SB();
#pragma unroll
    for (int t = 10; t < 15; ++t) acc = mfma(a[t], V[t], acc);
    base *= eo;
    asm volatile("" : "+v"(base) : "s"(wsr));
    acc = mfma(a[15], V[15], acc);
    if (closer) {
      acc = acc * f + base;
      publish(wsr, poff, 0, seal(cvtpk(acc[0], acc[1]), cvtpk(acc[2], acc[3]), lse, tag));
    } else {
      *(f32x4*)&L.part[wave][hd][oi] = acc;
      lds_inc<offsetof(Lds, done)>(lb + 4 * br);
    }
  }

  const rsrc_t wsr = mkrs(a4[3], WS_END);
  const int hp = (h + 4) & 15, pbr = s >> 3;
  if (wave == 3) {
    unsigned left = SPIN_CAP;
    __builtin_amdgcn_s_sleep(12);
    asm volatile(POLL2("")
        "v_max_f32 v112, v102, v106\n\tv_lshlrev_b32 v108, 16, v100\n\tv_and_b32 v109, 0xffff0000, v100\n\t"
        "v_max_f32_dpp v112, v112, v112 row_ror:8 row_mask:0xf bank_mask:0xf\n\tv_lshlrev_b32 v110, 16, v101\n\t"
        "v_mov_b32 v113, v112\n\tv_and_b32 v111, 0xffff0000, v101\n\tv_lshlrev_b32 v114, 16, v104\n\t"
        "v_permlane16_swap_b32 v112, v113\n\tv_max_f32 v112, v112, v113\n\t"
        "v_mov_b32 v113, v112\n\tv_and_b32 v115, 0xffff0000, v104\n\tv_lshlrev_b32 v116, 16, v105\n\t"
        "v_permlane32_swap_b32 v112, v113\n\tv_max_f32 v112, v112, v113\n\t"
        "v_sub_f32 v102, v102, v112\n\tv_sub_f32 v106, v106, v112\n\tv_exp_f32 v102, v102\n\tv_exp_f32 v106, v106\n\t"
        "v_and_b32 v117, 0xffff0000, v105\n\tv_add_f32 v112, v102, v106\n\t"
        "v_pk_mul_f32 v[108:109], v[108:109], v[102:103] op_sel_hi:[1,0]\n\tv_pk_mul_f32 v[110:111], v[110:111], v[102:103] op_sel_hi:[1,0]\n\t"
        "v_add_f32_dpp v112, v112, v112 row_ror:8 row_mask:0xf bank_mask:0xf\n\t"
        "v_pk_fma_f32 v[108:109], v[114:115], v[106:107], v[108:109] op_sel_hi:[1,0,1]\n\tv_mov_b32 v113, v112\n\t"
        "v_pk_fma_f32 v[110:111], v[116:117], v[106:107], v[110:111] op_sel_hi:[1,0,1]\n\ts_nop 0\n\t"
        "v_permlane16_swap_b32 v112, v113\n\tv_permlane32_swap_b32 v108, v110\n\tv_permlane32_swap_b32 v109, v111\n\t"
        "v_add_f32 v112, v112, v113\n\tv_add_f32 v108, v108, v110\n\tv_add_f32 v109, v109, v111\n\tv_mov_b32 v113, v112\n\ts_nop 0\n\t"
        "v_permlane16_swap_b32 v108, v109\n\tv_permlane32_swap_b32 v112, v113\n\t"
        "v_add_f32 v108, v108, v109\n\tv_add_f32 v112, v112, v113\n\tv_rcp_f32 v112, v112\n\t"
        "v_add_f32_dpp v108, v108, v108 row_ror:8 row_mask:0xf bank_mask:0xf\n\tv_mul_f32 v108, v108, v112\n\t"
        "v_cvt_pk_bf16_f32 v108, v108, v108\n\tds_write_b16 %[la], v108\n\ts_waitcnt lgkmcnt(0)\n\ts_barrier"
        : [n] "+s"(left) : [vo] "v"(lane * 16), [rs] "s"(wsr), [so] "s"(WS_O + (hp * 16 + s) * 2048), [tag] "s"(tag),
          [la] "v"(lb + (unsigned)offsetof(Lds, o) + 8 * (lane & 7) + 2 * (lane >> 4)) : POLLCLOB);
  } else if (wave == 2) {
    asm volatile("s_barrier" ::: "memory");
  } else {
    const int pw = wave < 2 ? wave : wave - 2;
    const int row0 = hp * 256 + ((s >> 2) & 1) * 128 + (s & 1) * 64 + (s & 2) * 2;
    const rsrc_t wr = mkrs((pbr ? a4[1] : a4[0]) + (row0 * DM + pw * 64) * 2, 60 * DM * 2);
    const unsigned woff = ((8 * (lane >> 5) + ((lane >> 3) & 3)) * DM + (lane & 7) * 8) * 2;
    LAS unsigned char* dst = (LAS unsigned char*)&L.wo[pw][0];
    lds_wait<offsetof(Lds, issued)>(lb, 10u);
#pragma unroll
    for (int t = 0; t < 4; ++t) __builtin_amdgcn_raw_ptr_buffer_load_lds(wr, dst + t * 1024, 16, woff, t * 16 * DM * 2, 0, NT);
    unsigned yoff = lane < 16 ? WS_Y + (pbr * 18 + pw * 2 + (lane >> 3)) * 16384 + (hp * 8 + (s & 7)) * 128 + (lane & 7) * 16 : OOR;
    unsigned oa = (lane >> 4) * 16, k0 = PK0, k1 = PK1, k2 = PK2;
    asm volatile("s_waitcnt vmcnt(0)" ::: "memory");
    LAS unsigned char* src = dst + (lane >> 4) * 1024 + ((lane >> 2) & 3) * 128 + (lane & 3) * 8;
    u32x4 wf[4];
#pragma unroll
    for (int b = 0; b < 4; ++b) {
      const u32x2 lo = __builtin_bit_cast(u32x2, __builtin_amdgcn_ds_read_tr16_b64_v4i16((LAS s16x4*)(src + b * 32)));
      const u32x2 hi = __builtin_bit_cast(u32x2, __builtin_amdgcn_ds_read_tr16_b64_v4i16((LAS s16x4*)(src + b * 32 + 512)));
      wf[b] = u32x4{lo[0], lo[1], hi[0], hi[1]};
    }
    asm volatile("" : "+s"(k0), "+s"(k1), "+s"(k2) : "s"(wsr));
    asm volatile("" : "+v"(wf[0]), "+v"(wf[1]), "+v"(wf[2]), "+v"(wf[3]), "+v"(yoff), "+v"(oa));
    asm volatile("s_barrier" ::: "memory");
    const u32x4 ov = *(const u32x4*)((const char*)L.o + oa);
    const f32x4 z = {0, 0, 0, 0};
    const f32x4 y3 = mfma(ov, wf[3], z), y0 = mfma(ov, wf[0], z), y1 = mfma(ov, wf[1], z), y2 = mfma(ov, wf[2], z);
    SB();
    const unsigned p0 = __builtin_amdgcn_perm(fu(y0[0]), fu(y3[0]), k0), p1 = __builtin_amdgcn_perm(fu(y1[0]), fu(y3[0]), k1);
    const unsigned c01 = xor3(p0, p1, tag), p2 = __builtin_amdgcn_perm(fu(y2[0]), fu(y3[0]), k2);
    publish(wsr, yoff, 0, u32x4{p0, p1, p2, c01 ^ p2});
    asm volatile("" :: "v"(y0), "v"(y1), "v"(y2), "v"(y3));
  }
  if (w >= NRED) return;

  if (wave < 8) {
    unsigned left = SPIN_CAP;
    __builtin_amdgcn_s_sleep(16);
    asm volatile(POLL2("v_perm_b32 v110, v101, v100, %[k0]\n\tv_perm_b32 v110, v102, v110, %[k1]\n\t")
        "v_perm_b32 v111, v105, v104, %[k0]\n\tv_pk_add_f32 v[100:101], v[100:101], v[104:105]\n\tv_add_f32 v102, v102, v106\n\t"
        "v_perm_b32 v111, v106, v111, %[k1]\n\tv_add_f32 v110, v110, v111\n\t"
        "v_permlane32_swap_b32 v100, v102\n\tv_add_f32 v100, v100, v102\n\t"
        "v_permlane32_swap_b32 v101, v110\n\tv_add_f32 v101, v101, v110\n\ts_nop 1\n\t"
        "v_permlane16_swap_b32 v100, v101\n\tv_add_f32 v100, v100, v101\n\ts_nop 1\n\t"
        "v_add_f32_dpp v100, v100, v100 row_ror:8 row_mask:0xf bank_mask:0xf\n\tds_write_b32 %[la], v100\n\ts_waitcnt lgkmcnt(0)\n\ts_barrier"
        : [n] "+s"(left) : [vo] "v"(lane * 16), [rs] "s"(wsr), [so] "s"(WS_Y + w * 16384 + wave * 2048), [tag] "s"(tag), [k0] "s"(UP0), [k1] "s"(UP1),
          [la] "v"(lb + (unsigned)offsetof(Lds, fin) + ((lane >> 4) * 4 + ((lane & 7) >> 1)) * 64 + wave * 8 + (lane & 1) * 4) : POLLCLOB);
  } else if (wave == 8) {
    unsigned fa = (lane & 15) * 64, ooff = lane < 16 ? ((w >> 1) * 32 + (w & 1) * 4 + (lane >> 2) * 8 + (lane & 3)) * 4 : OOR;
    const rsrc_t outr = mkrs(a4[2], 2 * DM * 2);
    asm volatile("" : "+v"(fa), "+v"(ooff) : "s"(outr));
    asm volatile("s_barrier" ::: "memory");
    const f32x4* f = (const f32x4*)((const char*)L.fin + fa);
    const f32x4 t = (f[0] + f[1]) + (f[2] + f[3]);
    __builtin_amdgcn_raw_buffer_store_b32(cvtpk(t[0] + t[2], t[1] + t[3]), outr, ooff, 0, SC1);
  } else {
    asm volatile("s_barrier" ::: "memory");
    if (w == NRED - 1 && tid == 9 * 64) __hip_atomic_store((GAS unsigned*)a4[3], tag, __ATOMIC_RELAXED, __HIP_MEMORY_SCOPE_AGENT);
  }
}

extern "C" void kernel_launch(void* const* d_in, const int* in_sizes, int n_in, void* d_out, int out_size, void* d_ws, size_t ws_size, hipStream_t stream) {
  fused_decode<<<256, NTHR, 0, stream>>>(d_in[1], d_in[2], d_in[3], d_in[0], d_in[6], d_in[7], d_in[8], d_in[5], d_in[4], d_in[9], d_out, d_ws);
}
```

```cpp
#include <hip/hip_runtime.h>
#include <hip/hip_bf16.h>
#include <stdint.h>
#include <stddef.h>

#define GAS __attribute__((address_space(1)))
#define LAS __attribute__((address_space(3)))
#define SB() __builtin_amdgcn_sched_barrier(0)
typedef unsigned u32x4 __attribute__((ext_vector_type(4)));
typedef unsigned u32x2 __attribute__((ext_vector_type(2)));
typedef float f32x4 __attribute__((ext_vector_type(4)));
typedef short s16x4 __attribute__((ext_vector_type(4)));
typedef __bf16 bf16x2 __attribute__((ext_vector_type(2)));
typedef __bf16 bf16x8 __attribute__((ext_vector_type(8)));
typedef unsigned long long u64;
typedef u64 u64x2 __attribute__((ext_vector_type(2)));
typedef u64 u64x4 __attribute__((ext_vector_type(4)));
typedef __amdgpu_buffer_rsrc_t rsrc_t;

constexpr int DM = 576, NQ = 4096, NTHR = 11 * 64, NRED = 36;
constexpr unsigned WS_Q = 4096, WS_O = WS_Q + 16 * 4096, WS_Y = WS_O + 256 * 2048, WS_END = WS_Y + NRED * 16384;
constexpr unsigned SPIN_CAP = 1u << 17;
constexpr unsigned OOR = 0x40000000u;
constexpr int NT = 2, SC1 = 16;
constexpr unsigned VGATE = 675;

#define NOB 0
#define OST 2
#define OPT 0
struct Lds {
  unsigned char wo[9][4096];
  float part[11][2][132];
  unsigned q[2][128];
  unsigned x[144];
  float fin[16][8][2];
  unsigned short o[32];
  unsigned tag, kissued, issued, landed, done[2], pad[2];
};

__device__ __forceinline__ float bflo(unsigned u) { return __builtin_bit_cast(float, u << 16); }
__device__ __forceinline__ float bfhi(unsigned u) { return __builtin_bit_cast(float, u & 0xffff0000u); }
__device__ __forceinline__ unsigned fu(float f) { return __float_as_uint(f); }
__device__ __forceinline__ float uf(unsigned u) { return __uint_as_float(u); }
__device__ __forceinline__ unsigned cvtpk(float lo, float hi) { bf16x2 v = {(__bf16)lo, (__bf16)hi}; return __builtin_bit_cast(unsigned, v); }
__device__ __forceinline__ float dot2(unsigned a, unsigned b, float acc) {
  return __builtin_amdgcn_fdot2_f32_bf16(__builtin_bit_cast(bf16x2, a), __builtin_bit_cast(bf16x2, b), acc, false);
}
template <int CTRL> __device__ __forceinline__ float dpp(float x) {
  return __builtin_bit_cast(float, __builtin_amdgcn_mov_dpp(__builtin_bit_cast(int, x), CTRL, 0xf, 0xf, true));
}
constexpr int XOR1 = 0xB1, XOR2 = 0x4E, XOR7 = 0x141, XOR8 = 0x128;
__device__ __forceinline__ f32x4 mfma(u32x4 a, u32x4 b, f32x4 c) {
  return __builtin_amdgcn_mfma_f32_16x16x32_bf16(__builtin_bit_cast(bf16x8, a), __builtin_bit_cast(bf16x8, b), c, 0, 0, 0);
}
__device__ __forceinline__ float fold32(float a, float b) {
  auto r = __builtin_amdgcn_permlane32_swap(fu(a), fu(b), false, false);
  return uf(r[0]) + uf(r[1]);
}
__device__ __forceinline__ float fold16(float a, float b) {
  auto r = __builtin_amdgcn_permlane16_swap(fu(a), fu(b), false, false);
  return uf(r[0]) + uf(r[1]);
}
__device__ __forceinline__ float max16_32(float x) {
  auto s = __builtin_amdgcn_permlane16_swap(fu(x), fu(x), false, false);
  x = fmaxf(uf(s[0]), uf(s[1]));
  auto t = __builtin_amdgcn_permlane32_swap(fu(x), fu(x), false, false);
  return fmaxf(uf(t[0]), uf(t[1]));
}
__device__ __forceinline__ rsrc_t mkrs(u64 p, unsigned bytes) { return __builtin_amdgcn_make_buffer_rsrc((void*)p, 0, bytes, 0x00020000); }
template <int AUX> __device__ __forceinline__ u32x4 ldb(rsrc_t r, unsigned voff, unsigned soff) { return __builtin_amdgcn_raw_buffer_load_b128(r, voff, soff, AUX); }
__device__ __forceinline__ void publish(rsrc_t r, unsigned voff, unsigned soff, u32x4 v) { __builtin_amdgcn_raw_buffer_store_b128(v, r, voff, soff, SC1); }
__device__ __forceinline__ unsigned xor3(unsigned a, unsigned b, unsigned c) { return __builtin_amdgcn_bitop3_b32(a, b, c, 0x96); }
__device__ __forceinline__ u32x4 seal(unsigned a, unsigned b, unsigned c, unsigned tag) { return u32x4{a, b, c, xor3(a, b, c ^ tag)}; }
__device__ __forceinline__ unsigned bad(u32x4 g, unsigned tag) { return xor3(xor3(g[0], g[1], g[2]), g[3], tag); }
constexpr unsigned PK0 = 0x07060501u, PK1 = 0x07060502u, PK2 = 0x07060503u, UP0 = 0x0c04000cu, UP1 = 0x04020100u;
__device__ __forceinline__ float fourth(u32x4 g) { return uf(__builtin_amdgcn_perm(g[2], __builtin_amdgcn_perm(g[1], g[0], UP0), UP1)); }

template <int OFF> __device__ __forceinline__ void lds_inc(unsigned base) {
  asm volatile("s_mov_b64 exec, 1\n\tds_add_u32 %0, %1 offset:%2\n\ts_mov_b64 exec, -1" :: "v"(base), "v"(1u), "i"(OFF) : "memory");
}
template <int OFF> __device__ __forceinline__ unsigned lds_wait(unsigned base, unsigned n) {
  unsigned v, left = SPIN_CAP << 3;
  asm volatile("1:\n\tds_read_b32 %0, %2 offset:%4\n\ts_sub_u32 %1, %1, 1\n\ts_waitcnt lgkmcnt(0)\n\tv_cmp_gt_u32 vcc, %3, %0\n\t"
               "s_cbranch_vccz 2f\n\ts_sleep 1\n\ts_cbranch_scc0 1b\n2:" : "=&v"(v), "+s"(left) : "v"(base), "s"(n), "i"(OFF) : "memory", "vcc", "scc");
  return v;
}
#define POLL2(EARLY) \
    "1:\n\tbuffer_load_dwordx4 v[100:103], %[vo], %[rs], %[so] offen sc1\n\tbuffer_load_dwordx4 v[104:107], %[vo], %[rs], %[so] offen offset:1024 sc1\n\t" \
    "s_sub_u32 %[n], %[n], 1\n\ts_waitcnt vmcnt(1)\n\t" \
    "v_bitop3_b32 v108, v100, v101, v102 bitop3:0x96\n\tv_bitop3_b32 v108, v108, v103, %[tag] bitop3:0x96\n\t" EARLY "s_waitcnt vmcnt(0)\n\t" \
    "v_bitop3_b32 v109, v104, v105, v106 bitop3:0x96\n\tv_bitop3_b32 v109, v109, v107, %[tag] bitop3:0x96\n\t" \
    "v_or_b32 v108, v108, v109\n\tv_cmp_ne_u32 vcc, 0, v108\n\ts_cbranch_vccz 2f\n\ts_cbranch_scc0 1b\n2:\n\t"
#define POLLCLOB "memory", "vcc", "scc", "v100", "v101", "v102", "v103", "v104", "v105", "v106", "v107", "v108", "v109", "v110", "v111", "v112", "v113", "v114", "v115", "v116", "v117"

#define GETARGS(OFF) asm volatile("s_load_dwordx4 %0, %2, %3\n\ts_load_dwordx8 %1, %2, 0x40\n\ts_waitcnt lgkmcnt(0)" : "=&s"(a2), "=&s"(a4) : "s"(karg), "s"(OFF) : "memory")
__global__ void __launch_bounds__(NTHR, 3) fused_decode(const void* kA, const void* vA, const void* wqA, const void* xA, const void* kB, const void* vB, const void* wqB,
                                                         const void* xB, const void* woA, const void* woB, void* out, void* ws) {
  __shared__ __attribute__((aligned(16))) Lds L;
  const auto karg = __builtin_amdgcn_kernarg_segment_ptr();
  asm volatile("s_load_dword s94, %0, 0x0\n\ts_load_dword s95, %0, 0x40" :: "s"(karg) : "s94", "s95");
  SB();
  const int tid = threadIdx.x, lane = tid & 63, wave = __builtin_amdgcn_readfirstlane(tid >> 6);
  const int w = blockIdx.x, h = (w & 7) * 2 + (w >> 7), s = (w >> 3) & 15;
  const unsigned lb = (unsigned)(size_t)(LAS void*)&L;
  u64x2 a2; u64x4 a4;
  unsigned tag;

  if (__builtin_expect(wave == 0, 1)) {
    const int rs = lane >> 3, c = lane & 7;
    const int qbr = (w >> 1) & 1, half = (w >> 2) & 1, j = w >> 3, blk = (j >> 1) * 4 + (j & 1) * 2 + ((j ^ w) & 1);
    unsigned woff = (unsigned)((half * 288 + rs * 36) * NQ + blk * 64 + c * 8) * 2;
    asm volatile("" : "+v"(woff));
    GETARGS(qbr * 32 + 16);
    const rsrc_t wr = mkrs(a2[0], DM * NQ * 2), wsr = mkrs(a4[3], WS_END);
    u32x4 wv[36], xr;
    unsigned epoch, so = 0;
#pragma unroll
    for (int t = 0; t < 36; ++t) {
      wv[t] = ldb<NT>(wr, woff, so); SB();
      so += NQ * 2; asm volatile("" : "+s"(so)); SB();
      if (t == 0) asm volatile("s_load_dword s94, %0, 0x0" :: "s"(a2[1]) : "s94");
      if (t == 20) {
        xr = ldb<0>(mkrs(a2[1] + half * 576, 576), lane * 16, 0);
        epoch = __hip_atomic_load((GAS unsigned*)a4[3], __ATOMIC_RELAXED, __HIP_MEMORY_SCOPE_AGENT); SB();
      }
    }
    __builtin_amdgcn_s_barrier();
    SB();
    if (lane < 36) *(u32x4*)&L.x[lane * 4] = xr;
    float acc[8] = {0, 0, 0, 0, 0, 0, 0, 0};
#pragma unroll
    for (int t4 = 0; t4 < 9; ++t4) {
      const u32x2 xw = *(const u32x2*)&L.x[rs * 18 + t4 * 2];
#pragma unroll
      for (int u = 0; u < 4; ++u) {
        const unsigned xl = (u & 1) ? xw[u >> 1] >> 16 : xw[u >> 1] & 0xffffu, xh = (u & 1) ? xw[u >> 1] & 0xffff0000u : xw[u >> 1] << 16;
#pragma unroll
        for (int k = 0; k < 4; ++k) { acc[2 * k] = dot2(wv[t4 * 4 + u][k], xl, acc[2 * k]); acc[2 * k + 1] = dot2(wv[t4 * 4 + u][k], xh, acc[2 * k + 1]); }
      }
      SB();
    }
    float b[4];
#pragma unroll
    for (int k = 0; k < 4; ++k) { b[k] = fold32(acc[k], acc[k + 4]); b[k] = fold16(b[k], b[k]); b[k] += dpp<XOR8>(b[k]); }
    asm volatile("" : "+v"(epoch));
    tag = __builtin_amdgcn_readfirstlane(epoch) + 1u;
    tag += tag == 0u;
    publish(wsr, (lane & 24) ? OOR : (c * 2 + (lane >> 5)) * 16, WS_Q + (blk >> 2) * 4096 + (qbr * 2 + half) * 1024 + (blk & 3) * 256,
            seal(__builtin_amdgcn_perm(fu(b[0]), fu(b[3]), PK0), __builtin_amdgcn_perm(fu(b[1]), fu(b[3]), PK1), __builtin_amdgcn_perm(fu(b[2]), fu(b[3]), PK2), tag));
    unsigned opt = 0;
    if (OPT) opt = __builtin_amdgcn_raw_buffer_load_b32(mkrs(a4[2], 2 * DM * 2), lane == 0 && w < 18 ? w * 128 : OOR, 0, SC1);
    __builtin_amdgcn_s_sleep(64);
    u32x4 g[4];
    for (unsigned spins = 0; spins < SPIN_CAP; ++spins) {
      asm volatile("" ::: "memory");
#pragma unroll
      for (int i = 0; i < 4; ++i) g[i] = ldb<SC1>(wsr, lane * 16 + i * 1024, WS_Q + h * 4096);
      if (!__any((bad(g[0], tag) | bad(g[1], tag) | bad(g[2], tag) | bad(g[3], tag)) != 0u)) break;
    }
#pragma unroll
    for (int br = 0; br < 2; ++br) {
      const u32x4 p0 = g[2 * br], p1 = g[2 * br + 1];
      *(u32x2*)&L.q[br][2 * lane] = u32x2{cvtpk(uf(p0[0]) + uf(p1[0]), uf(p0[1]) + uf(p1[1])), cvtpk(uf(p0[2]) + uf(p1[2]), fourth(p0) + fourth(p1))};
    }
    asm volatile("ds_write_b32 %0, %1 offset:%2" :: "v"(lb), "v"(tag), "i"(offsetof(Lds, tag)) : "memory");
    asm volatile("" :: "v"(opt));
    asm volatile("s_branch 1f\n\t.p2align 12\n1:");
  } else {
    const unsigned tstart = (unsigned)__builtin_amdgcn_s_memrealtime();
    const int br = wave == 1 || wave == 3 ? 0 : 1;
    const int quarter = wave < 5 ? 3 : (wave - 5) >> 1, odd = wave < 5 ? (wave - 1) >> 1 : (wave - 5) & 1;
    const int g = lane >> 4, i = lane & 15;
    const unsigned sbase = (h * 2048 + quarter * 512 + s * 32 + odd * 2) * 1024;
    unsigned loff = sbase + lane * 16, so = ((s + 3 * h) & 7) * 4096, kneed = 2 * br;
    const bool late = wave < 4, closer = wave < 3;
    if (tid == 64) { *(u32x4*)&L.tag = u32x4{0u, 0u, 0u, 0u}; *(u32x2*)&L.done[0] = u32x2{0u, 0u}; }
    asm volatile("" : "+v"(loff));
    asm volatile("" : "+s"(so), "+s"(kneed));
    GETARGS(br * 32);
    if ((unsigned)(wave - 3) < 2u) asm volatile("s_load_dword s94, %0, %2\n\ts_load_dword s95, %1, %2" :: "s"(a2[0]), "s"(a2[1]), "s"(sbase) : "s94", "s95");
    const rsrc_t kr = mkrs(a2[0], 32 << 20), vr = mkrs(a2[1], 32 << 20), wsr = mkrs(a4[3], WS_END);
    asm volatile("" :: "s"(kr), "s"(vr));
    __builtin_amdgcn_s_barrier();
    SB();
    if (kneed) lds_wait<offsetof(Lds, kissued)>(lb, kneed);
    u32x4 K[16], V[16];
#define KL2(A, B) "buffer_load_dwordx4 " A ", %[vo], %[rs], %[so] offen nt\n\ts_add_u32 %[nx], %[so], %[st]\n\t" \
                  "buffer_load_dwordx4 " B ", %[vo], %[rs], %[so] offen offset:1024 nt\n\ts_and_b32 %[so], %[nx], %[mk]\n\t"
    unsigned nx;
    asm volatile(KL2("%0", "%1") KL2("%2", "%3") KL2("%4", "%5") KL2("%6", "%7") KL2("%8", "%9") KL2("%10", "%11") KL2("%12", "%13") KL2("%14", "%15")
                 "s_mov_b64 exec, 1\n\tds_add_u32 %[lb], %[one] offset:%[ko]\n\ts_mov_b64 exec, -1\n\ts_waitcnt vmcnt(0)"
                 : "=&v"(K[0]), "=&v"(K[1]), "=&v"(K[2]), "=&v"(K[3]), "=&v"(K[4]), "=&v"(K[5]), "=&v"(K[6]), "=&v"(K[7]), "=&v"(K[8]), "=&v"(K[9]), "=&v"(K[10]),
                   "=&v"(K[11]), "=&v"(K[12]), "=&v"(K[13]), "=&v"(K[14]), "=&v"(K[15]), [so] "+s"(so), [nx] "=&s"(nx)
                 : [vo] "v"(loff), [rs] "s"(kr), [st] "s"(0x1000), [mk] "s"(0x7000), [lb] "v"(lb), [one] "v"(1u), [ko] "i"(offsetof(Lds, kissued)) : "memory", "scc");
    if (wave > 7) __builtin_amdgcn_s_sleep(6);
    lds_inc<offsetof(Lds, landed)>(lb);
#define LOADV() _Pragma("unroll") for (int t = 0; t < 16; t += 2) { \
      V[t] = ldb<NT>(vr, loff, so); SB(); unsigned n2 = so + 4096u; asm volatile("" : "+s"(n2)); SB(); \
      V[t + 1] = ldb<NT>(vr, loff + 1024, so); SB(); so = n2 & 0x7000u; asm volatile("" : "+s"(so)); SB(); } \
      lds_inc<offsetof(Lds, issued)>(lb);
    if (!late) {
      lds_wait<offsetof(Lds, kissued)>(lb, 10u);
      for (unsigned spins = 0; (unsigned)__builtin_amdgcn_s_memrealtime() - tstart < VGATE && spins < SPIN_CAP; ++spins)
        if (__builtin_amdgcn_readfirstlane(lds_wait<offsetof(Lds, landed)>(lb, 0u)) >= 10u) break;
      SB();
      LOADV()
    }
    tag = __builtin_amdgcn_readfirstlane(lds_wait<offsetof(Lds, tag)>(lb, 1u));
    const u32x4 qa = *(const u32x4*)&L.q[br][(i & 8) * 8 + 4 * i], qb = *(const u32x4*)&L.q[br][64 - (i & 8) * 8 + 4 * i];
    float sc[16], m = -INFINITY;
#pragma unroll
    for (int t = 0; t < 16; ++t) {
      float d = 0, e = 0;
#pragma unroll
      for (int k = 0; k < 4; ++k) { d = dot2(K[t][k], qa[k], d); e = dot2(K[t][k], qb[k], e); }
      d += dpp<XOR8>(e); d += dpp<XOR7>(d); d += dpp<XOR2>(d); d += dpp<XOR1>(d);
      asm("" : "+v"(d));
      sc[t] = d;
    }
    constexpr float SCALE = 0.088388347648318440f, LOG2E = 1.4426950408889634f;
#pragma unroll
    for (int t = 0; t < 16; t += 2) {
      const unsigned r1 = cvtpk(sc[t], sc[t + 1]), r2 = cvtpk(bflo(r1) * SCALE, bfhi(r1) * SCALE);
      sc[t] = bflo(r2); sc[t + 1] = bfhi(r2);
      m = fmaxf(m, fmaxf(sc[t], sc[t + 1]));
    }
    m = max16_32(m) * LOG2E;
    unsigned msk[4];
#pragma unroll
    for (int k = 0; k < 4; ++k) msk[k] = (((i & 7) >> 1) == k) ? ((i & 1) ? 0xffff0000u : 0x0000ffffu) : 0u;
    float l = 0;
    u32x4 a[16];
#pragma unroll
    for (int t = 0; t < 16; ++t) {
      const float p = __builtin_amdgcn_exp2f(fmaf(sc[t], LOG2E, -m));
      const unsigned pp = cvtpk(p, p);
      l += p;
      a[t] = u32x4{pp & msk[0], pp & msk[1], pp & msk[2], pp & msk[3]};
    }
    l = fold16(l, l); l = fold32(l, l);
    if ((lane & 55) == 0) *(float2*)&L.part[wave][lane >> 3][0] = make_float2(m, l);
#pragma unroll
    for (int t = 0; t < 16; ++t) asm volatile("" : "+v"(a[t]));
    SB();
    if (late) {
      lds_wait<offsetof(Lds, issued)>(lb, wave == 1 ? 9u : wave + 5u);
      SB();
      LOADV()
    }
    const int hd = g >> 1, oi = 4 + 8 * i + 4 * (g & 1);
    float f = 0, eo = 0;
    unsigned lse = 0, poff = WS_O + h * 32768 + br * 16384 + s * 128 + (lane >> 3) * 2048 + (lane & 7) * 16;
    if (wave == 1) {
      const float2 me = *(const float2*)&L.part[1][hd][0], r = *(const float2*)&L.part[3][hd][0];
      const float M = fmaxf(me.x, r.x);
      f = __builtin_amdgcn_exp2f(me.x - M); eo = __builtin_amdgcn_exp2f(r.x - M);
      const float Ls = fmaf(f, me.y, eo * r.y), inv = __builtin_amdgcn_rcpf(Ls);
      f *= inv; eo *= inv;
      lse = fu(M + __builtin_amdgcn_logf(Ls));
    }
    asm volatile("" : "+v"(f), "+v"(eo), "+v"(lse), "+v"(poff));
    SB();
    f32x4 acc = {0, 0, 0, 0}, base = {0, 0, 0, 0};
#pragma unroll
    for (int t = 0; t < 10; ++t) acc = mfma(a[t], V[t], acc);
    asm volatile("" : "+v"(acc));
    SB();
    if (wave == 1) {
      lds_wait<offsetof(Lds, done)>(lb, 1u);
      base = *(const f32x4*)&L.part[3][hd][oi];
    } else if (wave == 2) {
      lds_wait<offsetof(Lds, done) + 4>(lb, 7u);
      const float2 me = *(const float2*)&L.part[2][hd][0];
      float2 r[7]; f32x4 o[7];
#pragma unroll
      for (int u = 0; u < 7; ++u) { r[u] = *(const float2*)&L.part[4 + u][hd][0]; o[u] = *(const f32x4*)&L.part[4 + u][hd][oi]; }
      float M = me.x;
#pragma unroll
      for (int u = 0; u < 7; ++u) M = fmaxf(M, r[u].x);
      f = __builtin_amdgcn_exp2f(me.x - M);
      float Ls = f * me.y;
#pragma unroll
      for (int u = 0; u < 7; ++u) { const float e = __builtin_amdgcn_exp2f(r[u].x - M); Ls = fmaf(e, r[u].y, Ls); base += e * o[u]; }
      eo = __builtin_amdgcn_rcpf(Ls);
      f *= eo;
      lse = fu(M + __builtin_amdgcn_logf(Ls));
    }
    SB();
#pragma unroll
    for (int t = 10; t < 15; ++t) acc = mfma(a[t], V[t], acc);
    base *= eo;
    asm volatile("" : "+v"(base) : "s"(wsr));
    acc = mfma(a[15], V[15], acc);
    if (closer) {
      acc = acc * f + base;
      publish(wsr, poff, 0, seal(cvtpk(acc[0], acc[1]), cvtpk(acc[2], acc[3]), lse, tag));
    } else {
      *(f32x4*)&L.part[wave][hd][oi] = acc;
      lds_inc<offsetof(Lds, done)>(lb + 4 * br);
    }
  }

  const rsrc_t wsr = mkrs(a4[3], WS_END);
  const int hp = (h + 4) & 15, pbr = s >> 3;
  if (wave == 3) {
    unsigned left = SPIN_CAP;
    __builtin_amdgcn_s_sleep(12);
    asm volatile(POLL2("")
        "v_max_f32 v112, v102, v106\n\tv_lshlrev_b32 v108, 16, v100\n\tv_and_b32 v109, 0xffff0000, v100\n\t"
        "v_max_f32_dpp v112, v112, v112 row_ror:8 row_mask:0xf bank_mask:0xf\n\tv_lshlrev_b32 v110, 16, v101\n\t"
        "v_mov_b32 v113, v112\n\tv_and_b32 v111, 0xffff0000, v101\n\tv_lshlrev_b32 v114, 16, v104\n\t"
        "v_permlane16_swap_b32 v112, v113\n\tv_max_f32 v112, v112, v113\n\t"
        "v_mov_b32 v113, v112\n\tv_and_b32 v115, 0xffff0000, v104\n\tv_lshlrev_b32 v116, 16, v105\n\t"
        "v_permlane32_swap_b32 v112, v113\n\tv_max_f32 v112, v112, v113\n\t"
        "v_sub_f32 v102, v102, v112\n\tv_sub_f32 v106, v106, v112\n\tv_exp_f32 v102, v102\n\tv_exp_f32 v106, v106\n\t"
        "v_and_b32 v117, 0xffff0000, v105\n\tv_add_f32 v112, v102, v106\n\t"
        "v_pk_mul_f32 v[108:109], v[108:109], v[102:103] op_sel_hi:[1,0]\n\tv_pk_mul_f32 v[110:111], v[110:111], v[102:103] op_sel_hi:[1,0]\n\t"
        "v_add_f32_dpp v112, v112, v112 row_ror:8 row_mask:0xf bank_mask:0xf\n\t"
        "v_pk_fma_f32 v[108:109], v[114:115], v[106:107], v[108:109] op_sel_hi:[1,0,1]\n\tv_mov_b32 v113, v112\n\t"
        "v_pk_fma_f32 v[110:111], v[116:117], v[106:107], v[110:111] op_sel_hi:[1,0,1]\n\ts_nop 0\n\t"
        "v_permlane16_swap_b32 v112, v113\n\tv_permlane32_swap_b32 v108, v110\n\tv_permlane32_swap_b32 v109, v111\n\t"
        "v_add_f32 v112, v112, v113\n\tv_add_f32 v108, v108, v110\n\tv_add_f32 v109, v109, v111\n\tv_mov_b32 v113, v112\n\ts_nop 0\n\t"
        "v_permlane16_swap_b32 v108, v109\n\tv_permlane32_swap_b32 v112, v113\n\t"
        "v_add_f32 v108, v108, v109\n\tv_add_f32 v112, v112, v113\n\tv_rcp_f32 v112, v112\n\t"
        "v_add_f32_dpp v108, v108, v108 row_ror:8 row_mask:0xf bank_mask:0xf\n\tv_mul_f32 v108, v108, v112\n\t"
        "v_cvt_pk_bf16_f32 v108, v108, v108\n\tds_write_b16 %[la], v108\n\ts_waitcnt lgkmcnt(0)\n\ts_barrier"
        : [n] "+s"(left) : [vo] "v"(lane * 16), [rs] "s"(wsr), [so] "s"(WS_O + (hp * 16 + s) * 2048), [tag] "s"(tag),
          [la] "v"(lb + (unsigned)offsetof(Lds, o) + 8 * (lane & 7) + 2 * (lane >> 4)) : POLLCLOB);
  } else if (wave == 2) {
    asm volatile("s_barrier" ::: "memory");
  } else {
    const int pw = wave < 2 ? wave : wave - 2;
    const int row0 = hp * 256 + ((s >> 2) & 1) * 128 + (s & 1) * 64 + (s & 2) * 2;
    const rsrc_t wr = mkrs((pbr ? a4[1] : a4[0]) + (row0 * DM + pw * 64) * 2, 60 * DM * 2);
    const unsigned woff = ((8 * (lane >> 5) + ((lane >> 3) & 3)) * DM + (lane & 7) * 8) * 2;
    LAS unsigned char* dst = (LAS unsigned char*)&L.wo[pw][0];
    lds_wait<offsetof(Lds, issued)>(lb, 10u);
#pragma unroll
    for (int t = 0; t < 4; ++t) __builtin_amdgcn_raw_ptr_buffer_load_lds(wr, dst + t * 1024, 16, woff, t * 16 * DM * 2, 0, NT);
    unsigned yoff = lane < 16 ? WS_Y + (pbr * 18 + pw * 2 + (lane >> 3)) * 16384 + (hp * 8 + (s & 7)) * 128 + (lane & 7) * 16 : OOR;
    unsigned oa = (lane >> 4) * 16, k0 = PK0, k1 = PK1, k2 = PK2;
    asm volatile("s_waitcnt vmcnt(0)" ::: "memory");
    LAS unsigned char* src = dst + (lane >> 4) * 1024 + ((lane >> 2) & 3) * 128 + (lane & 2) * 32 + (lane & 1) * 8;
    u32x4 wf[4];
#pragma unroll
    for (int b = 0; b < 4; ++b) {
      const u32x2 lo = __builtin_bit_cast(u32x2, __builtin_amdgcn_ds_read_tr16_b64_v4i16((LAS s16x4*)(src + b * 16)));
      const u32x2 hi = __builtin_bit_cast(u32x2, __builtin_amdgcn_ds_read_tr16_b64_v4i16((LAS s16x4*)(src + b * 16 + 512)));
      wf[b] = u32x4{lo[0], lo[1], hi[0], hi[1]};
    }
    asm volatile("" : "+s"(k0), "+s"(k1), "+s"(k2) : "s"(wsr));
    asm volatile("" : "+v"(wf[0]), "+v"(wf[1]), "+v"(wf[2]), "+v"(wf[3]), "+v"(yoff), "+v"(oa));
    asm volatile("s_barrier" ::: "memory");
    const u32x4 ov = *(const u32x4*)((const char*)L.o + oa);
    const f32x4 z = {0, 0, 0, 0};
    const f32x4 y3 = mfma(ov, wf[3], z), y0 = mfma(ov, wf[0], z), y1 = mfma(ov, wf[1], z), y2 = mfma(ov, wf[2], z);
    SB();
    const unsigned p0 = __builtin_amdgcn_perm(fu(y0[0]), fu(y3[0]), k0), p1 = __builtin_amdgcn_perm(fu(y1[0]), fu(y3[0]), k1);
    const unsigned c01 = xor3(p0, p1, tag), p2 = __builtin_amdgcn_perm(fu(y2[0]), fu(y3[0]), k2);
    publish(wsr, yoff, 0, u32x4{p0, p1, p2, c01 ^ p2});
    asm volatile("" :: "v"(y0), "v"(y1), "v"(y2), "v"(y3));
  }
  if (w >= NRED) return;

  if (wave < 8) {
    unsigned left = SPIN_CAP;
    __builtin_amdgcn_s_sleep(16);
    asm volatile(POLL2("v_perm_b32 v110, v101, v100, %[k0]\n\tv_perm_b32 v110, v102, v110, %[k1]\n\t")
        "v_perm_b32 v111, v105, v104, %[k0]\n\tv_pk_add_f32 v[100:101], v[100:101], v[104:105]\n\tv_add_f32 v102, v102, v106\n\t"
        "v_perm_b32 v111, v106, v111, %[k1]\n\tv_add_f32 v110, v110, v111\n\t"
        "v_permlane32_swap_b32 v100, v102\n\tv_add_f32 v100, v100, v102\n\t"
        "v_permlane32_swap_b32 v101, v110\n\tv_add_f32 v101, v101, v110\n\ts_nop 1\n\t"
        "v_permlane16_swap_b32 v100, v101\n\tv_add_f32 v100, v100, v101\n\ts_nop 1\n\t"
        "v_add_f32_dpp v100, v100, v100 row_ror:8 row_mask:0xf bank_mask:0xf\n\tds_write_b32 %[la], v100\n\ts_waitcnt lgkmcnt(0)\n\ts_barrier"
        : [n] "+s"(left) : [vo] "v"(lane * 16), [rs] "s"(wsr), [so] "s"(WS_Y + w * 16384 + wave * 2048), [tag] "s"(tag), [k0] "s"(UP0), [k1] "s"(UP1),
          [la] "v"(lb + (unsigned)offsetof(Lds, fin) + ((lane >> 4) * 4 + ((lane & 7) >> 1)) * 64 + wave * 8 + (lane & 1) * 4) : POLLCLOB);
  } else if (wave == 8) {
    unsigned fa = (lane & 15) * 64, ooff = (lane & (OST >= 2 ? 51 : 48)) ? OOR : w * 64 + lane * 4;
    const rsrc_t outr = mkrs(a4[2], 2 * DM * 2);
    asm volatile("" : "+v"(fa), "+v"(ooff) : "s"(outr));
    asm volatile("s_barrier" ::: "memory");
    const f32x4* f = (const f32x4*)((const char*)L.fin + fa);
    const f32x4 t = (f[0] + f[1]) + (f[2] + f[3]);
    const unsigned r = cvtpk(t[0] + t[2], t[1] + t[3]);
    if (OST < 2) __builtin_amdgcn_raw_buffer_store_b32(r, outr, ooff, 0, OST ? SC1 : 0);
    else __builtin_amdgcn_raw_buffer_store_b128(u32x4{r, fu(dpp<0x55>(uf(r))), fu(dpp<0xAA>(uf(r))), fu(dpp<0xFF>(uf(r)))}, outr, ooff, 0, OST == 2 ? SC1 : 0);
  } else {
    asm volatile("s_barrier" ::: "memory");
    if (!NOB && w == NRED - 1 && tid == 9 * 64) __hip_atomic_store((GAS unsigned*)a4[3], tag, __ATOMIC_RELAXED, __HIP_MEMORY_SCOPE_AGENT);
  }
}

extern "C" void kernel_launch(void* const* d_in, const int* in_sizes, int n_in, void* d_out, int out_size, void* d_ws, size_t ws_size, hipStream_t stream) {
  fused_decode<<<256, NTHR, 0, stream>>>(d_in[1], d_in[2], d_in[3], d_in[0], d_in[6], d_in[7], d_in[8], d_in[5], d_in[4], d_in[9], d_out, d_ws);
}
```

```cpp
#include <hip/hip_runtime.h>
#include <hip/hip_bf16.h>
#include <stdint.h>
#include <stddef.h>

#define GAS __attribute__((address_space(1)))
#define LAS __attribute__((address_space(3)))
#define SB() __builtin_amdgcn_sched_barrier(0)
typedef unsigned u32x4 __attribute__((ext_vector_type(4)));
typedef unsigned u32x2 __attribute__((ext_vector_type(2)));
typedef float f32x4 __attribute__((ext_vector_type(4)));
typedef short s16x4 __attribute__((ext_vector_type(4)));
typedef __bf16 bf16x2 __attribute__((ext_vector_type(2)));
typedef __bf16 bf16x8 __attribute__((ext_vector_type(8)));
typedef unsigned long long u64;
typedef u64 u64x2 __attribute__((ext_vector_type(2)));
typedef u64 u64x4 __attribute__((ext_vector_type(4)));
typedef __amdgpu_buffer_rsrc_t rsrc_t;

constexpr int DM = 576, NQ = 4096, NTHR = 11 * 64, NRED = 36;
constexpr unsigned WS_Q = 4096, WS_O = WS_Q + 16 * 4096, WS_Y = WS_O + 256 * 2048, WS_END = WS_Y + NRED * 16384;
constexpr unsigned SPIN_CAP = 1u << 17;
constexpr unsigned OOR = 0x40000000u;
constexpr int NT = 2, SC1 = 16;
constexpr unsigned VGATE = 675;

struct Lds {
  unsigned char wo[9][4096];
  float part[11][2][132];
  unsigned q[2][128];
  unsigned x[144];
  float fin[16][8][2];
  unsigned short o[32];
  unsigned tag, kissued, issued, landed, done[2], pad[2];
};

__device__ __forceinline__ float bflo(unsigned u) { return __builtin_bit_cast(float, u << 16); }
__device__ __forceinline__ float bfhi(unsigned u) { return __builtin_bit_cast(float, u & 0xffff0000u); }
__device__ __forceinline__ unsigned fu(float f) { return __float_as_uint(f); }
__device__ __forceinline__ float uf(unsigned u) { return __uint_as_float(u); }
__device__ __forceinline__ unsigned cvtpk(float lo, float hi) { bf16x2 v = {(__bf16)lo, (__bf16)hi}; return __builtin_bit_cast(unsigned, v); }
__device__ __forceinline__ float dot2(unsigned a, unsigned b, float acc) {
  return __builtin_amdgcn_fdot2_f32_bf16(__builtin_bit_cast(bf16x2, a), __builtin_bit_cast(bf16x2, b), acc, false);
}
template <int CTRL> __device__ __forceinline__ float dpp(float x) {
  return __builtin_bit_cast(float, __builtin_amdgcn_mov_dpp(__builtin_bit_cast(int, x), CTRL, 0xf, 0xf, true));
}
constexpr int XOR1 = 0xB1, XOR2 = 0x4E, XOR7 = 0x141, XOR8 = 0x128;
__device__ __forceinline__ f32x4 mfma(u32x4 a, u32x4 b, f32x4 c) {
  return __builtin_amdgcn_mfma_f32_16x16x32_bf16(__builtin_bit_cast(bf16x8, a), __builtin_bit_cast(bf16x8, b), c, 0, 0, 0);
}
__device__ __forceinline__ float fold32(float a, float b) {
  auto r = __builtin_amdgcn_permlane32_swap(fu(a), fu(b), false, false);
  return uf(r[0]) + uf(r[1]);
}
__device__ __forceinline__ float fold16(float a, float b) {
  auto r = __builtin_amdgcn_permlane16_swap(fu(a), fu(b), false, false);
  return uf(r[0]) + uf(r[1]);
}
__device__ __forceinline__ float max16_32(float x) {
  auto s = __builtin_amdgcn_permlane16_swap(fu(x), fu(x), false, false);
  x = fmaxf(uf(s[0]), uf(s[1]));
  auto t = __builtin_amdgcn_permlane32_swap(fu(x), fu(x), false, false);
  return fmaxf(uf(t[0]), uf(t[1]));
}
__device__ __forceinline__ rsrc_t mkrs(u64 p, unsigned bytes) { return __builtin_amdgcn_make_buffer_rsrc((void*)p, 0, bytes, 0x00020000); }
template <int AUX> __device__ __forceinline__ u32x4 ldb(rsrc_t r, unsigned voff, unsigned soff) { return __builtin_amdgcn_raw_buffer_load_b128(r, voff, soff, AUX); }
__device__ __forceinline__ void publish(rsrc_t r, unsigned voff, unsigned soff, u32x4 v) { __builtin_amdgcn_raw_buffer_store_b128(v, r, voff, soff, SC1); }
__device__ __forceinline__ unsigned xor3(unsigned a, unsigned b, unsigned c) { return __builtin_amdgcn_bitop3_b32(a, b, c, 0x96); }
__device__ __forceinline__ u32x4 seal(unsigned a, unsigned b, unsigned c, unsigned tag) { return u32x4{a, b, c, xor3(a, b, c ^ tag)}; }
__device__ __forceinline__ unsigned bad(u32x4 g, unsigned tag) { return xor3(xor3(g[0], g[1], g[2]), g[3], tag); }
constexpr unsigned PK0 = 0x07060501u, PK1 = 0x07060502u, PK2 = 0x07060503u, UP0 = 0x0c04000cu, UP1 = 0x04020100u;
__device__ __forceinline__ float fourth(u32x4 g) { return uf(__builtin_amdgcn_perm(g[2], __builtin_amdgcn_perm(g[1], g[0], UP0), UP1)); }

template <int OFF> __device__ __forceinline__ void lds_inc(unsigned base) {
  asm volatile("s_mov_b64 exec, 1\n\tds_add_u32 %0, %1 offset:%2\n\ts_mov_b64 exec, -1" :: "v"(base), "v"(1u), "i"(OFF) : "memory");
}
template <int OFF> __device__ __forceinline__ unsigned lds_wait(unsigned base, unsigned n) {
  unsigned v, left = SPIN_CAP << 3;
  asm volatile("1:\n\tds_read_b32 %0, %2 offset:%4\n\ts_sub_u32 %1, %1, 1\n\ts_waitcnt lgkmcnt(0)\n\tv_cmp_gt_u32 vcc, %3, %0\n\t"
               "s_cbranch_vccz 2f\n\ts_sleep 1\n\ts_cbranch_scc0 1b\n2:" : "=&v"(v), "+&s"(left) : "v"(base), "s"(n), "i"(OFF) : "memory", "vcc", "scc");
  return v;
}
#define POLL2(EARLY) \
    "1:\n\tbuffer_load_dwordx4 v[100:103], %[vo], %[rs], %[so] offen sc1\n\tbuffer_load_dwordx4 v[104:107], %[vo], %[rs], %[so] offen offset:1024 sc1\n\t" \
    "s_sub_u32 %[n], %[n], 1\n\ts_waitcnt vmcnt(1)\n\t" \
    "v_bitop3_b32 v108, v100, v101, v102 bitop3:0x96\n\tv_bitop3_b32 v108, v108, v103, %[tag] bitop3:0x96\n\t" EARLY "s_waitcnt vmcnt(0)\n\t" \
    "v_bitop3_b32 v109, v104, v105, v106 bitop3:0x96\n\tv_bitop3_b32 v109, v109, v107, %[tag] bitop3:0x96\n\t" \
    "v_or_b32 v108, v108, v109\n\tv_cmp_ne_u32 vcc, 0, v108\n\ts_cbranch_vccz 2f\n\ts_cbranch_scc0 1b\n2:\n\t"
#define POLLCLOB "memory", "vcc", "scc", "v100", "v101", "v102", "v103", "v104", "v105", "v106", "v107", "v108", "v109", "v110", "v111", "v112", "v113", "v114", "v115", "v116", "v117"

#define GETARGS(OFF) asm volatile("s_load_dwordx4 %0, %2, %3\n\ts_load_dwordx8 %1, %2, 0x40\n\ts_waitcnt lgkmcnt(0)" : "=&s"(a2), "=&s"(a4) : "s"(karg), "s"(OFF) : "memory")
__global__ void __launch_bounds__(NTHR, 3) fused_decode(const void* kA, const void* vA, const void* wqA, const void* xA, const void* kB, const void* vB, const void* wqB,
                                                         const void* xB, const void* woA, const void* woB, void* out, void* ws) {
  __shared__ __attribute__((aligned(16))) Lds L;
  const auto karg = __builtin_amdgcn_kernarg_segment_ptr();
  asm volatile("s_load_dword s94, %0, 0x0\n\ts_load_dword s95, %0, 0x40" :: "s"(karg) : "s94", "s95");
  SB();
  const int tid = threadIdx.x, lane = tid & 63, wave = __builtin_amdgcn_readfirstlane(tid >> 6);
  const int w = blockIdx.x, h = (w & 7) * 2 + (w >> 7), s = (w >> 3) & 15;
  const unsigned lb = (unsigned)(size_t)(LAS void*)&L;
  u64x2 a2; u64x4 a4;
  unsigned tag;

  if (__builtin_expect(wave == 0, 1)) {
    const int rs = lane >> 3, c = lane & 7;
    const int qbr = (w >> 1) & 1, half = (w >> 2) & 1, j = w >> 3, blk = (j >> 1) * 4 + (j & 1) * 2 + ((j ^ w) & 1);
    unsigned woff = (unsigned)((half * 288 + rs * 36) * NQ + blk * 64 + c * 8) * 2;
    asm volatile("" : "+v"(woff));
    GETARGS(qbr * 32 + 16);
    const rsrc_t wr = mkrs(a2[0], DM * NQ * 2), wsr = mkrs(a4[3], WS_END);
    u32x4 wv[36], xr;
    unsigned epoch, so = 0;
#pragma unroll
    for (int t = 0; t < 36; ++t) {
      wv[t] = ldb<NT>(wr, woff, so); SB();
      so += NQ * 2; asm volatile("" : "+s"(so)); SB();
      if (t == 0) asm volatile("s_load_dword s94, %0, 0x0" :: "s"(a2[1]) : "s94");
      if (t == 20) {
        xr = ldb<0>(mkrs(a2[1] + half * 576, 576), lane * 16, 0);
        epoch = __hip_atomic_load((GAS unsigned*)a4[3], __ATOMIC_RELAXED, __HIP_MEMORY_SCOPE_AGENT); SB();
      }
    }
    __builtin_amdgcn_s_barrier();
    SB();
    if (lane < 36) *(u32x4*)&L.x[lane * 4] = xr;
    asm volatile("" ::: "memory");
    float acc[8] = {0, 0, 0, 0, 0, 0, 0, 0};
#pragma unroll
    for (int t4 = 0; t4 < 9; ++t4) {
      const u32x2 xw = *(const u32x2*)&L.x[rs * 18 + t4 * 2];
#pragma unroll
      for (int u = 0; u < 4; ++u) {
        const unsigned xl = (u & 1) ? xw[u >> 1] >> 16 : xw[u >> 1] & 0xffffu, xh = (u & 1) ? xw[u >> 1] & 0xffff0000u : xw[u >> 1] << 16;
#pragma unroll
        for (int k = 0; k < 4; ++k) { acc[2 * k] = dot2(wv[t4 * 4 + u][k], xl, acc[2 * k]); acc[2 * k + 1] = dot2(wv[t4 * 4 + u][k], xh, acc[2 * k + 1]); }
      }
      SB();
    }
    float b[4];
#pragma unroll
    for (int k = 0; k < 4; ++k) { b[k] = fold32(acc[k], acc[k + 4]); b[k] = fold16(b[k], b[k]); b[k] += dpp<XOR8>(b[k]); }
    asm volatile("" : "+v"(epoch));
    tag = __builtin_amdgcn_readfirstlane(epoch) + 1u;
    tag += tag == 0u;
    publish(wsr, (lane & 24) ? OOR : (c * 2 + (lane >> 5)) * 16, WS_Q + (blk >> 2) * 4096 + (qbr * 2 + half) * 1024 + (blk & 3) * 256,
            seal(__builtin_amdgcn_perm(fu(b[0]), fu(b[3]), PK0), __builtin_amdgcn_perm(fu(b[1]), fu(b[3]), PK1), __builtin_amdgcn_perm(fu(b[2]), fu(b[3]), PK2), tag));
    __builtin_amdgcn_s_sleep(64);
    u32x4 g[4];
    for (unsigned spins = 0; spins < SPIN_CAP; ++spins) {
      asm volatile("" ::: "memory");
#pragma unroll
      for (int i = 0; i < 4; ++i) g[i] = ldb<SC1>(wsr, lane * 16 + i * 1024, WS_Q + h * 4096);
      if (!__any((bad(g[0], tag) | bad(g[1], tag) | bad(g[2], tag) | bad(g[3], tag)) != 0u)) break;
    }
#pragma unroll
    for (int br = 0; br < 2; ++br) {
      const u32x4 p0 = g[2 * br], p1 = g[2 * br + 1];
      *(u32x2*)&L.q[br][2 * lane] = u32x2{cvtpk(uf(p0[0]) + uf(p1[0]), uf(p0[1]) + uf(p1[1])), cvtpk(uf(p0[2]) + uf(p1[2]), fourth(p0) + fourth(p1))};
    }
    asm volatile("ds_write_b32 %0, %1 offset:%2" :: "v"(lb), "v"(tag), "i"(offsetof(Lds, tag)) : "memory");
    asm volatile("s_branch 1f\n\t.p2align 12\n1:");
  } else {
    const unsigned tstart = (unsigned)__builtin_amdgcn_s_memrealtime();
    const int br = wave == 1 || wave == 3 ? 0 : 1;
    const int quarter = wave < 5 ? 3 : (wave - 5) >> 1, odd = wave < 5 ? (wave - 1) >> 1 : (wave - 5) & 1;
    const int g = lane >> 4, i = lane & 15;
    const unsigned sbase = (h * 2048 + quarter * 512 + s * 32 + odd * 2) * 1024;
    unsigned loff = sbase + lane * 16, so = ((s + 3 * h) & 7) * 4096, kneed = 2 * br;
    const bool late = wave < 4, closer = wave < 3;
    if (tid == 64) { *(u32x4*)&L.tag = u32x4{0u, 0u, 0u, 0u}; *(u32x2*)&L.done[0] = u32x2{0u, 0u}; }
    asm volatile("" : "+v"(loff));
    asm volatile("" : "+s"(so), "+s"(kneed));
    GETARGS(br * 32);
    if ((unsigned)(wave - 3) < 2u) asm volatile("s_load_dword s94, %0, %2\n\ts_load_dword s95, %1, %2" :: "s"(a2[0]), "s"(a2[1]), "s"(sbase) : "s94", "s95");
    const rsrc_t kr = mkrs(a2[0], 32 << 20), vr = mkrs(a2[1], 32 << 20), wsr = mkrs(a4[3], WS_END);
    asm volatile("" :: "s"(kr), "s"(vr));
    __builtin_amdgcn_s_barrier();
    SB();
    if (kneed) lds_wait<offsetof(Lds, kissued)>(lb, kneed);
    u32x4 K[16], V[16];
#define KL2(A, B) "buffer_load_dwordx4 " A ", %[vo], %[rs], %[so] offen nt\n\ts_add_u32 %[nx], %[so], %[st]\n\t" \
                  "buffer_load_dwordx4 " B ", %[vo], %[rs], %[so] offen offset:1024 nt\n\ts_and_b32 %[so], %[nx], %[mk]\n\t"
    unsigned nx;
    asm volatile(KL2("%0", "%1") KL2("%2", "%3") KL2("%4", "%5") KL2("%6", "%7") KL2("%8", "%9") KL2("%10", "%11") KL2("%12", "%13") KL2("%14", "%15")
                 "s_mov_b64 exec, 1\n\tds_add_u32 %[lb], %[one] offset:%[ko]\n\ts_mov_b64 exec, -1\n\ts_waitcnt vmcnt(0)"
                 : "=&v"(K[0]), "=&v"(K[1]), "=&v"(K[2]), "=&v"(K[3]), "=&v"(K[4]), "=&v"(K[5]), "=&v"(K[6]), "=&v"(K[7]), "=&v"(K[8]), "=&v"(K[9]), "=&v"(K[10]),
                   "=&v"(K[11]), "=&v"(K[12]), "=&v"(K[13]), "=&v"(K[14]), "=&v"(K[15]), [so] "+&s"(so), [nx] "=&s"(nx)
                 : [vo] "v"(loff), [rs] "s"(kr), [st] "s"(0x1000), [mk] "s"(0x7000), [lb] "v"(lb), [one] "v"(1u), [ko] "i"(offsetof(Lds, kissued)) : "memory", "scc");
    if (wave > 7) __builtin_amdgcn_s_sleep(6);
    lds_inc<offsetof(Lds, landed)>(lb);
#define LOADV() _Pragma("unroll") for (int t = 0; t < 16; t += 2) { \
      V[t] = ldb<NT>(vr, loff, so); SB(); unsigned n2 = so + 4096u; asm volatile("" : "+s"(n2)); SB(); \
      V[t + 1] = ldb<NT>(vr, loff + 1024, so); SB(); so = n2 & 0x7000u; asm volatile("" : "+s"(so)); SB(); } \
      lds_inc<offsetof(Lds, issued)>(lb);
    if (!late) {
      lds_wait<offsetof(Lds, kissued)>(lb, 10u);
      for (unsigned spins = 0; (unsigned)__builtin_amdgcn_s_memrealtime() - tstart < VGATE && spins < SPIN_CAP; ++spins)
        if (__builtin_amdgcn_readfirstlane(lds_wait<offsetof(Lds, landed)>(lb, 0u)) >= 10u) break;
      SB();
      LOADV()
    }
    tag = __builtin_amdgcn_readfirstlane(lds_wait<offsetof(Lds, tag)>(lb, 1u));
    const u32x4 qa = *(const u32x4*)&L.q[br][(i & 8) * 8 + 4 * i], qb = *(const u32x4*)&L.q[br][64 - (i & 8) * 8 + 4 * i];
    float sc[16], m = -INFINITY;
#pragma unroll
    for (int t = 0; t < 16; ++t) {
      float d = 0, e = 0;
#pragma unroll
      for (int k = 0; k < 4; ++k) { d = dot2(K[t][k], qa[k], d); e = dot2(K[t][k], qb[k], e); }
      d += dpp<XOR8>(e); d += dpp<XOR7>(d); d += dpp<XOR2>(d); d += dpp<XOR1>(d);
      asm("" : "+v"(d));
      sc[t] = d;
    }
    constexpr float SCALE = 0.088388347648318440f, LOG2E = 1.4426950408889634f;
#pragma unroll
    for (int t = 0; t < 16; t += 2) {
      const unsigned r1 = cvtpk(sc[t], sc[t + 1]), r2 = cvtpk(bflo(r1) * SCALE, bfhi(r1) * SCALE);
      sc[t] = bflo(r2); sc[t + 1] = bfhi(r2);
      m = fmaxf(m, fmaxf(sc[t], sc[t + 1]));
    }
    m = max16_32(m) * LOG2E;
    unsigned msk[4];
#pragma unroll
    for (int k = 0; k < 4; ++k) msk[k] = (((i & 7) >> 1) == k) ? ((i & 1) ? 0xffff0000u : 0x0000ffffu) : 0u;
    float l = 0;
    u32x4 a[16];
#pragma unroll
    for (int t = 0; t < 16; ++t) {
      const float p = __builtin_amdgcn_exp2f(fmaf(sc[t], LOG2E, -m));
      const unsigned pp = cvtpk(p, p);
      l += p;
      a[t] = u32x4{pp & msk[0], pp & msk[1], pp & msk[2], pp & msk[3]};
    }
    l = fold16(l, l); l = fold32(l, l);
    if ((lane & 55) == 0) *(float2*)&L.part[wave][lane >> 3][0] = make_float2(m, l);
#pragma unroll
    for (int t = 0; t < 16; ++t) asm volatile("" : "+v"(a[t]));
    SB();
    if (late) {
      lds_wait<offsetof(Lds, issued)>(lb, wave == 1 ? 9u : wave + 5u);
      SB();
      LOADV()
    }
    const int hd = g >> 1, oi = 4 + 8 * i + 4 * (g & 1);
    float f = 0, eo = 0;
    unsigned lse = 0, poff = WS_O + h * 32768 + br * 16384 + s * 128 + (lane >> 3) * 2048 + (lane & 7) * 16;
    if (wave == 1) {
      const float2 me = *(const float2*)&L.part[1][hd][0], r = *(const float2*)&L.part[3][hd][0];
      const float M = fmaxf(me.x, r.x);
      f = __builtin_amdgcn_exp2f(me.x - M); eo = __builtin_amdgcn_exp2f(r.x - M);
      const float Ls = fmaf(f, me.y, eo * r.y), inv = __builtin_amdgcn_rcpf(Ls);
      f *= inv; eo *= inv;
      lse = fu(M + __builtin_amdgcn_logf(Ls));
    }
    asm volatile("" : "+v"(f), "+v"(eo), "+v"(lse), "+v"(poff));
    SB();
    f32x4 acc = {0, 0, 0, 0}, base = {0, 0, 0, 0};
#pragma unroll
    for (int t = 0; t < 10; ++t) acc = mfma(a[t], V[t], acc);
    asm volatile("" : "+v"(acc));
    SB();
    if (wave == 1) {
      lds_wait<offsetof(Lds, done)>(lb, 1u);
      base = *(const f32x4*)&L.part[3][hd][oi];
    } else if (wave == 2) {
      lds_wait<offsetof(Lds, done) + 4>(lb, 7u);
      const float2 me = *(const float2*)&L.part[2][hd][0];
      float2 r[7]; f32x4 o[7];
#pragma unroll
      for (int u = 0; u < 7; ++u) { r[u] = *(const float2*)&L.part[4 + u][hd][0]; o[u] = *(const f32x4*)&L.part[4 + u][hd][oi]; }
      float M = me.x;
#pragma unroll
      for (int u = 0; u < 7; ++u) M = fmaxf(M, r[u].x);
      f = __builtin_amdgcn_exp2f(me.x - M);
      float Ls = f * me.y;
#pragma unroll
      for (int u = 0; u < 7; ++u) { const float e = __builtin_amdgcn_exp2f(r[u].x - M); Ls = fmaf(e, r[u].y, Ls); base += e * o[u]; }
      eo = __builtin_amdgcn_rcpf(Ls);
      f *= eo;
      lse = fu(M + __builtin_amdgcn_logf(Ls));
    }
    SB();
#pragma unroll
    for (int t = 10; t < 15; ++t) acc = mfma(a[t], V[t], acc);
    base *= eo;
    asm volatile("" : "+v"(base) : "s"(wsr));
    acc = mfma(a[15], V[15], acc);
    if (closer) {
      acc = acc * f + base;
      publish(wsr, poff, 0, seal(cvtpk(acc[0], acc[1]), cvtpk(acc[2], acc[3]), lse, tag));
    } else {
      *(f32x4*)&L.part[wave][hd][oi] = acc;
      lds_inc<offsetof(Lds, done)>(lb + 4 * br);
    }
  }

  const rsrc_t wsr = mkrs(a4[3], WS_END);
  const int hp = (h + 4) & 15, pbr = s >> 3;
  if (wave == 3) {
    unsigned left = SPIN_CAP;
    __builtin_amdgcn_s_sleep(12);
    asm volatile(POLL2("")
        "v_max_f32 v112, v102, v106\n\tv_lshlrev_b32 v108, 16, v100\n\tv_and_b32 v109, 0xffff0000, v100\n\t"
        "v_max_f32_dpp v112, v112, v112 row_ror:8 row_mask:0xf bank_mask:0xf\n\tv_lshlrev_b32 v110, 16, v101\n\t"
        "v_mov_b32 v113, v112\n\tv_and_b32 v111, 0xffff0000, v101\n\tv_lshlrev_b32 v114, 16, v104\n\t"
        "v_permlane16_swap_b32 v112, v113\n\tv_max_f32 v112, v112, v113\n\t"
        "v_mov_b32 v113, v112\n\tv_and_b32 v115, 0xffff0000, v104\n\tv_lshlrev_b32 v116, 16, v105\n\t"
        "v_permlane32_swap_b32 v112, v113\n\tv_max_f32 v112, v112, v113\n\t"
        "v_sub_f32 v102, v102, v112\n\tv_sub_f32 v106, v106, v112\n\tv_exp_f32 v102, v102\n\tv_exp_f32 v106, v106\n\t"
        "v_and_b32 v117, 0xffff0000, v105\n\tv_add_f32 v112, v102, v106\n\t"
        "v_pk_mul_f32 v[108:109], v[108:109], v[102:103] op_sel_hi:[1,0]\n\tv_pk_mul_f32 v[110:111], v[110:111], v[102:103] op_sel_hi:[1,0]\n\t"
        "v_add_f32_dpp v112, v112, v112 row_ror:8 row_mask:0xf bank_mask:0xf\n\t"
        "v_pk_fma_f32 v[108:109], v[114:115], v[106:107], v[108:109] op_sel_hi:[1,0,1]\n\tv_mov_b32 v113, v112\n\t"
        "v_pk_fma_f32 v[110:111], v[116:117], v[106:107], v[110:111] op_sel_hi:[1,0,1]\n\ts_nop 0\n\t"
        "v_permlane16_swap_b32 v112, v113\n\tv_permlane32_swap_b32 v108, v110\n\tv_permlane32_swap_b32 v109, v111\n\t"
        "v_add_f32 v112, v112, v113\n\tv_add_f32 v108, v108, v110\n\tv_add_f32 v109, v109, v111\n\tv_mov_b32 v113, v112\n\ts_nop 0\n\t"
        "v_permlane16_swap_b32 v108, v109\n\tv_permlane32_swap_b32 v112, v113\n\t"
        "v_add_f32 v108, v108, v109\n\tv_add_f32 v112, v112, v113\n\tv_rcp_f32 v112, v112\n\t"
        "v_add_f32_dpp v108, v108, v108 row_ror:8 row_mask:0xf bank_mask:0xf\n\tv_mul_f32 v108, v108, v112\n\t"
        "v_cvt_pk_bf16_f32 v108, v108, v108\n\tds_write_b16 %[la], v108\n\ts_waitcnt lgkmcnt(0)\n\ts_barrier"
        : [n] "+&s"(left) : [vo] "v"(lane * 16), [rs] "s"(wsr), [so] "s"(WS_O + (hp * 16 + s) * 2048), [tag] "s"(tag),
          [la] "v"(lb + (unsigned)offsetof(Lds, o) + 8 * (lane & 7) + 2 * (lane >> 4)) : POLLCLOB);
  } else if (wave == 2) {
    asm volatile("s_barrier" ::: "memory");
  } else {
    const int pw = wave < 2 ? wave : wave - 2;
    const int row0 = hp * 256 + ((s >> 2) & 1) * 128 + (s & 1) * 64 + (s & 2) * 2;
    const rsrc_t wr = mkrs((pbr ? a4[1] : a4[0]) + (row0 * DM + pw * 64) * 2, 60 * DM * 2);
    const unsigned woff = ((8 * (lane >> 5) + ((lane >> 3) & 3)) * DM + (lane & 7) * 8) * 2;
    LAS unsigned char* dst = (LAS unsigned char*)&L.wo[pw][0];
    lds_wait<offsetof(Lds, issued)>(lb, 10u);
#pragma unroll
    for (int t = 0; t < 4; ++t) __builtin_amdgcn_raw_ptr_buffer_load_lds(wr, dst + t * 1024, 16, woff, t * 16 * DM * 2, 0, NT);
    unsigned yoff = lane < 16 ? WS_Y + (pbr * 18 + pw * 2 + (lane >> 3)) * 16384 + (hp * 8 + (s & 7)) * 128 + (lane & 7) * 16 : OOR;
    unsigned oa = (lane >> 4) * 16, k0 = PK0, k1 = PK1, k2 = PK2;
    asm volatile("s_waitcnt vmcnt(0)" ::: "memory");
    LAS unsigned char* src = dst + (lane >> 4) * 1024 + ((lane >> 2) & 3) * 128 + (lane & 2) * 32 + (lane & 1) * 8;
    u32x4 wf[4];
#pragma unroll
    for (int b = 0; b < 4; ++b) {
      const u32x2 lo = __builtin_bit_cast(u32x2, __builtin_amdgcn_ds_read_tr16_b64_v4i16((LAS s16x4*)(src + b * 16)));
      const u32x2 hi = __builtin_bit_cast(u32x2, __builtin_amdgcn_ds_read_tr16_b64_v4i16((LAS s16x4*)(src + b * 16 + 512)));
      wf[b] = u32x4{lo[0], lo[1], hi[0], hi[1]};
    }
    asm volatile("" : "+s"(k0), "+s"(k1), "+s"(k2) : "s"(wsr));
    asm volatile("" : "+v"(wf[0]), "+v"(wf[1]), "+v"(wf[2]), "+v"(wf[3]), "+v"(yoff), "+v"(oa));
    asm volatile("s_barrier" ::: "memory");
    const u32x4 ov = *(const u32x4*)((const char*)L.o + oa);
    const f32x4 z = {0, 0, 0, 0};
    const f32x4 y3 = mfma(ov, wf[3], z), y0 = mfma(ov, wf[0], z), y1 = mfma(ov, wf[1], z), y2 = mfma(ov, wf[2], z);
    SB();
    const unsigned p0 = __builtin_amdgcn_perm(fu(y0[0]), fu(y3[0]), k0), p1 = __builtin_amdgcn_perm(fu(y1[0]), fu(y3[0]), k1);
    const unsigned c01 = xor3(p0, p1, tag), p2 = __builtin_amdgcn_perm(fu(y2[0]), fu(y3[0]), k2);
    publish(wsr, yoff, 0, u32x4{p0, p1, p2, c01 ^ p2});
    asm volatile("" :: "v"(y0), "v"(y1), "v"(y2), "v"(y3));
  }
  if (w >= NRED) return;

  if (wave < 8) {
    unsigned left = SPIN_CAP;
    __builtin_amdgcn_s_sleep(16);
    asm volatile(POLL2("v_perm_b32 v110, v101, v100, %[k0]\n\tv_perm_b32 v110, v102, v110, %[k1]\n\t")
        "v_perm_b32 v111, v105, v104, %[k0]\n\tv_pk_add_f32 v[100:101], v[100:101], v[104:105]\n\tv_add_f32 v102, v102, v106\n\t"
        "v_perm_b32 v111, v106, v111, %[k1]\n\tv_add_f32 v110, v110, v111\n\t"
        "v_permlane32_swap_b32 v100, v102\n\tv_add_f32 v100, v100, v102\n\t"
        "v_permlane32_swap_b32 v101, v110\n\tv_add_f32 v101, v101, v110\n\ts_nop 1\n\t"
        "v_permlane16_swap_b32 v100, v101\n\tv_add_f32 v100, v100, v101\n\ts_nop 1\n\t"
        "v_add_f32_dpp v100, v100, v100 row_ror:8 row_mask:0xf bank_mask:0xf\n\tds_write_b32 %[la], v100\n\ts_waitcnt lgkmcnt(0)\n\ts_barrier"
        : [n] "+&s"(left) : [vo] "v"(lane * 16), [rs] "s"(wsr), [so] "s"(WS_Y + w * 16384 + wave * 2048), [tag] "s"(tag), [k0] "s"(UP0), [k1] "s"(UP1),
          [la] "v"(lb + (unsigned)offsetof(Lds, fin) + ((lane >> 4) * 4 + ((lane & 7) >> 1)) * 64 + wave * 8 + (lane & 1) * 4) : POLLCLOB);
  } else if (wave == 8) {
    unsigned fa = (lane & 15) * 64, ooff = lane < 16 ? w * 64 + lane * 4 : OOR;
    const rsrc_t outr = mkrs(a4[2], 2 * DM * 2);
    asm volatile("" : "+v"(fa), "+v"(ooff) : "s"(outr));
    asm volatile("s_barrier" ::: "memory");
    const f32x4* f = (const f32x4*)((const char*)L.fin + fa);
    const f32x4 t = (f[0] + f[1]) + (f[2] + f[3]);
    __builtin_amdgcn_raw_buffer_store_b32(cvtpk(t[0] + t[2], t[1] + t[3]), outr, ooff, 0, SC1);
  } else {
    asm volatile("s_barrier" ::: "memory");
    if (w == NRED - 1 && tid == 9 * 64) __hip_atomic_store((GAS unsigned*)a4[3], tag, __ATOMIC_RELAXED, __HIP_MEMORY_SCOPE_AGENT);
  }
}

extern "C" void kernel_launch(void* const* d_in, const int* in_sizes, int n_in, void* d_out, int out_size, void* d_ws, size_t ws_size, hipStream_t stream) {
  fused_decode<<<256, NTHR, 0, stream>>>(d_in[1], d_in[2], d_in[3], d_in[0], d_in[6], d_in[7], d_in[8], d_in[5], d_in[4], d_in[9], d_out, d_ws);
}
```
